```python
import torch

def _softmax(x, axis=-1):
    return torch.softmax(x, dim=axis)
def _gelu(x):
    return torch.nn.functional.gelu(x, approximate="tanh")
def _silu(x):
    return torch.nn.functional.silu(x)
def _sigmoid(x):
    return torch.sigmoid(x)
def _layernorm(x, axis=-1, eps=1e-5):
    m = x.mean(dim=axis, keepdim=True)
    v = x.var(dim=axis, keepdim=True, unbiased=False)
    return (x - m) / torch.sqrt(v + eps)
def _scatter_add(tgt, idx, src, axis=0):
    return tgt.clone().index_add_(axis, idx, src)
def _segment_sum(src, seg, n):
    out = torch.zeros((n,) + src.shape[1:], dtype=src.dtype)
    return out.index_add_(0, seg, src)
def _pad(x, pw):
    flat = tuple(v for pair in reversed(pw) for v in pair)
    return torch.nn.functional.pad(x, flat)

kernel_name = 'gated_mix__ssm_conv_h3__swiglu_ffn__prompt'

def setup_inputs():

    _w = (lambda a: (lambda t: t.to(torch.bfloat16) if t.is_floating_point() else t)(torch.as_tensor(a)))
    def _A_setup():
        B, S, D = 1, 2048, 1024
        ks = 4
        return {
            "q": _w((lambda *s: torch.randn(*s, dtype=torch.bfloat16) * 0.05)(B, S, D)),
            "k": _w((lambda *s: torch.randn(*s, dtype=torch.bfloat16) * 0.4)(B, S, D)),
            "v": _w((lambda *s: torch.randn(*s, dtype=torch.bfloat16) * 0.4)(B, S, D)),
            "shift_k": _w((lambda *s: torch.randn(*s, dtype=torch.bfloat16) * 0.4)(D, ks) * 0.5),
            "ssm_k": _w((lambda *s: torch.randn(*s, dtype=torch.bfloat16) * 0.4)(D, S) * (1.0 / S ** 0.5)),
            "ssm_D": _w((lambda *s: torch.randn(*s, dtype=torch.bfloat16) * 0.4)(D)),
        }
    _A_in = _A_setup()

    def _B_setup():
        B, S, D, F = 1, 2048, 1024, 3584
        return {
            "x": _w((lambda *s: torch.randn(*s, dtype=torch.bfloat16) * 0.05)(B, S, D)),
            "w_gate": _w((lambda *s: torch.randn(*s, dtype=torch.bfloat16) * 0.05)(D, F)),
            "w_up": _w((lambda *s: torch.randn(*s, dtype=torch.bfloat16) * 0.05)(D, F)),
            "w_down": _w((lambda *s: torch.randn(*s, dtype=torch.bfloat16) * 0.05)(F, D)),
        }
    _B_in = _B_setup()

    return {
        "A_q": _A_in["q"],
        "A_k": _A_in["k"],
        "A_v": _A_in["v"],
        "A_shift_k": _A_in["shift_k"],
        "A_ssm_k": _A_in["ssm_k"],
        "A_ssm_D": _A_in["ssm_D"],
        "B_x": _B_in["x"],
        "B_w_gate": _B_in["w_gate"],
        "B_w_up": _B_in["w_up"],
        "B_w_down": _B_in["w_down"],
        "gate_w": _w((lambda *s: torch.randn(*s, dtype=torch.bfloat16) * 0.05)(1024) * 16.0),
    }

def reference(A_q, A_k, A_v, A_shift_k, A_ssm_k, A_ssm_D, B_x, B_w_gate, B_w_up, B_w_down, gate_w):
    def _A_ref(q, k, v, shift_k, ssm_k, ssm_D):
        B, S, D = q.shape
        ks = shift_k.shape[-1]
        k_pad = _pad(k, ((0, 0), (ks - 1, 0), (0, 0)))
        k_shift = torch.zeros_like(k)
        for t in range(ks):
            k_shift = k_shift + k_pad[:, t:t + S, :] * shift_k[:, ks - 1 - t][None, None, :]
        kv = k_shift * v
        L = 2 * S
        kv_pad = _pad(kv, ((0, 0), (0, S), (0, 0)))
        kf_pad = _pad(ssm_k, ((0, 0), (0, S)))
        kv_f = (lambda x, axes=(-2,-1): torch.fft.rfft2(x.float(), dim=axes))(kv_pad[:, None, :, :], axes=(1, 2))
        kf_f = (lambda x, axes=(-2,-1): torch.fft.rfft2(x.float(), dim=axes))(kf_pad[:, None, :], axes=(1, 2))
        kf_f = (lambda x, *d: x.permute(*d))(kf_f, 1, 2, 0)
        y_f = kv_f * kf_f[None, :, :, :]
        y = (lambda x, s, axes=(-2,-1): torch.fft.irfft2(x, s=s, dim=axes).to(torch.bfloat16))(y_f, (1, L), axes=(1, 2))[:, 0, :S, :]
        y = y + kv * ssm_D[None, None, :]
        return q * y

    def _B_ref(x, w_gate, w_up, w_down):
        g = _silu(x @ w_gate)
        u = x @ w_up
        return (g * u) @ w_down

    x = A_q
    g = _sigmoid((x * gate_w).sum(-1, keepdims=True))
    a = _A_ref(x, A_k, A_v, A_shift_k, A_ssm_k, A_ssm_D)
    b = _B_ref(x, B_w_gate, B_w_up, B_w_down)
    return g * a + (1 - g) * b
```

```cpp
#include <hip/hip_runtime.h>
#include <hip/hip_bf16.h>
#include <stdint.h>
typedef unsigned short u16;
typedef short bf16x8 __attribute__((ext_vector_type(8)));
typedef float f32x4 __attribute__((ext_vector_type(4)));
constexpr int S=2048, D=1024, F=3584;
__device__ inline float b2f(u16 x){ return __uint_as_float(((unsigned)x)<<16); }
__device__ inline u16 f2b(float f){ unsigned u=__float_as_uint(f); u += 0x7FFF + ((u>>16)&1); return (u16)(u>>16); }

__global__ void k_prep(const u16* k, const u16* v, const u16* sk, u16* kvT){
  int d = blockIdx.x*64 + (threadIdx.x&63); int t = blockIdx.y*4 + (threadIdx.x>>6);
  float acc=0;
  for(int j=0;j<4;j++){ int tt=t-j; float kk = tt>=0? b2f(k[tt*D+d]):0.f; acc += kk*b2f(sk[d*4+j]); }
  kvT[d*S+t] = f2b(acc*b2f(v[t*D+d]));
}
__global__ void k_conv(const u16* kvT, const u16* h, const u16* Dv, float* yT){
  __shared__ float su[S], sh[S];
  int d=blockIdx.x;
  for(int i=threadIdx.x;i<S;i+=256){ su[i]=b2f(kvT[d*S+i]); sh[i]=b2f(h[d*S+i]); }
  __syncthreads();
  for(int t=threadIdx.x;t<S;t+=256){ float acc=0; for(int j=0;j<=t;j++) acc += sh[j]*su[t-j]; yT[d*S+t]=acc + su[t]*b2f(Dv[d]); }
}
template<int MODE>
__global__ void k_gemm(const u16* A, const u16* B0, const u16* B1, u16* H, int K, int N,
                       const u16* q, const u16* gw, const float* yT, u16* out){
  int lane=threadIdx.x&63, w=threadIdx.x>>6;
  int m0=blockIdx.y*64+w*16, n0=blockIdx.x*16;
  f32x4 c0={0,0,0,0}, c1={0,0,0,0};
  for(int k0=0;k0<K;k0+=32){
    bf16x8 a=*(const bf16x8*)(A+(size_t)(m0+(lane&15))*K+k0+8*(lane>>4));
    bf16x8 b,b1;
    for(int j=0;j<8;j++){ b[j]=B0[(size_t)(k0+8*(lane>>4)+j)*N+n0+(lane&15)]; if(MODE==0) b1[j]=B1[(size_t)(k0+8*(lane>>4)+j)*N+n0+(lane&15)]; }
    c0=__builtin_amdgcn_mfma_f32_16x16x32_bf16(a,b,c0,0,0,0);
    if(MODE==0) c1=__builtin_amdgcn_mfma_f32_16x16x32_bf16(a,b1,c1,0,0,0);
  }
  for(int j=0;j<4;j++){
    int r=m0+(lane>>4)*4+j, c=n0+(lane&15);
    if(MODE==0){ float g=c0[j]; float s=g/(1.f+__expf(-g)); H[(size_t)r*N+c]=f2b(s*c1[j]); }
    else { float gs=0; for(int i=0;i<D;i++) gs+=b2f(q[r*D+i])*b2f(gw[i]); float g=1.f/(1.f+__expf(-gs));
      float a=b2f(q[r*D+c])*yT[c*S+r]; out[(size_t)r*N+c]=f2b(g*a+(1.f-g)*c0[j]); }
  }
}
extern "C" void kernel_launch(void* const* d_in, const int* in_sizes, int n_in, void* d_out, int out_size, void* d_ws, size_t ws_size, hipStream_t stream){
  const u16* q=(const u16*)d_in[0]; const u16* k=(const u16*)d_in[1]; const u16* v=(const u16*)d_in[2];
  const u16* sk=(const u16*)d_in[3]; const u16* h=(const u16*)d_in[4]; const u16* Dv=(const u16*)d_in[5];
  const u16* wg=(const u16*)d_in[7]; const u16* wu=(const u16*)d_in[8]; const u16* wd=(const u16*)d_in[9]; const u16* gw=(const u16*)d_in[10];
  char* ws=(char*)d_ws; u16* kvT=(u16*)ws; float* yT=(float*)(ws+(8<<20)); u16* H=(u16*)(ws+(32<<20));
  k_prep<<<dim3(D/64,S/4),256,0,stream>>>(k,v,sk,kvT);
  k_conv<<<D,256,0,stream>>>(kvT,h,Dv,yT);
  k_gemm<0><<<dim3(F/16,S/64),256,0,stream>>>(q,wg,wu,H,D,F,nullptr,nullptr,nullptr,nullptr);
  k_gemm<1><<<dim3(D/16,S/64),256,0,stream>>>(H,wd,nullptr,nullptr,F,D,q,gw,yT,(u16*)d_out);
}
```

```cpp
#include <hip/hip_runtime.h>
#include <hip/hip_bf16.h>
#include <stdint.h>
typedef unsigned short u16;
typedef short bf16x8 __attribute__((ext_vector_type(8)));
typedef float f32x4 __attribute__((ext_vector_type(4)));
constexpr int S=2048, D=1024, F=3584;
__device__ inline float b2f(u16 x){ return __uint_as_float(((unsigned)x)<<16); }
__device__ inline u16 f2b(float f){ unsigned u=__float_as_uint(f); u += 0x7FFF + ((u>>16)&1); return (u16)(u>>16); }

__global__ void k_prep(const u16* k, const u16* v, const u16* sk, u16* kvT){
  int d = blockIdx.x*64 + (threadIdx.x&63); int t = blockIdx.y*4 + (threadIdx.x>>6);
  float acc=0;
  for(int j=0;j<4;j++){ int tt=t-j; float kk = tt>=0? b2f(k[tt*D+d]):0.f; acc += kk*b2f(sk[d*4+j]); }
  kvT[d*S+t] = f2b(acc*b2f(v[t*D+d]));
}
__global__ void k_conv(const u16* kvT, const u16* h, const u16* Dv, float* yT){
  __shared__ float su[S], sh[S];
  int d=blockIdx.x;
  for(int i=threadIdx.x;i<S;i+=256){ su[i]=b2f(kvT[d*S+i]); sh[i]=b2f(h[d*S+i]); }
  __syncthreads();
  for(int t=threadIdx.x;t<S;t+=256){ float acc=0; for(int j=0;j<=t;j++) acc += sh[j]*su[t-j]; yT[d*S+t]=acc + su[t]*b2f(Dv[d]); }
}
template<int MODE>
__global__ void k_gemm(const u16* A, const u16* B0, const u16* B1, u16* H, int K, int N,
                       const u16* q, const u16* gw, const float* yT, u16* out){
  int lane=threadIdx.x&63, w=threadIdx.x>>6;
  int m0=blockIdx.y*64+w*16, n0=blockIdx.x*16;
  f32x4 c0={0,0,0,0}, c1={0,0,0,0};
  for(int k0=0;k0<K;k0+=32){
    bf16x8 a=*(const bf16x8*)(A+(size_t)(m0+(lane&15))*K+k0+8*(lane>>4));
    bf16x8 b,b1;
    for(int j=0;j<8;j++){ b[j]=B0[(size_t)(k0+8*(lane>>4)+j)*N+n0+(lane&15)]; if(MODE==0) b1[j]=B1[(size_t)(k0+8*(lane>>4)+j)*N+n0+(lane&15)]; }
    c0=__builtin_amdgcn_mfma_f32_16x16x32_bf16(a,b,c0,0,0,0);
    if(MODE==0) c1=__builtin_amdgcn_mfma_f32_16x16x32_bf16(a,b1,c1,0,0,0);
  }
  for(int j=0;j<4;j++){
    int r=m0+(lane>>4)*4+j, c=n0+(lane&15);
    if(MODE==0){ float g=c0[j]; float s=g/(1.f+__expf(-g)); H[(size_t)r*N+c]=f2b(s*c1[j]); }
    else { float gs=0; for(int i=0;i<D;i++) gs+=b2f(q[r*D+i])*b2f(gw[i]); float g=1.f/(1.f+__expf(-gs));
      float a=b2f(q[r*D+c])*yT[c*S+r]; out[(size_t)r*N+c]=f2b(g*a+(1.f-g)*c0[j]); }
  }
}

__global__ void k_probe(const char* p, int n, int stride, unsigned* sink){
  unsigned acc=0;
  for(int i=0;i<n;i++){ unsigned r; const char* a=p+(size_t)i*stride; asm volatile("global_load_dword %0, %1, off\n s_waitcnt vmcnt(0)":"=v"(r):"v"(a):"memory"); acc+=r; }
  if(acc==0x12345678) sink[0]=acc;
}
extern "C" void kernel_launch(void* const* d_in, const int* in_sizes, int n_in, void* d_out, int out_size, void* d_ws, size_t ws_size, hipStream_t stream){
  const u16* q=(const u16*)d_in[0]; const u16* k=(const u16*)d_in[1]; const u16* v=(const u16*)d_in[2];
  const u16* sk=(const u16*)d_in[3]; const u16* h=(const u16*)d_in[4]; const u16* Dv=(const u16*)d_in[5];
  const u16* wg=(const u16*)d_in[7]; const u16* wu=(const u16*)d_in[8]; const u16* wd=(const u16*)d_in[9]; const u16* gw=(const u16*)d_in[10];
  char* ws=(char*)d_ws; u16* kvT=(u16*)ws; float* yT=(float*)(ws+(8<<20)); u16* H=(u16*)(ws+(32<<20));
  k_prep<<<dim3(D/64,S/4),256,0,stream>>>(k,v,sk,kvT);
  k_conv<<<D,256,0,stream>>>(kvT,h,Dv,yT);
  k_gemm<0><<<dim3(F/16,S/64),256,0,stream>>>(q,wg,wu,H,D,F,nullptr,nullptr,nullptr,nullptr);
  k_gemm<1><<<dim3(D/16,S/64),256,0,stream>>>(H,wd,nullptr,nullptr,F,D,q,gw,yT,(u16*)d_out);
}
```

```cpp
#include <hip/hip_runtime.h>
#include <hip/hip_bf16.h>

__global__ __launch_bounds__(256,1) void gated_mix__ssm_conv_h3__swiglu_ffn__prompt_kernel(const void* q, unsigned q_bytes, unsigned q_fmt, const void* wg, unsigned wg_bytes, unsigned wg_fmt, const void* wu, unsigned wu_bytes, unsigned wu_fmt, const void* ws, unsigned ws_bytes, unsigned ws_fmt, const void* wd, unsigned wd_bytes, unsigned wd_fmt, const void* hk, unsigned hk_bytes, unsigned hk_fmt, const void* out, unsigned out_bytes, unsigned out_fmt, const void* Dv, const void* gw, const void* k, unsigned k_bytes, unsigned k_fmt, const void* v, unsigned v_bytes, unsigned v_fmt, const void* sk){
  asm volatile(
    "KSTART_gated_mix__ssm_conv_h3__swiglu_ffn__prompt_kernel:\n"
    "s_cmp_ge_u32 %1, 224\n"
    "s_cbranch_scc1 SPARE_gated_mix__ssm_conv_h3__swiglu_ffn__prompt_kernel\n"
    "s_load_dwordx16 s[8:23], %0, 0\n"
    "s_load_dwordx16 s[76:91], %0, 64\n"
    "v_lshrrev_b32 v16, 6, %2\n"
    "v_and_b32 v1, 63, %2\n"
    "v_readfirstlane_b32 s34, v16\n"
    "s_mov_b32 s39, %1\n"
    "s_and_b32 s35, s39, 1\n"
    "s_lshl_b32 s35, s35, 2\n"
    "s_bfe_u32 s30, s39, 0x20003\n"
    "s_add_u32 s35, s35, s30\n"
    "v_lshlrev_b32 v16, 8, %2\n"
    "v_and_b32 v16, 0x7f800, v16\n"
    "v_lshrrev_b32 v17, 4, v1\n"
    "v_and_b32 v18, 7, v1\n"
    "v_xor_b32 v17, v17, v18\n"
    "s_mul_i32 s30, s34, 114688\n"
    "s_lshl_b32 s24, s35, 19\n"
    "s_add_u32 s24, s24, s30\n"
    "s_lshl_b32 s26, s34, 13\n"
    "v_lshl_add_u32 v9, v17, 4, v16\n"
    "v_xor_b32 v17, 4, v17\n"
    "v_lshl_add_u32 v10, v17, 4, v16\n"
    "v_add_u32 v11, 30720, v9\n"
    "v_add_u32 v12, 46080, v10\n"
    "v_add_u32 v10, 15360, v10\n"
    "s_bfe_u32 s36, s39, 0x20001\n"
    "s_mul_i32 s36, s36, 7\n"
    "s_lshr_b32 s30, s39, 5\n"
    "s_add_u32 s36, s36, s30\n"
    "v_lshrrev_b32 v16, 4, v1\n"
    "v_mul_u32_u24 v17, 7168, v16\n"
    "s_and_b32 s30, s34, 1\n"
    "s_lshl_b32 s30, s30, 2\n"
    "v_or_b32 v16, s30, v16\n"
    "v_bfe_u32 v18, v1, 1, 3\n"
    "v_xor_b32 v16, v16, v18\n"
    "v_lshl_add_u32 v17, v16, 5, v17\n"
    "v_and_b32 v16, 1, v1\n"
    "v_lshl_add_u32 v13, v16, 4, v17\n"
    "v_add_u32 v14, 27648, v13\n"
    "s_lshl_b32 s25, s36, 8\n"
    "s_mul_i32 s30, s34, 57344\n"
    "s_add_u32 s25, s25, s30\n"
    "s_lshl_b32 s28, s34, 12\n"
    "s_add_u32 s28, s28, 98304\n"
    "s_waitcnt lgkmcnt(0)\n"
    "s_add_u32 m0, s26, 0\n"
    "s_nop 0\n"
    "buffer_load_dwordx4 v9, s[8:11], s24 offen offset:0 lds\n"
    "buffer_load_dwordx4 v10, s[8:11], s24 offen offset:1024 lds\n"
    "buffer_load_dwordx4 v11, s[8:11], s24 offen offset:2048 lds\n"
    "buffer_load_dwordx4 v12, s[8:11], s24 offen offset:3072 lds\n"
    "s_sub_u32 s16, s16, 2048\n"
    "s_subb_u32 s17, s17, 0\n"
    "s_add_u32 s24, s24, 65536\n"
    "s_add_u32 m0, s28, 0\n"
    "s_nop 0\n"
    "buffer_load_dwordx4 v13, s[12:15], s25 offen lds\n"
    "buffer_load_dwordx4 v14, s[12:15], s25 offen offset:1024 lds\n"
    "buffer_load_dwordx4 v13, s[16:19], s25 offen offset:2048 lds\n"
    "buffer_load_dwordx4 v14, s[16:19], s25 offen offset:3072 lds\n"
    "s_add_u32 s25, s25, 229376\n"
    "s_add_u32 m0, s26, 4096\n"
    "s_nop 0\n"
    "buffer_load_dwordx4 v9, s[8:11], s24 offen offset:0 lds\n"
    "buffer_load_dwordx4 v10, s[8:11], s24 offen offset:1024 lds\n"
    "buffer_load_dwordx4 v11, s[8:11], s24 offen offset:2048 lds\n"
    "buffer_load_dwordx4 v12, s[8:11], s24 offen offset:3072 lds\n"
    "s_sub_u32 s24, s24, 65408\n"
    "s_add_u32 s26, s26, 32768\n"
    "s_cmp_ge_u32 s26, 98304\n"
    "s_cselect_b32 s30, 98304, 0\n"
    "s_sub_u32 s26, s26, s30\n"
    "s_add_u32 m0, s28, 16384\n"
    "s_nop 0\n"
    "buffer_load_dwordx4 v13, s[12:15], s25 offen lds\n"
    "buffer_load_dwordx4 v14, s[12:15], s25 offen offset:1024 lds\n"
    "buffer_load_dwordx4 v13, s[16:19], s25 offen offset:2048 lds\n"
    "buffer_load_dwordx4 v14, s[16:19], s25 offen offset:3072 lds\n"
    "s_add_u32 s25, s25, 229376\n"
    "s_add_u32 m0, s26, 0\n"
    "s_nop 0\n"
    "buffer_load_dwordx4 v9, s[8:11], s24 offen offset:0 lds\n"
    "buffer_load_dwordx4 v10, s[8:11], s24 offen offset:1024 lds\n"
    "buffer_load_dwordx4 v11, s[8:11], s24 offen offset:2048 lds\n"
    "buffer_load_dwordx4 v12, s[8:11], s24 offen offset:3072 lds\n"
    "s_add_u32 s24, s24, 65536\n"
    "s_add_u32 s66, s20, 40960000\n"
    "s_addc_u32 s67, s21, 0\n"
    "s_load_dwordx2 s[70:71], s[66:67], 0\n"
    "s_lshl_b32 s30, s39, 2\n"
    "s_add_u32 s30, s30, 1024\n"
    "s_load_dword s75, s[88:89], s30\n"
    "s_add_u32 m0, s28, 32768\n"
    "s_nop 0\n"
    "buffer_load_dwordx4 v13, s[12:15], s25 offen lds\n"
    "buffer_load_dwordx4 v14, s[12:15], s25 offen offset:1024 lds\n"
    "buffer_load_dwordx4 v13, s[16:19], s25 offen offset:2048 lds\n"
    "buffer_load_dwordx4 v14, s[16:19], s25 offen offset:3072 lds\n"
    "s_add_u32 s25, s25, 229376\n"
    "s_add_u32 m0, s26, 4096\n"
    "s_nop 0\n"
    "buffer_load_dwordx4 v9, s[8:11], s24 offen offset:0 lds\n"
    "buffer_load_dwordx4 v10, s[8:11], s24 offen offset:1024 lds\n"
    "buffer_load_dwordx4 v11, s[8:11], s24 offen offset:2048 lds\n"
    "buffer_load_dwordx4 v12, s[8:11], s24 offen offset:3072 lds\n"
    "s_sub_u32 s24, s24, 65408\n"
    "s_add_u32 s26, s26, 32768\n"
    "s_cmp_ge_u32 s26, 98304\n"
    "s_cselect_b32 s30, 98304, 0\n"
    "s_sub_u32 s26, s26, s30\n"
    "s_add_u32 m0, s28, 49152\n"
    "s_nop 0\n"
    "buffer_load_dwordx4 v13, s[12:15], s25 offen lds\n"
    "buffer_load_dwordx4 v14, s[12:15], s25 offen offset:1024 lds\n"
    "buffer_load_dwordx4 v13, s[16:19], s25 offen offset:2048 lds\n"
    "buffer_load_dwordx4 v14, s[16:19], s25 offen offset:3072 lds\n"
    "s_add_u32 s25, s25, 229376\n"
    "s_add_u32 m0, s26, 0\n"
    "s_nop 0\n"
    "buffer_load_dwordx4 v9, s[8:11], s24 offen offset:0 lds\n"
    "buffer_load_dwordx4 v10, s[8:11], s24 offen offset:1024 lds\n"
    "buffer_load_dwordx4 v11, s[8:11], s24 offen offset:2048 lds\n"
    "buffer_load_dwordx4 v12, s[8:11], s24 offen offset:3072 lds\n"
    "s_add_u32 s24, s24, 65536\n"
    "s_getpc_b64 s[92:93]\n"
    "PCA_gated_mix__ssm_conv_h3__swiglu_ffn__prompt_kernel:\n"
    "s_sub_u32 s92, s92, PCA_gated_mix__ssm_conv_h3__swiglu_ffn__prompt_kernel-KSTART_gated_mix__ssm_conv_h3__swiglu_ffn__prompt_kernel\n"
    "s_subb_u32 s93, s93, 0\n"
    "s_mov_b32 s94, KEND_gated_mix__ssm_conv_h3__swiglu_ffn__prompt_kernel-KSTART_gated_mix__ssm_conv_h3__swiglu_ffn__prompt_kernel\n"
    "s_mov_b32 s95, 0x20000\n"
    "s_lshr_b32 s30, s39, 3\n"
    "s_lshl_b32 s30, s30, 2\n"
    "s_add_u32 s30, s30, s34\n"
    "v_mul_u32_u24 v15, 112, v1\n"
    "v_add_lshl_u32 v15, v15, s30, 7\n"
    "buffer_load_dword a50, v15, s[92:95], 0 offen\n"
    "s_mov_b32 s27, 0\n"
    "s_lshr_b32 s30, s34, 1\n"
    "s_lshl_b32 s30, s30, 14\n"
    "v_and_b32 v16, 15, v1\n"
    "v_lshlrev_b32 v16, 7, v16\n"
    "v_add_u32 v16, s30, v16\n"
    "v_lshrrev_b32 v17, 4, v1\n"
    "v_bfe_u32 v18, v1, 1, 3\n"
    "v_xor_b32 v17, v17, v18\n"
    "v_lshl_add_u32 v2, v17, 4, v16\n"
    "v_xor_b32 v3, 64, v2\n"
    "v_lshrrev_b32 v16, 4, v1\n"
    "v_lshlrev_b32 v17, 12, v16\n"
    "v_bfe_u32 v18, v1, 2, 2\n"
    "v_lshl_add_u32 v17, v18, 8, v17\n"
    "v_and_b32 v19, 3, v1\n"
    "v_lshl_add_u32 v17, v19, 3, v17\n"
    "v_add_u32 v17, 98304, v17\n"
    "v_and_b32 v16, 1, v16\n"
    "v_lshl_or_b32 v16, v16, 2, v18\n"
    "s_and_b32 s30, s34, 1\n"
    "s_lshl_b32 s30, s30, 2\n"
    "v_xor_b32 v16, s30, v16\n"
    "v_xor_b32 v18, 0, v16\n"
    "v_lshl_add_u32 v5, v18, 5, v17\n"
    "v_xor_b32 v18, 1, v16\n"
    "v_lshl_add_u32 v6, v18, 5, v17\n"
    "v_xor_b32 v18, 2, v16\n"
    "v_lshl_add_u32 v7, v18, 5, v17\n"
    "v_xor_b32 v18, 3, v16\n"
    "v_lshl_add_u32 v8, v18, 5, v17\n"
    "s_lshl_b32 s73, s39, 2\n"
    "s_add_u32 s73, s73, s34\n"
    "s_lshl_b32 s73, s73, 11\n"
    "s_add_u32 s73, s73, 2097152\n"
    "v_lshlrev_b32 v27, 7, v1\n"
    "v_and_b32 v28, 0x7ff, v27\n"
    "v_and_b32 v26, 0xfff, v27\n"
    "s_lshl_b32 s72, s39, 2\n"
    "s_add_u32 s72, s72, s34\n"
    "s_lshl_b32 s72, s72, 13\n"
    "s_waitcnt vmcnt(25)\n"
    "s_barrier\n"
    "v_mov_b32 v4, v2\n"
    "ds_read_b64_tr_b16 a[128:129], v5 offset:0\n"
    "ds_read_b64_tr_b16 a[130:131], v5 offset:1024\n"
    "ds_read_b128 a[160:163], v4 offset:0\n"
    "ds_read_b64_tr_b16 a[132:133], v6 offset:0\n"
    "ds_read_b64_tr_b16 a[134:135], v6 offset:1024\n"
    "ds_read_b128 a[164:167], v4 offset:2048\n"
    "ds_read_b64_tr_b16 a[136:137], v7 offset:0\n"
    "ds_read_b64_tr_b16 a[138:139], v7 offset:1024\n"
    "ds_read_b128 a[168:171], v4 offset:4096\n"
    "ds_read_b64_tr_b16 a[140:141], v8 offset:0\n"
    "ds_read_b64_tr_b16 a[142:143], v8 offset:1024\n"
    "ds_read_b128 a[172:175], v4 offset:6144\n"
    "ds_read_b64_tr_b16 a[144:145], v5 offset:2048\n"
    "ds_read_b64_tr_b16 a[146:147], v5 offset:3072\n"
    "ds_read_b128 a[176:179], v4 offset:8192\n"
    "ds_read_b64_tr_b16 a[148:149], v6 offset:2048\n"
    "ds_read_b64_tr_b16 a[150:151], v6 offset:3072\n"
    "ds_read_b128 a[180:183], v4 offset:10240\n"
    "ds_read_b64_tr_b16 a[152:153], v7 offset:2048\n"
    "ds_read_b64_tr_b16 a[154:155], v7 offset:3072\n"
    "ds_read_b128 a[184:187], v4 offset:12288\n"
    "ds_read_b64_tr_b16 a[156:157], v8 offset:2048\n"
    "ds_read_b64_tr_b16 a[158:159], v8 offset:3072\n"
    "ds_read_b128 a[188:191], v4 offset:14336\n"
    "s_waitcnt vmcnt(21) lgkmcnt(0)\n"
    "s_barrier\n"
    "s_add_u32 s70, s70, 1\n"
    "s_mov_b32 s29, 7\n"
    "v_mfma_f32_16x16x32_bf16 v[128:131], a[128:131], a[160:163], 0\n"
    "v_add_u32 v4, s27, v3\n"
    "s_add_u32 m0, s28, 0\n"
    "v_mfma_f32_16x16x32_bf16 v[132:135], a[128:131], a[164:167], 0\n"
    "ds_read_b64_tr_b16 a[192:193], v5 offset:16384\n"
    "v_mfma_f32_16x16x32_bf16 v[136:139], a[128:131], a[168:171], 0\n"
    "buffer_load_dwordx4 v13, s[12:15], s25 offen lds\n"
    "v_mfma_f32_16x16x32_bf16 v[140:143], a[128:131], a[172:175], 0\n"
    "ds_read_b64_tr_b16 a[194:195], v5 offset:17408\n"
    "v_mfma_f32_16x16x32_bf16 v[144:147], a[128:131], a[176:179], 0\n"
    "v_mfma_f32_16x16x32_bf16 v[148:151], a[128:131], a[180:183], 0\n"
    "ds_read_b128 a[224:227], v4 offset:0\n"
    "v_mfma_f32_16x16x32_bf16 v[152:155], a[128:131], a[184:187], 0\n"
    "v_mfma_f32_16x16x32_bf16 v[156:159], a[128:131], a[188:191], 0\n"
    "ds_read_b64_tr_b16 a[196:197], v6 offset:16384\n"
    "v_mfma_f32_16x16x32_bf16 v[188:191], a[132:135], a[188:191], 0\n"
    "buffer_load_dwordx4 v14, s[12:15], s25 offen offset:1024 lds\n"
    "v_mfma_f32_16x16x32_bf16 v[184:187], a[132:135], a[184:187], 0\n"
    "ds_read_b64_tr_b16 a[198:199], v6 offset:17408\n"
    "v_mfma_f32_16x16x32_bf16 v[180:183], a[132:135], a[180:183], 0\n"
    "v_mfma_f32_16x16x32_bf16 v[176:179], a[132:135], a[176:179], 0\n"
    "ds_read_b128 a[228:231], v4 offset:2048\n"
    "v_mfma_f32_16x16x32_bf16 v[172:175], a[132:135], a[172:175], 0\n"
    "v_mfma_f32_16x16x32_bf16 v[168:171], a[132:135], a[168:171], 0\n"
    "ds_read_b64_tr_b16 a[200:201], v7 offset:16384\n"
    "v_mfma_f32_16x16x32_bf16 v[164:167], a[132:135], a[164:167], 0\n"
    "buffer_load_dwordx4 v13, s[16:19], s25 offen offset:2048 lds\n"
    "v_mfma_f32_16x16x32_bf16 v[160:163], a[132:135], a[160:163], 0\n"
    "ds_read_b64_tr_b16 a[202:203], v7 offset:17408\n"
    "v_mfma_f32_16x16x32_bf16 v[192:195], a[136:139], a[160:163], 0\n"
    "v_mfma_f32_16x16x32_bf16 v[196:199], a[136:139], a[164:167], 0\n"
    "ds_read_b128 a[232:235], v4 offset:4096\n"
    "v_mfma_f32_16x16x32_bf16 v[200:203], a[136:139], a[168:171], 0\n"
    "v_mfma_f32_16x16x32_bf16 v[204:207], a[136:139], a[172:175], 0\n"
    "ds_read_b64_tr_b16 a[204:205], v8 offset:16384\n"
    "v_mfma_f32_16x16x32_bf16 v[208:211], a[136:139], a[176:179], 0\n"
    "buffer_load_dwordx4 v14, s[16:19], s25 offen offset:3072 lds\n"
    "v_mfma_f32_16x16x32_bf16 v[212:215], a[136:139], a[180:183], 0\n"
    "ds_read_b64_tr_b16 a[206:207], v8 offset:17408\n"
    "s_add_u32 s25, s25, 229376\n"
    "v_mfma_f32_16x16x32_bf16 v[216:219], a[136:139], a[184:187], 0\n"
    "v_mfma_f32_16x16x32_bf16 v[220:223], a[136:139], a[188:191], 0\n"
    "ds_read_b128 a[236:239], v4 offset:6144\n"
    "v_mfma_f32_16x16x32_bf16 v[252:255], a[140:143], a[188:191], 0\n"
    "s_add_u32 m0, s26, 4096\n"
    "v_mfma_f32_16x16x32_bf16 v[248:251], a[140:143], a[184:187], 0\n"
    "ds_read_b64_tr_b16 a[208:209], v5 offset:18432\n"
    "v_mfma_f32_16x16x32_bf16 v[244:247], a[140:143], a[180:183], 0\n"
    "buffer_load_dwordx4 v9, s[8:11], s24 offen offset:0 lds\n"
    "v_mfma_f32_16x16x32_bf16 v[240:243], a[140:143], a[176:179], 0\n"
    "ds_read_b64_tr_b16 a[210:211], v5 offset:19456\n"
    "v_mfma_f32_16x16x32_bf16 v[236:239], a[140:143], a[172:175], 0\n"
    "v_mfma_f32_16x16x32_bf16 v[232:235], a[140:143], a[168:171], 0\n"
    "ds_read_b128 a[240:243], v4 offset:8192\n"
    "v_mfma_f32_16x16x32_bf16 v[228:231], a[140:143], a[164:167], 0\n"
    "v_mfma_f32_16x16x32_bf16 v[224:227], a[140:143], a[160:163], 0\n"
    "ds_read_b64_tr_b16 a[212:213], v6 offset:18432\n"
    "v_mfma_f32_16x16x32_bf16 v[32:35], a[144:147], a[160:163], 0\n"
    "buffer_load_dwordx4 v10, s[8:11], s24 offen offset:1024 lds\n"
    "v_mfma_f32_16x16x32_bf16 v[36:39], a[144:147], a[164:167], 0\n"
    "ds_read_b64_tr_b16 a[214:215], v6 offset:19456\n"
    "v_mfma_f32_16x16x32_bf16 v[40:43], a[144:147], a[168:171], 0\n"
    "v_mfma_f32_16x16x32_bf16 v[44:47], a[144:147], a[172:175], 0\n"
    "ds_read_b128 a[244:247], v4 offset:10240\n"
    "v_mfma_f32_16x16x32_bf16 v[48:51], a[144:147], a[176:179], 0\n"
    "v_mfma_f32_16x16x32_bf16 v[52:55], a[144:147], a[180:183], 0\n"
    "ds_read_b64_tr_b16 a[216:217], v7 offset:18432\n"
    "v_mfma_f32_16x16x32_bf16 v[56:59], a[144:147], a[184:187], 0\n"
    "buffer_load_dwordx4 v11, s[8:11], s24 offen offset:2048 lds\n"
    "v_mfma_f32_16x16x32_bf16 v[60:63], a[144:147], a[188:191], 0\n"
    "ds_read_b64_tr_b16 a[218:219], v7 offset:19456\n"
    "v_mfma_f32_16x16x32_bf16 v[92:95], a[148:151], a[188:191], 0\n"
    "v_mfma_f32_16x16x32_bf16 v[88:91], a[148:151], a[184:187], 0\n"
    "ds_read_b128 a[248:251], v4 offset:12288\n"
    "v_mfma_f32_16x16x32_bf16 v[84:87], a[148:151], a[180:183], 0\n"
    "v_mfma_f32_16x16x32_bf16 v[80:83], a[148:151], a[176:179], 0\n"
    "ds_read_b64_tr_b16 a[220:221], v8 offset:18432\n"
    "v_mfma_f32_16x16x32_bf16 v[76:79], a[148:151], a[172:175], 0\n"
    "buffer_load_dwordx4 v12, s[8:11], s24 offen offset:3072 lds\n"
    "v_mfma_f32_16x16x32_bf16 v[72:75], a[148:151], a[168:171], 0\n"
    "ds_read_b64_tr_b16 a[222:223], v8 offset:19456\n"
    "s_sub_u32 s24, s24, 65408\n"
    "v_mfma_f32_16x16x32_bf16 v[68:71], a[148:151], a[164:167], 0\n"
    "s_add_u32 s26, s26, 32768\n"
    "v_mfma_f32_16x16x32_bf16 v[64:67], a[148:151], a[160:163], 0\n"
    "ds_read_b128 a[252:255], v4 offset:14336\n"
    "s_cmp_ge_u32 s26, 98304\n"
    "v_mfma_f32_16x16x32_bf16 v[96:99], a[152:155], a[160:163], 0\n"
    "s_cselect_b32 s30, 98304, 0\n"
    "v_mfma_f32_16x16x32_bf16 v[100:103], a[152:155], a[164:167], 0\n"
    "s_sub_u32 s26, s26, s30\n"
    "v_mfma_f32_16x16x32_bf16 v[104:107], a[152:155], a[168:171], 0\n"
    "v_mfma_f32_16x16x32_bf16 v[108:111], a[152:155], a[172:175], 0\n"
    "v_mfma_f32_16x16x32_bf16 v[112:115], a[152:155], a[176:179], 0\n"
    "v_mfma_f32_16x16x32_bf16 v[116:119], a[152:155], a[180:183], 0\n"
    "v_mfma_f32_16x16x32_bf16 v[120:123], a[152:155], a[184:187], 0\n"
    "s_add_u32 s27, s27, 32768\n"
    "v_mfma_f32_16x16x32_bf16 v[124:127], a[152:155], a[188:191], 0\n"
    "s_cmp_ge_u32 s27, 98304\n"
    "v_mfma_f32_16x16x32_bf16 a[28:31], a[156:159], a[188:191], 0\n"
    "s_cselect_b32 s31, 98304, 0\n"
    "v_mfma_f32_16x16x32_bf16 a[24:27], a[156:159], a[184:187], 0\n"
    "s_sub_u32 s27, s27, s31\n"
    "v_mfma_f32_16x16x32_bf16 a[20:23], a[156:159], a[180:183], 0\n"
    "v_mfma_f32_16x16x32_bf16 a[16:19], a[156:159], a[176:179], 0\n"
    "v_mfma_f32_16x16x32_bf16 a[12:15], a[156:159], a[172:175], 0\n"
    "v_mfma_f32_16x16x32_bf16 a[8:11], a[156:159], a[168:171], 0\n"
    "v_mfma_f32_16x16x32_bf16 a[4:7], a[156:159], a[164:167], 0\n"
    "v_mfma_f32_16x16x32_bf16 a[0:3], a[156:159], a[160:163], 0\n"
    "s_waitcnt vmcnt(16) lgkmcnt(0)\n"
    "s_barrier\n"
    "s_branch LOOP1B_gated_mix__ssm_conv_h3__swiglu_ffn__prompt_kernel\n"
    "LOOP1_gated_mix__ssm_conv_h3__swiglu_ffn__prompt_kernel:\n"
    "v_mfma_f32_16x16x32_bf16 v[128:131], a[128:131], a[160:163], v[128:131]\n"
    "v_add_u32 v4, s27, v3\n"
    "s_add_u32 m0, s28, 0\n"
    "v_mfma_f32_16x16x32_bf16 v[132:135], a[128:131], a[164:167], v[132:135]\n"
    "ds_read_b64_tr_b16 a[192:193], v5 offset:16384\n"
    "v_mfma_f32_16x16x32_bf16 v[136:139], a[128:131], a[168:171], v[136:139]\n"
    "buffer_load_dwordx4 v13, s[12:15], s25 offen lds\n"
    "v_mfma_f32_16x16x32_bf16 v[140:143], a[128:131], a[172:175], v[140:143]\n"
    "ds_read_b64_tr_b16 a[194:195], v5 offset:17408\n"
    "v_mfma_f32_16x16x32_bf16 v[144:147], a[128:131], a[176:179], v[144:147]\n"
    "v_mfma_f32_16x16x32_bf16 v[148:151], a[128:131], a[180:183], v[148:151]\n"
    "ds_read_b128 a[224:227], v4 offset:0\n"
    "v_mfma_f32_16x16x32_bf16 v[152:155], a[128:131], a[184:187], v[152:155]\n"
    "v_mfma_f32_16x16x32_bf16 v[156:159], a[128:131], a[188:191], v[156:159]\n"
    "ds_read_b64_tr_b16 a[196:197], v6 offset:16384\n"
    "v_mfma_f32_16x16x32_bf16 v[188:191], a[132:135], a[188:191], v[188:191]\n"
    "buffer_load_dwordx4 v14, s[12:15], s25 offen offset:1024 lds\n"
    "v_mfma_f32_16x16x32_bf16 v[184:187], a[132:135], a[184:187], v[184:187]\n"
    "ds_read_b64_tr_b16 a[198:199], v6 offset:17408\n"
    "v_mfma_f32_16x16x32_bf16 v[180:183], a[132:135], a[180:183], v[180:183]\n"
    "v_mfma_f32_16x16x32_bf16 v[176:179], a[132:135], a[176:179], v[176:179]\n"
    "ds_read_b128 a[228:231], v4 offset:2048\n"
    "v_mfma_f32_16x16x32_bf16 v[172:175], a[132:135], a[172:175], v[172:175]\n"
    "v_mfma_f32_16x16x32_bf16 v[168:171], a[132:135], a[168:171], v[168:171]\n"
    "ds_read_b64_tr_b16 a[200:201], v7 offset:16384\n"
    "v_mfma_f32_16x16x32_bf16 v[164:167], a[132:135], a[164:167], v[164:167]\n"
    "buffer_load_dwordx4 v13, s[16:19], s25 offen offset:2048 lds\n"
    "v_mfma_f32_16x16x32_bf16 v[160:163], a[132:135], a[160:163], v[160:163]\n"
    "ds_read_b64_tr_b16 a[202:203], v7 offset:17408\n"
    "v_mfma_f32_16x16x32_bf16 v[192:195], a[136:139], a[160:163], v[192:195]\n"
    "v_mfma_f32_16x16x32_bf16 v[196:199], a[136:139], a[164:167], v[196:199]\n"
    "ds_read_b128 a[232:235], v4 offset:4096\n"
    "v_mfma_f32_16x16x32_bf16 v[200:203], a[136:139], a[168:171], v[200:203]\n"
    "v_mfma_f32_16x16x32_bf16 v[204:207], a[136:139], a[172:175], v[204:207]\n"
    "ds_read_b64_tr_b16 a[204:205], v8 offset:16384\n"
    "v_mfma_f32_16x16x32_bf16 v[208:211], a[136:139], a[176:179], v[208:211]\n"
    "buffer_load_dwordx4 v14, s[16:19], s25 offen offset:3072 lds\n"
    "v_mfma_f32_16x16x32_bf16 v[212:215], a[136:139], a[180:183], v[212:215]\n"
    "ds_read_b64_tr_b16 a[206:207], v8 offset:17408\n"
    "s_add_u32 s25, s25, 229376\n"
    "v_mfma_f32_16x16x32_bf16 v[216:219], a[136:139], a[184:187], v[216:219]\n"
    "v_mfma_f32_16x16x32_bf16 v[220:223], a[136:139], a[188:191], v[220:223]\n"
    "ds_read_b128 a[236:239], v4 offset:6144\n"
    "v_mfma_f32_16x16x32_bf16 v[252:255], a[140:143], a[188:191], v[252:255]\n"
    "s_add_u32 m0, s26, 4096\n"
    "v_mfma_f32_16x16x32_bf16 v[248:251], a[140:143], a[184:187], v[248:251]\n"
    "ds_read_b64_tr_b16 a[208:209], v5 offset:18432\n"
    "v_mfma_f32_16x16x32_bf16 v[244:247], a[140:143], a[180:183], v[244:247]\n"
    "buffer_load_dwordx4 v9, s[8:11], s24 offen offset:0 lds\n"
    "v_mfma_f32_16x16x32_bf16 v[240:243], a[140:143], a[176:179], v[240:243]\n"
    "ds_read_b64_tr_b16 a[210:211], v5 offset:19456\n"
    "v_mfma_f32_16x16x32_bf16 v[236:239], a[140:143], a[172:175], v[236:239]\n"
    "v_mfma_f32_16x16x32_bf16 v[232:235], a[140:143], a[168:171], v[232:235]\n"
    "ds_read_b128 a[240:243], v4 offset:8192\n"
    "v_mfma_f32_16x16x32_bf16 v[228:231], a[140:143], a[164:167], v[228:231]\n"
    "v_mfma_f32_16x16x32_bf16 v[224:227], a[140:143], a[160:163], v[224:227]\n"
    "ds_read_b64_tr_b16 a[212:213], v6 offset:18432\n"
    "v_mfma_f32_16x16x32_bf16 v[32:35], a[144:147], a[160:163], v[32:35]\n"
    "buffer_load_dwordx4 v10, s[8:11], s24 offen offset:1024 lds\n"
    "v_mfma_f32_16x16x32_bf16 v[36:39], a[144:147], a[164:167], v[36:39]\n"
    "ds_read_b64_tr_b16 a[214:215], v6 offset:19456\n"
    "v_mfma_f32_16x16x32_bf16 v[40:43], a[144:147], a[168:171], v[40:43]\n"
    "v_mfma_f32_16x16x32_bf16 v[44:47], a[144:147], a[172:175], v[44:47]\n"
    "ds_read_b128 a[244:247], v4 offset:10240\n"
    "v_mfma_f32_16x16x32_bf16 v[48:51], a[144:147], a[176:179], v[48:51]\n"
    "v_mfma_f32_16x16x32_bf16 v[52:55], a[144:147], a[180:183], v[52:55]\n"
    "ds_read_b64_tr_b16 a[216:217], v7 offset:18432\n"
    "v_mfma_f32_16x16x32_bf16 v[56:59], a[144:147], a[184:187], v[56:59]\n"
    "buffer_load_dwordx4 v11, s[8:11], s24 offen offset:2048 lds\n"
    "v_mfma_f32_16x16x32_bf16 v[60:63], a[144:147], a[188:191], v[60:63]\n"
    "ds_read_b64_tr_b16 a[218:219], v7 offset:19456\n"
    "v_mfma_f32_16x16x32_bf16 v[92:95], a[148:151], a[188:191], v[92:95]\n"
    "v_mfma_f32_16x16x32_bf16 v[88:91], a[148:151], a[184:187], v[88:91]\n"
    "ds_read_b128 a[248:251], v4 offset:12288\n"
    "v_mfma_f32_16x16x32_bf16 v[84:87], a[148:151], a[180:183], v[84:87]\n"
    "v_mfma_f32_16x16x32_bf16 v[80:83], a[148:151], a[176:179], v[80:83]\n"
    "ds_read_b64_tr_b16 a[220:221], v8 offset:18432\n"
    "v_mfma_f32_16x16x32_bf16 v[76:79], a[148:151], a[172:175], v[76:79]\n"
    "buffer_load_dwordx4 v12, s[8:11], s24 offen offset:3072 lds\n"
    "v_mfma_f32_16x16x32_bf16 v[72:75], a[148:151], a[168:171], v[72:75]\n"
    "ds_read_b64_tr_b16 a[222:223], v8 offset:19456\n"
    "s_sub_u32 s24, s24, 65408\n"
    "v_mfma_f32_16x16x32_bf16 v[68:71], a[148:151], a[164:167], v[68:71]\n"
    "s_add_u32 s26, s26, 32768\n"
    "v_mfma_f32_16x16x32_bf16 v[64:67], a[148:151], a[160:163], v[64:67]\n"
    "ds_read_b128 a[252:255], v4 offset:14336\n"
    "s_cmp_ge_u32 s26, 98304\n"
    "v_mfma_f32_16x16x32_bf16 v[96:99], a[152:155], a[160:163], v[96:99]\n"
    "s_cselect_b32 s30, 98304, 0\n"
    "v_mfma_f32_16x16x32_bf16 v[100:103], a[152:155], a[164:167], v[100:103]\n"
    "s_sub_u32 s26, s26, s30\n"
    "v_mfma_f32_16x16x32_bf16 v[104:107], a[152:155], a[168:171], v[104:107]\n"
    "v_mfma_f32_16x16x32_bf16 v[108:111], a[152:155], a[172:175], v[108:111]\n"
    "v_mfma_f32_16x16x32_bf16 v[112:115], a[152:155], a[176:179], v[112:115]\n"
    "v_mfma_f32_16x16x32_bf16 v[116:119], a[152:155], a[180:183], v[116:119]\n"
    "v_mfma_f32_16x16x32_bf16 v[120:123], a[152:155], a[184:187], v[120:123]\n"
    "s_add_u32 s27, s27, 32768\n"
    "v_mfma_f32_16x16x32_bf16 v[124:127], a[152:155], a[188:191], v[124:127]\n"
    "s_cmp_ge_u32 s27, 98304\n"
    "v_mfma_f32_16x16x32_bf16 a[28:31], a[156:159], a[188:191], a[28:31]\n"
    "s_cselect_b32 s31, 98304, 0\n"
    "v_mfma_f32_16x16x32_bf16 a[24:27], a[156:159], a[184:187], a[24:27]\n"
    "s_sub_u32 s27, s27, s31\n"
    "v_mfma_f32_16x16x32_bf16 a[20:23], a[156:159], a[180:183], a[20:23]\n"
    "v_mfma_f32_16x16x32_bf16 a[16:19], a[156:159], a[176:179], a[16:19]\n"
    "v_mfma_f32_16x16x32_bf16 a[12:15], a[156:159], a[172:175], a[12:15]\n"
    "v_mfma_f32_16x16x32_bf16 a[8:11], a[156:159], a[168:171], a[8:11]\n"
    "v_mfma_f32_16x16x32_bf16 a[4:7], a[156:159], a[164:167], a[4:7]\n"
    "v_mfma_f32_16x16x32_bf16 a[0:3], a[156:159], a[160:163], a[0:3]\n"
    "s_waitcnt vmcnt(16) lgkmcnt(0)\n"
    "s_barrier\n"
    "LOOP1B_gated_mix__ssm_conv_h3__swiglu_ffn__prompt_kernel:\n"
    "v_mfma_f32_16x16x32_bf16 v[128:131], a[192:195], a[224:227], v[128:131]\n"
    "v_add_u32 v4, s27, v2\n"
    "s_add_u32 m0, s28, 16384\n"
    "v_mfma_f32_16x16x32_bf16 v[132:135], a[192:195], a[228:231], v[132:135]\n"
    "ds_read_b64_tr_b16 a[128:129], v5 offset:32768\n"
    "v_mfma_f32_16x16x32_bf16 v[136:139], a[192:195], a[232:235], v[136:139]\n"
    "buffer_load_dwordx4 v13, s[12:15], s25 offen lds\n"
    "v_mfma_f32_16x16x32_bf16 v[140:143], a[192:195], a[236:239], v[140:143]\n"
    "ds_read_b64_tr_b16 a[130:131], v5 offset:33792\n"
    "v_mfma_f32_16x16x32_bf16 v[144:147], a[192:195], a[240:243], v[144:147]\n"
    "v_mfma_f32_16x16x32_bf16 v[148:151], a[192:195], a[244:247], v[148:151]\n"
    "ds_read_b128 a[160:163], v4 offset:0\n"
    "v_mfma_f32_16x16x32_bf16 v[152:155], a[192:195], a[248:251], v[152:155]\n"
    "v_mfma_f32_16x16x32_bf16 v[156:159], a[192:195], a[252:255], v[156:159]\n"
    "ds_read_b64_tr_b16 a[132:133], v6 offset:32768\n"
    "v_mfma_f32_16x16x32_bf16 v[188:191], a[196:199], a[252:255], v[188:191]\n"
    "buffer_load_dwordx4 v14, s[12:15], s25 offen offset:1024 lds\n"
    "v_mfma_f32_16x16x32_bf16 v[184:187], a[196:199], a[248:251], v[184:187]\n"
    "ds_read_b64_tr_b16 a[134:135], v6 offset:33792\n"
    "v_mfma_f32_16x16x32_bf16 v[180:183], a[196:199], a[244:247], v[180:183]\n"
    "v_mfma_f32_16x16x32_bf16 v[176:179], a[196:199], a[240:243], v[176:179]\n"
    "ds_read_b128 a[164:167], v4 offset:2048\n"
    "v_mfma_f32_16x16x32_bf16 v[172:175], a[196:199], a[236:239], v[172:175]\n"
    "v_mfma_f32_16x16x32_bf16 v[168:171], a[196:199], a[232:235], v[168:171]\n"
    "ds_read_b64_tr_b16 a[136:137], v7 offset:32768\n"
    "v_mfma_f32_16x16x32_bf16 v[164:167], a[196:199], a[228:231], v[164:167]\n"
    "buffer_load_dwordx4 v13, s[16:19], s25 offen offset:2048 lds\n"
    "v_mfma_f32_16x16x32_bf16 v[160:163], a[196:199], a[224:227], v[160:163]\n"
    "ds_read_b64_tr_b16 a[138:139], v7 offset:33792\n"
    "v_mfma_f32_16x16x32_bf16 v[192:195], a[200:203], a[224:227], v[192:195]\n"
    "v_mfma_f32_16x16x32_bf16 v[196:199], a[200:203], a[228:231], v[196:199]\n"
    "ds_read_b128 a[168:171], v4 offset:4096\n"
    "v_mfma_f32_16x16x32_bf16 v[200:203], a[200:203], a[232:235], v[200:203]\n"
    "v_mfma_f32_16x16x32_bf16 v[204:207], a[200:203], a[236:239], v[204:207]\n"
    "ds_read_b64_tr_b16 a[140:141], v8 offset:32768\n"
    "v_mfma_f32_16x16x32_bf16 v[208:211], a[200:203], a[240:243], v[208:211]\n"
    "buffer_load_dwordx4 v14, s[16:19], s25 offen offset:3072 lds\n"
    "v_mfma_f32_16x16x32_bf16 v[212:215], a[200:203], a[244:247], v[212:215]\n"
    "ds_read_b64_tr_b16 a[142:143], v8 offset:33792\n"
    "s_add_u32 s25, s25, 229376\n"
    "v_mfma_f32_16x16x32_bf16 v[216:219], a[200:203], a[248:251], v[216:219]\n"
    "v_mfma_f32_16x16x32_bf16 v[220:223], a[200:203], a[252:255], v[220:223]\n"
    "ds_read_b128 a[172:175], v4 offset:6144\n"
    "v_mfma_f32_16x16x32_bf16 v[252:255], a[204:207], a[252:255], v[252:255]\n"
    "s_add_u32 m0, s26, 0\n"
    "v_mfma_f32_16x16x32_bf16 v[248:251], a[204:207], a[248:251], v[248:251]\n"
    "ds_read_b64_tr_b16 a[144:145], v5 offset:34816\n"
    "v_mfma_f32_16x16x32_bf16 v[244:247], a[204:207], a[244:247], v[244:247]\n"
    "buffer_load_dwordx4 v9, s[8:11], s24 offen offset:0 lds\n"
    "v_mfma_f32_16x16x32_bf16 v[240:243], a[204:207], a[240:243], v[240:243]\n"
    "ds_read_b64_tr_b16 a[146:147], v5 offset:35840\n"
    "v_mfma_f32_16x16x32_bf16 v[236:239], a[204:207], a[236:239], v[236:239]\n"
    "v_mfma_f32_16x16x32_bf16 v[232:235], a[204:207], a[232:235], v[232:235]\n"
    "ds_read_b128 a[176:179], v4 offset:8192\n"
    "v_mfma_f32_16x16x32_bf16 v[228:231], a[204:207], a[228:231], v[228:231]\n"
    "v_mfma_f32_16x16x32_bf16 v[224:227], a[204:207], a[224:227], v[224:227]\n"
    "ds_read_b64_tr_b16 a[148:149], v6 offset:34816\n"
    "v_mfma_f32_16x16x32_bf16 v[32:35], a[208:211], a[224:227], v[32:35]\n"
    "buffer_load_dwordx4 v10, s[8:11], s24 offen offset:1024 lds\n"
    "v_mfma_f32_16x16x32_bf16 v[36:39], a[208:211], a[228:231], v[36:39]\n"
    "ds_read_b64_tr_b16 a[150:151], v6 offset:35840\n"
    "v_mfma_f32_16x16x32_bf16 v[40:43], a[208:211], a[232:235], v[40:43]\n"
    "v_mfma_f32_16x16x32_bf16 v[44:47], a[208:211], a[236:239], v[44:47]\n"
    "ds_read_b128 a[180:183], v4 offset:10240\n"
    "v_mfma_f32_16x16x32_bf16 v[48:51], a[208:211], a[240:243], v[48:51]\n"
    "v_mfma_f32_16x16x32_bf16 v[52:55], a[208:211], a[244:247], v[52:55]\n"
    "ds_read_b64_tr_b16 a[152:153], v7 offset:34816\n"
    "v_mfma_f32_16x16x32_bf16 v[56:59], a[208:211], a[248:251], v[56:59]\n"
    "buffer_load_dwordx4 v11, s[8:11], s24 offen offset:2048 lds\n"
    "v_mfma_f32_16x16x32_bf16 v[60:63], a[208:211], a[252:255], v[60:63]\n"
    "ds_read_b64_tr_b16 a[154:155], v7 offset:35840\n"
    "v_mfma_f32_16x16x32_bf16 v[92:95], a[212:215], a[252:255], v[92:95]\n"
    "v_mfma_f32_16x16x32_bf16 v[88:91], a[212:215], a[248:251], v[88:91]\n"
    "ds_read_b128 a[184:187], v4 offset:12288\n"
    "v_mfma_f32_16x16x32_bf16 v[84:87], a[212:215], a[244:247], v[84:87]\n"
    "v_mfma_f32_16x16x32_bf16 v[80:83], a[212:215], a[240:243], v[80:83]\n"
    "ds_read_b64_tr_b16 a[156:157], v8 offset:34816\n"
    "v_mfma_f32_16x16x32_bf16 v[76:79], a[212:215], a[236:239], v[76:79]\n"
    "buffer_load_dwordx4 v12, s[8:11], s24 offen offset:3072 lds\n"
    "v_mfma_f32_16x16x32_bf16 v[72:75], a[212:215], a[232:235], v[72:75]\n"
    "ds_read_b64_tr_b16 a[158:159], v8 offset:35840\n"
    "s_add_u32 s24, s24, 65536\n"
    "v_mfma_f32_16x16x32_bf16 v[68:71], a[212:215], a[228:231], v[68:71]\n"
    "v_mfma_f32_16x16x32_bf16 v[64:67], a[212:215], a[224:227], v[64:67]\n"
    "ds_read_b128 a[188:191], v4 offset:14336\n"
    "v_mfma_f32_16x16x32_bf16 v[96:99], a[216:219], a[224:227], v[96:99]\n"
    "v_mfma_f32_16x16x32_bf16 v[100:103], a[216:219], a[228:231], v[100:103]\n"
    "v_mfma_f32_16x16x32_bf16 v[104:107], a[216:219], a[232:235], v[104:107]\n"
    "v_mfma_f32_16x16x32_bf16 v[108:111], a[216:219], a[236:239], v[108:111]\n"
    "v_mfma_f32_16x16x32_bf16 v[112:115], a[216:219], a[240:243], v[112:115]\n"
    "v_mfma_f32_16x16x32_bf16 v[116:119], a[216:219], a[244:247], v[116:119]\n"
    "v_mfma_f32_16x16x32_bf16 v[120:123], a[216:219], a[248:251], v[120:123]\n"
    "v_mfma_f32_16x16x32_bf16 v[124:127], a[216:219], a[252:255], v[124:127]\n"
    "v_mfma_f32_16x16x32_bf16 a[28:31], a[220:223], a[252:255], a[28:31]\n"
    "v_mfma_f32_16x16x32_bf16 a[24:27], a[220:223], a[248:251], a[24:27]\n"
    "v_mfma_f32_16x16x32_bf16 a[20:23], a[220:223], a[244:247], a[20:23]\n"
    "v_mfma_f32_16x16x32_bf16 a[16:19], a[220:223], a[240:243], a[16:19]\n"
    "s_cmp_lg_u32 s29, 2\n"
    "s_cbranch_scc1 NOCP_gated_mix__ssm_conv_h3__swiglu_ffn__prompt_kernel\n"
    "buffer_load_dword a50, v15, s[92:95], 0 offen\n"
    "buffer_load_dword a50, v28, s[80:83], s73 offen\n"
    "NOCP_gated_mix__ssm_conv_h3__swiglu_ffn__prompt_kernel:\n"
    "v_mfma_f32_16x16x32_bf16 a[12:15], a[220:223], a[236:239], a[12:15]\n"
    "v_mfma_f32_16x16x32_bf16 a[8:11], a[220:223], a[232:235], a[8:11]\n"
    "v_mfma_f32_16x16x32_bf16 a[4:7], a[220:223], a[228:231], a[4:7]\n"
    "v_mfma_f32_16x16x32_bf16 a[0:3], a[220:223], a[224:227], a[0:3]\n"
    "s_waitcnt vmcnt(16) lgkmcnt(0)\n"
    "s_barrier\n"
    "v_mfma_f32_16x16x32_bf16 v[128:131], a[128:131], a[160:163], v[128:131]\n"
    "v_add_u32 v4, s27, v3\n"
    "s_add_u32 m0, s28, 32768\n"
    "v_mfma_f32_16x16x32_bf16 v[132:135], a[128:131], a[164:167], v[132:135]\n"
    "ds_read_b64_tr_b16 a[192:193], v5 offset:49152\n"
    "v_mfma_f32_16x16x32_bf16 v[136:139], a[128:131], a[168:171], v[136:139]\n"
    "buffer_load_dwordx4 v13, s[12:15], s25 offen lds\n"
    "v_mfma_f32_16x16x32_bf16 v[140:143], a[128:131], a[172:175], v[140:143]\n"
    "ds_read_b64_tr_b16 a[194:195], v5 offset:50176\n"
    "v_mfma_f32_16x16x32_bf16 v[144:147], a[128:131], a[176:179], v[144:147]\n"
    "v_mfma_f32_16x16x32_bf16 v[148:151], a[128:131], a[180:183], v[148:151]\n"
    "ds_read_b128 a[224:227], v4 offset:0\n"
    "v_mfma_f32_16x16x32_bf16 v[152:155], a[128:131], a[184:187], v[152:155]\n"
    "v_mfma_f32_16x16x32_bf16 v[156:159], a[128:131], a[188:191], v[156:159]\n"
    "ds_read_b64_tr_b16 a[196:197], v6 offset:49152\n"
    "v_mfma_f32_16x16x32_bf16 v[188:191], a[132:135], a[188:191], v[188:191]\n"
    "buffer_load_dwordx4 v14, s[12:15], s25 offen offset:1024 lds\n"
    "v_mfma_f32_16x16x32_bf16 v[184:187], a[132:135], a[184:187], v[184:187]\n"
    "ds_read_b64_tr_b16 a[198:199], v6 offset:50176\n"
    "v_mfma_f32_16x16x32_bf16 v[180:183], a[132:135], a[180:183], v[180:183]\n"
    "v_mfma_f32_16x16x32_bf16 v[176:179], a[132:135], a[176:179], v[176:179]\n"
    "ds_read_b128 a[228:231], v4 offset:2048\n"
    "v_mfma_f32_16x16x32_bf16 v[172:175], a[132:135], a[172:175], v[172:175]\n"
    "v_mfma_f32_16x16x32_bf16 v[168:171], a[132:135], a[168:171], v[168:171]\n"
    "ds_read_b64_tr_b16 a[200:201], v7 offset:49152\n"
    "v_mfma_f32_16x16x32_bf16 v[164:167], a[132:135], a[164:167], v[164:167]\n"
    "buffer_load_dwordx4 v13, s[16:19], s25 offen offset:2048 lds\n"
    "v_mfma_f32_16x16x32_bf16 v[160:163], a[132:135], a[160:163], v[160:163]\n"
    "ds_read_b64_tr_b16 a[202:203], v7 offset:50176\n"
    "v_mfma_f32_16x16x32_bf16 v[192:195], a[136:139], a[160:163], v[192:195]\n"
    "v_mfma_f32_16x16x32_bf16 v[196:199], a[136:139], a[164:167], v[196:199]\n"
    "ds_read_b128 a[232:235], v4 offset:4096\n"
    "v_mfma_f32_16x16x32_bf16 v[200:203], a[136:139], a[168:171], v[200:203]\n"
    "v_mfma_f32_16x16x32_bf16 v[204:207], a[136:139], a[172:175], v[204:207]\n"
    "ds_read_b64_tr_b16 a[204:205], v8 offset:49152\n"
    "v_mfma_f32_16x16x32_bf16 v[208:211], a[136:139], a[176:179], v[208:211]\n"
    "buffer_load_dwordx4 v14, s[16:19], s25 offen offset:3072 lds\n"
    "v_mfma_f32_16x16x32_bf16 v[212:215], a[136:139], a[180:183], v[212:215]\n"
    "ds_read_b64_tr_b16 a[206:207], v8 offset:50176\n"
    "s_add_u32 s25, s25, 229376\n"
    "v_mfma_f32_16x16x32_bf16 v[216:219], a[136:139], a[184:187], v[216:219]\n"
    "v_mfma_f32_16x16x32_bf16 v[220:223], a[136:139], a[188:191], v[220:223]\n"
    "ds_read_b128 a[236:239], v4 offset:6144\n"
    "v_mfma_f32_16x16x32_bf16 v[252:255], a[140:143], a[188:191], v[252:255]\n"
    "s_add_u32 m0, s26, 4096\n"
    "v_mfma_f32_16x16x32_bf16 v[248:251], a[140:143], a[184:187], v[248:251]\n"
    "ds_read_b64_tr_b16 a[208:209], v5 offset:51200\n"
    "v_mfma_f32_16x16x32_bf16 v[244:247], a[140:143], a[180:183], v[244:247]\n"
    "buffer_load_dwordx4 v9, s[8:11], s24 offen offset:0 lds\n"
    "v_mfma_f32_16x16x32_bf16 v[240:243], a[140:143], a[176:179], v[240:243]\n"
    "ds_read_b64_tr_b16 a[210:211], v5 offset:52224\n"
    "v_mfma_f32_16x16x32_bf16 v[236:239], a[140:143], a[172:175], v[236:239]\n"
    "v_mfma_f32_16x16x32_bf16 v[232:235], a[140:143], a[168:171], v[232:235]\n"
    "ds_read_b128 a[240:243], v4 offset:8192\n"
    "v_mfma_f32_16x16x32_bf16 v[228:231], a[140:143], a[164:167], v[228:231]\n"
    "v_mfma_f32_16x16x32_bf16 v[224:227], a[140:143], a[160:163], v[224:227]\n"
    "ds_read_b64_tr_b16 a[212:213], v6 offset:51200\n"
    "v_mfma_f32_16x16x32_bf16 v[32:35], a[144:147], a[160:163], v[32:35]\n"
    "buffer_load_dwordx4 v10, s[8:11], s24 offen offset:1024 lds\n"
    "v_mfma_f32_16x16x32_bf16 v[36:39], a[144:147], a[164:167], v[36:39]\n"
    "ds_read_b64_tr_b16 a[214:215], v6 offset:52224\n"
    "v_mfma_f32_16x16x32_bf16 v[40:43], a[144:147], a[168:171], v[40:43]\n"
    "v_mfma_f32_16x16x32_bf16 v[44:47], a[144:147], a[172:175], v[44:47]\n"
    "ds_read_b128 a[244:247], v4 offset:10240\n"
    "v_mfma_f32_16x16x32_bf16 v[48:51], a[144:147], a[176:179], v[48:51]\n"
    "v_mfma_f32_16x16x32_bf16 v[52:55], a[144:147], a[180:183], v[52:55]\n"
    "ds_read_b64_tr_b16 a[216:217], v7 offset:51200\n"
    "v_mfma_f32_16x16x32_bf16 v[56:59], a[144:147], a[184:187], v[56:59]\n"
    "buffer_load_dwordx4 v11, s[8:11], s24 offen offset:2048 lds\n"
    "v_mfma_f32_16x16x32_bf16 v[60:63], a[144:147], a[188:191], v[60:63]\n"
    "ds_read_b64_tr_b16 a[218:219], v7 offset:52224\n"
    "v_mfma_f32_16x16x32_bf16 v[92:95], a[148:151], a[188:191], v[92:95]\n"
    "v_mfma_f32_16x16x32_bf16 v[88:91], a[148:151], a[184:187], v[88:91]\n"
    "ds_read_b128 a[248:251], v4 offset:12288\n"
    "v_mfma_f32_16x16x32_bf16 v[84:87], a[148:151], a[180:183], v[84:87]\n"
    "v_mfma_f32_16x16x32_bf16 v[80:83], a[148:151], a[176:179], v[80:83]\n"
    "ds_read_b64_tr_b16 a[220:221], v8 offset:51200\n"
    "v_mfma_f32_16x16x32_bf16 v[76:79], a[148:151], a[172:175], v[76:79]\n"
    "buffer_load_dwordx4 v12, s[8:11], s24 offen offset:3072 lds\n"
    "v_mfma_f32_16x16x32_bf16 v[72:75], a[148:151], a[168:171], v[72:75]\n"
    "ds_read_b64_tr_b16 a[222:223], v8 offset:52224\n"
    "s_sub_u32 s24, s24, 65408\n"
    "v_mfma_f32_16x16x32_bf16 v[68:71], a[148:151], a[164:167], v[68:71]\n"
    "s_add_u32 s26, s26, 32768\n"
    "v_mfma_f32_16x16x32_bf16 v[64:67], a[148:151], a[160:163], v[64:67]\n"
    "ds_read_b128 a[252:255], v4 offset:14336\n"
    "s_cmp_ge_u32 s26, 98304\n"
    "v_mfma_f32_16x16x32_bf16 v[96:99], a[152:155], a[160:163], v[96:99]\n"
    "s_cselect_b32 s30, 98304, 0\n"
    "v_mfma_f32_16x16x32_bf16 v[100:103], a[152:155], a[164:167], v[100:103]\n"
    "s_sub_u32 s26, s26, s30\n"
    "v_mfma_f32_16x16x32_bf16 v[104:107], a[152:155], a[168:171], v[104:107]\n"
    "v_mfma_f32_16x16x32_bf16 v[108:111], a[152:155], a[172:175], v[108:111]\n"
    "v_mfma_f32_16x16x32_bf16 v[112:115], a[152:155], a[176:179], v[112:115]\n"
    "v_mfma_f32_16x16x32_bf16 v[116:119], a[152:155], a[180:183], v[116:119]\n"
    "v_mfma_f32_16x16x32_bf16 v[120:123], a[152:155], a[184:187], v[120:123]\n"
    "s_add_u32 s27, s27, 32768\n"
    "v_mfma_f32_16x16x32_bf16 v[124:127], a[152:155], a[188:191], v[124:127]\n"
    "s_cmp_ge_u32 s27, 98304\n"
    "v_mfma_f32_16x16x32_bf16 a[28:31], a[156:159], a[188:191], a[28:31]\n"
    "s_cselect_b32 s31, 98304, 0\n"
    "v_mfma_f32_16x16x32_bf16 a[24:27], a[156:159], a[184:187], a[24:27]\n"
    "s_sub_u32 s27, s27, s31\n"
    "v_mfma_f32_16x16x32_bf16 a[20:23], a[156:159], a[180:183], a[20:23]\n"
    "v_mfma_f32_16x16x32_bf16 a[16:19], a[156:159], a[176:179], a[16:19]\n"
    "v_mfma_f32_16x16x32_bf16 a[12:15], a[156:159], a[172:175], a[12:15]\n"
    "v_mfma_f32_16x16x32_bf16 a[8:11], a[156:159], a[168:171], a[8:11]\n"
    "v_mfma_f32_16x16x32_bf16 a[4:7], a[156:159], a[164:167], a[4:7]\n"
    "v_mfma_f32_16x16x32_bf16 a[0:3], a[156:159], a[160:163], a[0:3]\n"
    "s_waitcnt vmcnt(16) lgkmcnt(0)\n"
    "s_barrier\n"
    "v_mfma_f32_16x16x32_bf16 v[128:131], a[192:195], a[224:227], v[128:131]\n"
    "v_add_u32 v4, s27, v2\n"
    "s_add_u32 m0, s28, 49152\n"
    "v_mfma_f32_16x16x32_bf16 v[132:135], a[192:195], a[228:231], v[132:135]\n"
    "ds_read_b64_tr_b16 a[128:129], v5 offset:0\n"
    "v_mfma_f32_16x16x32_bf16 v[136:139], a[192:195], a[232:235], v[136:139]\n"
    "buffer_load_dwordx4 v13, s[12:15], s25 offen lds\n"
    "v_mfma_f32_16x16x32_bf16 v[140:143], a[192:195], a[236:239], v[140:143]\n"
    "ds_read_b64_tr_b16 a[130:131], v5 offset:1024\n"
    "v_mfma_f32_16x16x32_bf16 v[144:147], a[192:195], a[240:243], v[144:147]\n"
    "v_mfma_f32_16x16x32_bf16 v[148:151], a[192:195], a[244:247], v[148:151]\n"
    "ds_read_b128 a[160:163], v4 offset:0\n"
    "v_mfma_f32_16x16x32_bf16 v[152:155], a[192:195], a[248:251], v[152:155]\n"
    "v_mfma_f32_16x16x32_bf16 v[156:159], a[192:195], a[252:255], v[156:159]\n"
    "ds_read_b64_tr_b16 a[132:133], v6 offset:0\n"
    "v_mfma_f32_16x16x32_bf16 v[188:191], a[196:199], a[252:255], v[188:191]\n"
    "buffer_load_dwordx4 v14, s[12:15], s25 offen offset:1024 lds\n"
    "v_mfma_f32_16x16x32_bf16 v[184:187], a[196:199], a[248:251], v[184:187]\n"
    "ds_read_b64_tr_b16 a[134:135], v6 offset:1024\n"
    "v_mfma_f32_16x16x32_bf16 v[180:183], a[196:199], a[244:247], v[180:183]\n"
    "v_mfma_f32_16x16x32_bf16 v[176:179], a[196:199], a[240:243], v[176:179]\n"
    "ds_read_b128 a[164:167], v4 offset:2048\n"
    "v_mfma_f32_16x16x32_bf16 v[172:175], a[196:199], a[236:239], v[172:175]\n"
    "v_mfma_f32_16x16x32_bf16 v[168:171], a[196:199], a[232:235], v[168:171]\n"
    "ds_read_b64_tr_b16 a[136:137], v7 offset:0\n"
    "v_mfma_f32_16x16x32_bf16 v[164:167], a[196:199], a[228:231], v[164:167]\n"
    "buffer_load_dwordx4 v13, s[16:19], s25 offen offset:2048 lds\n"
    "v_mfma_f32_16x16x32_bf16 v[160:163], a[196:199], a[224:227], v[160:163]\n"
    "ds_read_b64_tr_b16 a[138:139], v7 offset:1024\n"
    "v_mfma_f32_16x16x32_bf16 v[192:195], a[200:203], a[224:227], v[192:195]\n"
    "v_mfma_f32_16x16x32_bf16 v[196:199], a[200:203], a[228:231], v[196:199]\n"
    "ds_read_b128 a[168:171], v4 offset:4096\n"
    "v_mfma_f32_16x16x32_bf16 v[200:203], a[200:203], a[232:235], v[200:203]\n"
    "v_mfma_f32_16x16x32_bf16 v[204:207], a[200:203], a[236:239], v[204:207]\n"
    "ds_read_b64_tr_b16 a[140:141], v8 offset:0\n"
    "v_mfma_f32_16x16x32_bf16 v[208:211], a[200:203], a[240:243], v[208:211]\n"
    "buffer_load_dwordx4 v14, s[16:19], s25 offen offset:3072 lds\n"
    "v_mfma_f32_16x16x32_bf16 v[212:215], a[200:203], a[244:247], v[212:215]\n"
    "ds_read_b64_tr_b16 a[142:143], v8 offset:1024\n"
    "s_add_u32 s25, s25, 229376\n"
    "v_mfma_f32_16x16x32_bf16 v[216:219], a[200:203], a[248:251], v[216:219]\n"
    "v_mfma_f32_16x16x32_bf16 v[220:223], a[200:203], a[252:255], v[220:223]\n"
    "ds_read_b128 a[172:175], v4 offset:6144\n"
    "s_cmp_eq_u32 s29, 1\n"
    "s_cselect_b32 s24, 2146435072, s24\n"
    "v_mfma_f32_16x16x32_bf16 v[252:255], a[204:207], a[252:255], v[252:255]\n"
    "s_add_u32 m0, s26, 0\n"
    "v_mfma_f32_16x16x32_bf16 v[248:251], a[204:207], a[248:251], v[248:251]\n"
    "ds_read_b64_tr_b16 a[144:145], v5 offset:2048\n"
    "v_mfma_f32_16x16x32_bf16 v[244:247], a[204:207], a[244:247], v[244:247]\n"
    "buffer_load_dwordx4 v9, s[8:11], s24 offen offset:0 lds\n"
    "v_mfma_f32_16x16x32_bf16 v[240:243], a[204:207], a[240:243], v[240:243]\n"
    "ds_read_b64_tr_b16 a[146:147], v5 offset:3072\n"
    "v_mfma_f32_16x16x32_bf16 v[236:239], a[204:207], a[236:239], v[236:239]\n"
    "v_mfma_f32_16x16x32_bf16 v[232:235], a[204:207], a[232:235], v[232:235]\n"
    "ds_read_b128 a[176:179], v4 offset:8192\n"
    "v_mfma_f32_16x16x32_bf16 v[228:231], a[204:207], a[228:231], v[228:231]\n"
    "v_mfma_f32_16x16x32_bf16 v[224:227], a[204:207], a[224:227], v[224:227]\n"
    "ds_read_b64_tr_b16 a[148:149], v6 offset:2048\n"
    "v_mfma_f32_16x16x32_bf16 v[32:35], a[208:211], a[224:227], v[32:35]\n"
    "buffer_load_dwordx4 v10, s[8:11], s24 offen offset:1024 lds\n"
    "v_mfma_f32_16x16x32_bf16 v[36:39], a[208:211], a[228:231], v[36:39]\n"
    "ds_read_b64_tr_b16 a[150:151], v6 offset:3072\n"
    "v_mfma_f32_16x16x32_bf16 v[40:43], a[208:211], a[232:235], v[40:43]\n"
    "v_mfma_f32_16x16x32_bf16 v[44:47], a[208:211], a[236:239], v[44:47]\n"
    "ds_read_b128 a[180:183], v4 offset:10240\n"
    "v_mfma_f32_16x16x32_bf16 v[48:51], a[208:211], a[240:243], v[48:51]\n"
    "v_mfma_f32_16x16x32_bf16 v[52:55], a[208:211], a[244:247], v[52:55]\n"
    "ds_read_b64_tr_b16 a[152:153], v7 offset:2048\n"
    "v_mfma_f32_16x16x32_bf16 v[56:59], a[208:211], a[248:251], v[56:59]\n"
    "buffer_load_dwordx4 v11, s[8:11], s24 offen offset:2048 lds\n"
    "v_mfma_f32_16x16x32_bf16 v[60:63], a[208:211], a[252:255], v[60:63]\n"
    "ds_read_b64_tr_b16 a[154:155], v7 offset:3072\n"
    "v_mfma_f32_16x16x32_bf16 v[92:95], a[212:215], a[252:255], v[92:95]\n"
    "v_mfma_f32_16x16x32_bf16 v[88:91], a[212:215], a[248:251], v[88:91]\n"
    "ds_read_b128 a[184:187], v4 offset:12288\n"
    "v_mfma_f32_16x16x32_bf16 v[84:87], a[212:215], a[244:247], v[84:87]\n"
    "v_mfma_f32_16x16x32_bf16 v[80:83], a[212:215], a[240:243], v[80:83]\n"
    "ds_read_b64_tr_b16 a[156:157], v8 offset:2048\n"
    "v_mfma_f32_16x16x32_bf16 v[76:79], a[212:215], a[236:239], v[76:79]\n"
    "buffer_load_dwordx4 v12, s[8:11], s24 offen offset:3072 lds\n"
    "v_mfma_f32_16x16x32_bf16 v[72:75], a[212:215], a[232:235], v[72:75]\n"
    "ds_read_b64_tr_b16 a[158:159], v8 offset:3072\n"
    "s_add_u32 s24, s24, 65536\n"
    "v_mfma_f32_16x16x32_bf16 v[68:71], a[212:215], a[228:231], v[68:71]\n"
    "v_mfma_f32_16x16x32_bf16 v[64:67], a[212:215], a[224:227], v[64:67]\n"
    "ds_read_b128 a[188:191], v4 offset:14336\n"
    "v_mfma_f32_16x16x32_bf16 v[96:99], a[216:219], a[224:227], v[96:99]\n"
    "v_mfma_f32_16x16x32_bf16 v[100:103], a[216:219], a[228:231], v[100:103]\n"
    "v_mfma_f32_16x16x32_bf16 v[104:107], a[216:219], a[232:235], v[104:107]\n"
    "v_mfma_f32_16x16x32_bf16 v[108:111], a[216:219], a[236:239], v[108:111]\n"
    "v_mfma_f32_16x16x32_bf16 v[112:115], a[216:219], a[240:243], v[112:115]\n"
    "v_mfma_f32_16x16x32_bf16 v[116:119], a[216:219], a[244:247], v[116:119]\n"
    "v_mfma_f32_16x16x32_bf16 v[120:123], a[216:219], a[248:251], v[120:123]\n"
    "v_mfma_f32_16x16x32_bf16 v[124:127], a[216:219], a[252:255], v[124:127]\n"
    "v_mfma_f32_16x16x32_bf16 a[28:31], a[220:223], a[252:255], a[28:31]\n"
    "v_mfma_f32_16x16x32_bf16 a[24:27], a[220:223], a[248:251], a[24:27]\n"
    "v_mfma_f32_16x16x32_bf16 a[20:23], a[220:223], a[244:247], a[20:23]\n"
    "v_mfma_f32_16x16x32_bf16 a[16:19], a[220:223], a[240:243], a[16:19]\n"
    "v_mfma_f32_16x16x32_bf16 a[12:15], a[220:223], a[236:239], a[12:15]\n"
    "v_mfma_f32_16x16x32_bf16 a[8:11], a[220:223], a[232:235], a[8:11]\n"
    "v_mfma_f32_16x16x32_bf16 a[4:7], a[220:223], a[228:231], a[4:7]\n"
    "v_mfma_f32_16x16x32_bf16 a[0:3], a[220:223], a[224:227], a[0:3]\n"
    "s_waitcnt vmcnt(16) lgkmcnt(0)\n"
    "s_barrier\n"
    "s_sub_u32 s29, s29, 1\n"
    "s_cmp_lg_u32 s29, 0\n"
    "s_cbranch_scc1 LOOP1_gated_mix__ssm_conv_h3__swiglu_ffn__prompt_kernel\n"
    "v_mfma_f32_16x16x32_bf16 v[128:131], a[128:131], a[160:163], v[128:131]\n"
    "v_add_u32 v4, s27, v3\n"
    "v_mfma_f32_16x16x32_bf16 v[132:135], a[128:131], a[164:167], v[132:135]\n"
    "ds_read_b64_tr_b16 a[192:193], v5 offset:16384\n"
    "v_and_b32 v16, 31, v1\n"
    "v_mfma_f32_16x16x32_bf16 v[136:139], a[128:131], a[168:171], v[136:139]\n"
    "v_lshlrev_b32 v16, 7, v16\n"
    "v_mfma_f32_16x16x32_bf16 v[140:143], a[128:131], a[172:175], v[140:143]\n"
    "ds_read_b64_tr_b16 a[194:195], v5 offset:17408\n"
    "v_add_u32 v16, 40964096, v16\n"
    "v_mfma_f32_16x16x32_bf16 v[144:147], a[128:131], a[176:179], v[144:147]\n"
    "buffer_load_dwordx2 v[18:19], v16, s[20:23], 0 offen sc1\n"
    "v_mfma_f32_16x16x32_bf16 v[148:151], a[128:131], a[180:183], v[148:151]\n"
    "ds_read_b128 a[224:227], v4 offset:0\n"
    "buffer_load_dword a50, v27, s[76:79], s72 offen\n"
    "v_mfma_f32_16x16x32_bf16 v[152:155], a[128:131], a[184:187], v[152:155]\n"
    "s_lshr_b32 s68, s34, 1\n"
    "v_mfma_f32_16x16x32_bf16 v[156:159], a[128:131], a[188:191], v[156:159]\n"
    "ds_read_b64_tr_b16 a[196:197], v6 offset:16384\n"
    "s_lshl_b32 s68, s68, 3\n"
    "v_mfma_f32_16x16x32_bf16 v[188:191], a[132:135], a[188:191], v[188:191]\n"
    "s_lshl_b32 s69, s35, 4\n"
    "v_mfma_f32_16x16x32_bf16 v[184:187], a[132:135], a[184:187], v[184:187]\n"
    "ds_read_b64_tr_b16 a[198:199], v6 offset:17408\n"
    "s_add_u32 s68, s68, s69\n"
    "v_mfma_f32_16x16x32_bf16 v[180:183], a[132:135], a[180:183], v[180:183]\n"
    "s_mul_i32 s96, s68, 112\n"
    "v_mfma_f32_16x16x32_bf16 v[176:179], a[132:135], a[176:179], v[176:179]\n"
    "ds_read_b128 a[228:231], v4 offset:2048\n"
    "s_and_b32 s68, s34, 1\n"
    "v_mfma_f32_16x16x32_bf16 v[172:175], a[132:135], a[172:175], v[172:175]\n"
    "s_lshl_b32 s68, s68, 1\n"
    "v_mfma_f32_16x16x32_bf16 v[168:171], a[132:135], a[168:171], v[168:171]\n"
    "ds_read_b64_tr_b16 a[200:201], v7 offset:16384\n"
    "s_lshl_b32 s69, s36, 2\n"
    "v_mfma_f32_16x16x32_bf16 v[164:167], a[132:135], a[164:167], v[164:167]\n"
    "s_add_u32 s68, s68, s69\n"
    "v_mfma_f32_16x16x32_bf16 v[160:163], a[132:135], a[160:163], v[160:163]\n"
    "ds_read_b64_tr_b16 a[202:203], v7 offset:17408\n"
    "s_add_u32 s96, s96, s68\n"
    "v_mfma_f32_16x16x32_bf16 v[192:195], a[136:139], a[160:163], v[192:195]\n"
    "s_lshl_b32 s96, s96, 10\n"
    "v_mfma_f32_16x16x32_bf16 v[196:199], a[136:139], a[164:167], v[196:199]\n"
    "ds_read_b128 a[232:235], v4 offset:4096\n"
    "s_add_u32 s96, s96, 24117248\n"
    "v_mfma_f32_16x16x32_bf16 v[200:203], a[136:139], a[168:171], v[200:203]\n"
    "s_mul_i32 s74, s35, 28\n"
    "v_mfma_f32_16x16x32_bf16 v[204:207], a[136:139], a[172:175], v[204:207]\n"
    "ds_read_b64_tr_b16 a[204:205], v8 offset:16384\n"
    "s_add_u32 s74, s74, s36\n"
    "v_mfma_f32_16x16x32_bf16 v[208:211], a[136:139], a[176:179], v[208:211]\n"
    "s_lshl_b32 s74, s74, 7\n"
    "v_mfma_f32_16x16x32_bf16 v[212:215], a[136:139], a[180:183], v[212:215]\n"
    "ds_read_b64_tr_b16 a[206:207], v8 offset:17408\n"
    "s_add_u32 s74, s74, 40968192\n"
    "v_mfma_f32_16x16x32_bf16 v[216:219], a[136:139], a[184:187], v[216:219]\n"
    "v_mfma_f32_16x16x32_bf16 v[220:223], a[136:139], a[188:191], v[220:223]\n"
    "ds_read_b128 a[236:239], v4 offset:6144\n"
    "v_mfma_f32_16x16x32_bf16 v[252:255], a[140:143], a[188:191], v[252:255]\n"
    "v_mfma_f32_16x16x32_bf16 v[248:251], a[140:143], a[184:187], v[248:251]\n"
    "ds_read_b64_tr_b16 a[208:209], v5 offset:18432\n"
    "v_mfma_f32_16x16x32_bf16 v[244:247], a[140:143], a[180:183], v[244:247]\n"
    "v_mfma_f32_16x16x32_bf16 v[240:243], a[140:143], a[176:179], v[240:243]\n"
    "ds_read_b64_tr_b16 a[210:211], v5 offset:19456\n"
    "v_mfma_f32_16x16x32_bf16 v[236:239], a[140:143], a[172:175], v[236:239]\n"
    "v_mfma_f32_16x16x32_bf16 v[232:235], a[140:143], a[168:171], v[232:235]\n"
    "ds_read_b128 a[240:243], v4 offset:8192\n"
    "v_mfma_f32_16x16x32_bf16 v[228:231], a[140:143], a[164:167], v[228:231]\n"
    "v_mfma_f32_16x16x32_bf16 v[224:227], a[140:143], a[160:163], v[224:227]\n"
    "ds_read_b64_tr_b16 a[212:213], v6 offset:18432\n"
    "v_mfma_f32_16x16x32_bf16 v[32:35], a[144:147], a[160:163], v[32:35]\n"
    "v_mfma_f32_16x16x32_bf16 v[36:39], a[144:147], a[164:167], v[36:39]\n"
    "ds_read_b64_tr_b16 a[214:215], v6 offset:19456\n"
    "v_mfma_f32_16x16x32_bf16 v[40:43], a[144:147], a[168:171], v[40:43]\n"
    "v_mfma_f32_16x16x32_bf16 v[44:47], a[144:147], a[172:175], v[44:47]\n"
    "ds_read_b128 a[244:247], v4 offset:10240\n"
    "v_mfma_f32_16x16x32_bf16 v[48:51], a[144:147], a[176:179], v[48:51]\n"
    "v_mfma_f32_16x16x32_bf16 v[52:55], a[144:147], a[180:183], v[52:55]\n"
    "ds_read_b64_tr_b16 a[216:217], v7 offset:18432\n"
    "v_mfma_f32_16x16x32_bf16 v[56:59], a[144:147], a[184:187], v[56:59]\n"
    "v_mfma_f32_16x16x32_bf16 v[60:63], a[144:147], a[188:191], v[60:63]\n"
    "ds_read_b64_tr_b16 a[218:219], v7 offset:19456\n"
    "v_mfma_f32_16x16x32_bf16 v[92:95], a[148:151], a[188:191], v[92:95]\n"
    "v_mfma_f32_16x16x32_bf16 v[88:91], a[148:151], a[184:187], v[88:91]\n"
    "ds_read_b128 a[248:251], v4 offset:12288\n"
    "v_mfma_f32_16x16x32_bf16 v[84:87], a[148:151], a[180:183], v[84:87]\n"
    "v_mfma_f32_16x16x32_bf16 v[80:83], a[148:151], a[176:179], v[80:83]\n"
    "ds_read_b64_tr_b16 a[220:221], v8 offset:18432\n"
    "v_mfma_f32_16x16x32_bf16 v[76:79], a[148:151], a[172:175], v[76:79]\n"
    "v_mfma_f32_16x16x32_bf16 v[72:75], a[148:151], a[168:171], v[72:75]\n"
    "ds_read_b64_tr_b16 a[222:223], v8 offset:19456\n"
    "v_mfma_f32_16x16x32_bf16 v[68:71], a[148:151], a[164:167], v[68:71]\n"
    "v_mfma_f32_16x16x32_bf16 v[64:67], a[148:151], a[160:163], v[64:67]\n"
    "ds_read_b128 a[252:255], v4 offset:14336\n"
    "v_mfma_f32_16x16x32_bf16 v[96:99], a[152:155], a[160:163], v[96:99]\n"
    "v_mfma_f32_16x16x32_bf16 v[100:103], a[152:155], a[164:167], v[100:103]\n"
    "v_mfma_f32_16x16x32_bf16 v[104:107], a[152:155], a[168:171], v[104:107]\n"
    "v_mfma_f32_16x16x32_bf16 v[108:111], a[152:155], a[172:175], v[108:111]\n"
    "v_mfma_f32_16x16x32_bf16 v[112:115], a[152:155], a[176:179], v[112:115]\n"
    "v_mfma_f32_16x16x32_bf16 v[116:119], a[152:155], a[180:183], v[116:119]\n"
    "v_mfma_f32_16x16x32_bf16 v[120:123], a[152:155], a[184:187], v[120:123]\n"
    "s_add_u32 s27, s27, 32768\n"
    "v_mfma_f32_16x16x32_bf16 v[124:127], a[152:155], a[188:191], v[124:127]\n"
    "s_cmp_ge_u32 s27, 98304\n"
    "v_mfma_f32_16x16x32_bf16 a[28:31], a[156:159], a[188:191], a[28:31]\n"
    "s_cselect_b32 s31, 98304, 0\n"
    "v_mfma_f32_16x16x32_bf16 a[24:27], a[156:159], a[184:187], a[24:27]\n"
    "s_sub_u32 s27, s27, s31\n"
    "v_mfma_f32_16x16x32_bf16 a[20:23], a[156:159], a[180:183], a[20:23]\n"
    "v_mfma_f32_16x16x32_bf16 a[16:19], a[156:159], a[176:179], a[16:19]\n"
    "v_mfma_f32_16x16x32_bf16 a[12:15], a[156:159], a[172:175], a[12:15]\n"
    "v_mfma_f32_16x16x32_bf16 a[8:11], a[156:159], a[168:171], a[8:11]\n"
    "v_mfma_f32_16x16x32_bf16 a[4:7], a[156:159], a[164:167], a[4:7]\n"
    "v_mfma_f32_16x16x32_bf16 a[0:3], a[156:159], a[160:163], a[0:3]\n"
    "s_waitcnt vmcnt(10) lgkmcnt(0)\n"
    "s_barrier\n"
    "v_mfma_f32_16x16x32_bf16 v[128:131], a[192:195], a[224:227], v[128:131]\n"
    "v_add_u32 v4, s27, v2\n"
    "v_mfma_f32_16x16x32_bf16 v[132:135], a[192:195], a[228:231], v[132:135]\n"
    "ds_read_b64_tr_b16 a[128:129], v5 offset:32768\n"
    "s_lshr_b32 s69, s34, 1\n"
    "v_mfma_f32_16x16x32_bf16 v[136:139], a[192:195], a[232:235], v[136:139]\n"
    "v_mfma_f32_16x16x32_bf16 v[140:143], a[192:195], a[236:239], v[140:143]\n"
    "ds_read_b64_tr_b16 a[130:131], v5 offset:33792\n"
    "s_lshl_b32 s72, s39, 1\n"
    "v_mfma_f32_16x16x32_bf16 v[144:147], a[192:195], a[240:243], v[144:147]\n"
    "v_mfma_f32_16x16x32_bf16 v[148:151], a[192:195], a[244:247], v[148:151]\n"
    "ds_read_b128 a[160:163], v4 offset:0\n"
    "s_add_u32 s72, s72, s69\n"
    "v_mfma_f32_16x16x32_bf16 v[152:155], a[192:195], a[248:251], v[152:155]\n"
    "v_mfma_f32_16x16x32_bf16 v[156:159], a[192:195], a[252:255], v[156:159]\n"
    "ds_read_b64_tr_b16 a[132:133], v6 offset:32768\n"
    "s_add_u32 s72, s72, 512\n"
    "v_mfma_f32_16x16x32_bf16 v[188:191], a[196:199], a[252:255], v[188:191]\n"
    "v_mfma_f32_16x16x32_bf16 v[184:187], a[196:199], a[248:251], v[184:187]\n"
    "ds_read_b64_tr_b16 a[134:135], v6 offset:33792\n"
    "s_mul_i32 s73, s69, 21248\n"
    "v_mfma_f32_16x16x32_bf16 v[180:183], a[196:199], a[244:247], v[180:183]\n"
    "v_mfma_f32_16x16x32_bf16 v[176:179], a[196:199], a[240:243], v[176:179]\n"
    "ds_read_b128 a[164:167], v4 offset:2048\n"
    "s_add_u32 s73, s73, 98304\n"
    "v_mfma_f32_16x16x32_bf16 v[172:175], a[196:199], a[236:239], v[172:175]\n"
    "v_mfma_f32_16x16x32_bf16 v[168:171], a[196:199], a[232:235], v[168:171]\n"
    "ds_read_b64_tr_b16 a[136:137], v7 offset:32768\n"
    "v_lshlrev_b32 v30, 4, v1\n"
    "v_mfma_f32_16x16x32_bf16 v[164:167], a[196:199], a[228:231], v[164:167]\n"
    "v_mfma_f32_16x16x32_bf16 v[160:163], a[196:199], a[224:227], v[160:163]\n"
    "ds_read_b64_tr_b16 a[138:139], v7 offset:33792\n"
    "v_cmp_eq_u32 vcc, 0, v1\n"
    "v_mfma_f32_16x16x32_bf16 v[192:195], a[200:203], a[224:227], v[192:195]\n"
    "v_mfma_f32_16x16x32_bf16 v[196:199], a[200:203], a[228:231], v[196:199]\n"
    "ds_read_b128 a[168:171], v4 offset:4096\n"
    "v_subrev_u32 v31, 8, v30\n"
    "v_mfma_f32_16x16x32_bf16 v[200:203], a[200:203], a[232:235], v[200:203]\n"
    "v_mfma_f32_16x16x32_bf16 v[204:207], a[200:203], a[236:239], v[204:207]\n"
    "ds_read_b64_tr_b16 a[140:141], v8 offset:32768\n"
    "v_mov_b32 v17, 0x80000000\n"
    "v_mfma_f32_16x16x32_bf16 v[208:211], a[200:203], a[240:243], v[208:211]\n"
    "v_mfma_f32_16x16x32_bf16 v[212:215], a[200:203], a[244:247], v[212:215]\n"
    "ds_read_b64_tr_b16 a[142:143], v8 offset:33792\n"
    "s_lshl_b32 s68, s72, 12\n"
    "v_mfma_f32_16x16x32_bf16 v[216:219], a[200:203], a[248:251], v[216:219]\n"
    "v_mfma_f32_16x16x32_bf16 v[220:223], a[200:203], a[252:255], v[220:223]\n"
    "ds_read_b128 a[172:175], v4 offset:6144\n"
    "v_cndmask_b32 v31, v31, v17, vcc\n"
    "v_mfma_f32_16x16x32_bf16 v[252:255], a[204:207], a[252:255], v[252:255]\n"
    "v_mfma_f32_16x16x32_bf16 v[248:251], a[204:207], a[248:251], v[248:251]\n"
    "ds_read_b64_tr_b16 a[144:145], v5 offset:34816\n"
    "s_and_b32 s66, s34, 1\n"
    "v_mfma_f32_16x16x32_bf16 v[244:247], a[204:207], a[244:247], v[244:247]\n"
    "v_mfma_f32_16x16x32_bf16 v[240:243], a[204:207], a[240:243], v[240:243]\n"
    "ds_read_b64_tr_b16 a[146:147], v5 offset:35840\n"
    "s_lshl_b32 s66, s66, 11\n"
    "v_mfma_f32_16x16x32_bf16 v[236:239], a[204:207], a[236:239], v[236:239]\n"
    "v_mfma_f32_16x16x32_bf16 v[232:235], a[204:207], a[232:235], v[232:235]\n"
    "ds_read_b128 a[176:179], v4 offset:8192\n"
    "s_mul_i32 s69, s72, 4224\n"
    "v_mfma_f32_16x16x32_bf16 v[228:231], a[204:207], a[228:231], v[228:231]\n"
    "v_mfma_f32_16x16x32_bf16 v[224:227], a[204:207], a[224:227], v[224:227]\n"
    "ds_read_b64_tr_b16 a[148:149], v6 offset:34816\n"
    "s_add_u32 s66, s66, s69\n"
    "v_mfma_f32_16x16x32_bf16 v[32:35], a[208:211], a[224:227], v[32:35]\n"
    "v_mfma_f32_16x16x32_bf16 v[36:39], a[208:211], a[228:231], v[36:39]\n"
    "ds_read_b64_tr_b16 a[150:151], v6 offset:35840\n"
    "s_add_u32 s66, s66, 18874368\n"
    "v_mfma_f32_16x16x32_bf16 v[40:43], a[208:211], a[232:235], v[40:43]\n"
    "v_mfma_f32_16x16x32_bf16 v[44:47], a[208:211], a[236:239], v[44:47]\n"
    "ds_read_b128 a[180:183], v4 offset:10240\n"
    "v_mfma_f32_16x16x32_bf16 v[48:51], a[208:211], a[240:243], v[48:51]\n"
    "v_mfma_f32_16x16x32_bf16 v[52:55], a[208:211], a[244:247], v[52:55]\n"
    "ds_read_b64_tr_b16 a[152:153], v7 offset:34816\n"
    "v_mfma_f32_16x16x32_bf16 v[56:59], a[208:211], a[248:251], v[56:59]\n"
    "v_mfma_f32_16x16x32_bf16 v[60:63], a[208:211], a[252:255], v[60:63]\n"
    "ds_read_b64_tr_b16 a[154:155], v7 offset:35840\n"
    "v_mfma_f32_16x16x32_bf16 v[92:95], a[212:215], a[252:255], v[92:95]\n"
    "v_mfma_f32_16x16x32_bf16 v[88:91], a[212:215], a[248:251], v[88:91]\n"
    "ds_read_b128 a[184:187], v4 offset:12288\n"
    "v_mfma_f32_16x16x32_bf16 v[84:87], a[212:215], a[244:247], v[84:87]\n"
    "v_mfma_f32_16x16x32_bf16 v[80:83], a[212:215], a[240:243], v[80:83]\n"
    "ds_read_b64_tr_b16 a[156:157], v8 offset:34816\n"
    "v_mfma_f32_16x16x32_bf16 v[76:79], a[212:215], a[236:239], v[76:79]\n"
    "v_mfma_f32_16x16x32_bf16 v[72:75], a[212:215], a[232:235], v[72:75]\n"
    "ds_read_b64_tr_b16 a[158:159], v8 offset:35840\n"
    "v_mfma_f32_16x16x32_bf16 v[68:71], a[212:215], a[228:231], v[68:71]\n"
    "v_mfma_f32_16x16x32_bf16 v[64:67], a[212:215], a[224:227], v[64:67]\n"
    "ds_read_b128 a[188:191], v4 offset:14336\n"
    "v_mfma_f32_16x16x32_bf16 v[96:99], a[216:219], a[224:227], v[96:99]\n"
    "v_mfma_f32_16x16x32_bf16 v[100:103], a[216:219], a[228:231], v[100:103]\n"
    "v_mfma_f32_16x16x32_bf16 v[104:107], a[216:219], a[232:235], v[104:107]\n"
    "v_mfma_f32_16x16x32_bf16 v[108:111], a[216:219], a[236:239], v[108:111]\n"
    "v_mfma_f32_16x16x32_bf16 v[112:115], a[216:219], a[240:243], v[112:115]\n"
    "v_mfma_f32_16x16x32_bf16 v[116:119], a[216:219], a[244:247], v[116:119]\n"
    "v_mfma_f32_16x16x32_bf16 v[120:123], a[216:219], a[248:251], v[120:123]\n"
    "v_mfma_f32_16x16x32_bf16 v[124:127], a[216:219], a[252:255], v[124:127]\n"
    "v_mfma_f32_16x16x32_bf16 a[28:31], a[220:223], a[252:255], a[28:31]\n"
    "v_mfma_f32_16x16x32_bf16 a[24:27], a[220:223], a[248:251], a[24:27]\n"
    "v_mfma_f32_16x16x32_bf16 a[20:23], a[220:223], a[244:247], a[20:23]\n"
    "v_mfma_f32_16x16x32_bf16 a[16:19], a[220:223], a[240:243], a[16:19]\n"
    "v_mfma_f32_16x16x32_bf16 a[12:15], a[220:223], a[236:239], a[12:15]\n"
    "v_mfma_f32_16x16x32_bf16 a[8:11], a[220:223], a[232:235], a[8:11]\n"
    "v_mfma_f32_16x16x32_bf16 a[4:7], a[220:223], a[228:231], a[4:7]\n"
    "v_mfma_f32_16x16x32_bf16 a[0:3], a[220:223], a[224:227], a[0:3]\n"
    "s_waitcnt vmcnt(1) lgkmcnt(0)\n"
    "s_barrier\n"
    "v_cmp_ne_u64 vcc, s[70:71], v[18:19]\n"
    "s_cbranch_vccz UOK_gated_mix__ssm_conv_h3__swiglu_ffn__prompt_kernel\n"
    "s_mov_b32 s29, 0x4000\n"
    "PU_gated_mix__ssm_conv_h3__swiglu_ffn__prompt_kernel:\n"
    "buffer_load_dwordx2 v[18:19], v16, s[20:23], 0 offen sc1\n"
    "s_waitcnt vmcnt(0)\n"
    "v_cmp_ne_u64 vcc, s[70:71], v[18:19]\n"
    "s_cbranch_vccz PUd_gated_mix__ssm_conv_h3__swiglu_ffn__prompt_kernel\n"
    "s_sleep 1\n"
    "s_sub_u32 s29, s29, 1\n"
    "s_cmp_lg_u32 s29, 0\n"
    "s_cbranch_scc1 PU_gated_mix__ssm_conv_h3__swiglu_ffn__prompt_kernel\n"
    "PUd_gated_mix__ssm_conv_h3__swiglu_ffn__prompt_kernel:\n"
    "UOK_gated_mix__ssm_conv_h3__swiglu_ffn__prompt_kernel:\n"
    "v_mfma_f32_16x16x32_bf16 v[128:131], a[128:131], a[160:163], v[128:131]\n"
    "v_add_u32 v4, s27, v3\n"
    "v_mfma_f32_16x16x32_bf16 v[132:135], a[128:131], a[164:167], v[132:135]\n"
    "ds_read_b64_tr_b16 a[192:193], v5 offset:49152\n"
    "v_mfma_f32_16x16x32_bf16 v[136:139], a[128:131], a[168:171], v[136:139]\n"
    "v_mfma_f32_16x16x32_bf16 v[140:143], a[128:131], a[172:175], v[140:143]\n"
    "ds_read_b64_tr_b16 a[194:195], v5 offset:50176\n"
    "v_mfma_f32_16x16x32_bf16 v[144:147], a[128:131], a[176:179], v[144:147]\n"
    "v_mfma_f32_16x16x32_bf16 v[148:151], a[128:131], a[180:183], v[148:151]\n"
    "ds_read_b128 a[224:227], v4 offset:0\n"
    "v_mfma_f32_16x16x32_bf16 v[152:155], a[128:131], a[184:187], v[152:155]\n"
    "v_mfma_f32_16x16x32_bf16 v[156:159], a[128:131], a[188:191], v[156:159]\n"
    "ds_read_b64_tr_b16 a[196:197], v6 offset:49152\n"
    "v_mfma_f32_16x16x32_bf16 v[188:191], a[132:135], a[188:191], v[188:191]\n"
    "v_mfma_f32_16x16x32_bf16 v[184:187], a[132:135], a[184:187], v[184:187]\n"
    "ds_read_b64_tr_b16 a[198:199], v6 offset:50176\n"
    "v_mfma_f32_16x16x32_bf16 v[180:183], a[132:135], a[180:183], v[180:183]\n"
    "v_mfma_f32_16x16x32_bf16 v[176:179], a[132:135], a[176:179], v[176:179]\n"
    "ds_read_b128 a[228:231], v4 offset:2048\n"
    "v_mfma_f32_16x16x32_bf16 v[172:175], a[132:135], a[172:175], v[172:175]\n"
    "v_mfma_f32_16x16x32_bf16 v[168:171], a[132:135], a[168:171], v[168:171]\n"
    "ds_read_b64_tr_b16 a[200:201], v7 offset:49152\n"
    "v_mfma_f32_16x16x32_bf16 v[164:167], a[132:135], a[164:167], v[164:167]\n"
    "v_mfma_f32_16x16x32_bf16 v[160:163], a[132:135], a[160:163], v[160:163]\n"
    "ds_read_b64_tr_b16 a[202:203], v7 offset:50176\n"
    "v_mfma_f32_16x16x32_bf16 v[192:195], a[136:139], a[160:163], v[192:195]\n"
    "v_mfma_f32_16x16x32_bf16 v[196:199], a[136:139], a[164:167], v[196:199]\n"
    "ds_read_b128 a[232:235], v4 offset:4096\n"
    "v_mfma_f32_16x16x32_bf16 v[200:203], a[136:139], a[168:171], v[200:203]\n"
    "v_mfma_f32_16x16x32_bf16 v[204:207], a[136:139], a[172:175], v[204:207]\n"
    "ds_read_b64_tr_b16 a[204:205], v8 offset:49152\n"
    "v_mfma_f32_16x16x32_bf16 v[208:211], a[136:139], a[176:179], v[208:211]\n"
    "v_mfma_f32_16x16x32_bf16 v[212:215], a[136:139], a[180:183], v[212:215]\n"
    "ds_read_b64_tr_b16 a[206:207], v8 offset:50176\n"
    "v_mfma_f32_16x16x32_bf16 v[216:219], a[136:139], a[184:187], v[216:219]\n"
    "v_mfma_f32_16x16x32_bf16 v[220:223], a[136:139], a[188:191], v[220:223]\n"
    "ds_read_b128 a[236:239], v4 offset:6144\n"
    "v_mfma_f32_16x16x32_bf16 v[252:255], a[140:143], a[188:191], v[252:255]\n"
    "v_mfma_f32_16x16x32_bf16 v[248:251], a[140:143], a[184:187], v[248:251]\n"
    "ds_read_b64_tr_b16 a[208:209], v5 offset:51200\n"
    "v_mfma_f32_16x16x32_bf16 v[244:247], a[140:143], a[180:183], v[244:247]\n"
    "v_mfma_f32_16x16x32_bf16 v[240:243], a[140:143], a[176:179], v[240:243]\n"
    "ds_read_b64_tr_b16 a[210:211], v5 offset:52224\n"
    "v_mfma_f32_16x16x32_bf16 v[236:239], a[140:143], a[172:175], v[236:239]\n"
    "v_mfma_f32_16x16x32_bf16 v[232:235], a[140:143], a[168:171], v[232:235]\n"
    "ds_read_b128 a[240:243], v4 offset:8192\n"
    "v_mfma_f32_16x16x32_bf16 v[228:231], a[140:143], a[164:167], v[228:231]\n"
    "v_mfma_f32_16x16x32_bf16 v[224:227], a[140:143], a[160:163], v[224:227]\n"
    "ds_read_b64_tr_b16 a[212:213], v6 offset:51200\n"
    "v_mfma_f32_16x16x32_bf16 v[32:35], a[144:147], a[160:163], v[32:35]\n"
    "v_mfma_f32_16x16x32_bf16 v[36:39], a[144:147], a[164:167], v[36:39]\n"
    "ds_read_b64_tr_b16 a[214:215], v6 offset:52224\n"
    "v_mfma_f32_16x16x32_bf16 v[40:43], a[144:147], a[168:171], v[40:43]\n"
    "v_mfma_f32_16x16x32_bf16 v[44:47], a[144:147], a[172:175], v[44:47]\n"
    "ds_read_b128 a[244:247], v4 offset:10240\n"
    "v_mfma_f32_16x16x32_bf16 v[48:51], a[144:147], a[176:179], v[48:51]\n"
    "v_mfma_f32_16x16x32_bf16 v[52:55], a[144:147], a[180:183], v[52:55]\n"
    "ds_read_b64_tr_b16 a[216:217], v7 offset:51200\n"
    "v_mfma_f32_16x16x32_bf16 v[56:59], a[144:147], a[184:187], v[56:59]\n"
    "v_mfma_f32_16x16x32_bf16 v[60:63], a[144:147], a[188:191], v[60:63]\n"
    "ds_read_b64_tr_b16 a[218:219], v7 offset:52224\n"
    "v_mfma_f32_16x16x32_bf16 v[92:95], a[148:151], a[188:191], v[92:95]\n"
    "v_mfma_f32_16x16x32_bf16 v[88:91], a[148:151], a[184:187], v[88:91]\n"
    "ds_read_b128 a[248:251], v4 offset:12288\n"
    "v_mfma_f32_16x16x32_bf16 v[84:87], a[148:151], a[180:183], v[84:87]\n"
    "v_mfma_f32_16x16x32_bf16 v[80:83], a[148:151], a[176:179], v[80:83]\n"
    "ds_read_b64_tr_b16 a[220:221], v8 offset:51200\n"
    "v_mfma_f32_16x16x32_bf16 v[76:79], a[148:151], a[172:175], v[76:79]\n"
    "v_mfma_f32_16x16x32_bf16 v[72:75], a[148:151], a[168:171], v[72:75]\n"
    "ds_read_b64_tr_b16 a[222:223], v8 offset:52224\n"
    "v_mfma_f32_16x16x32_bf16 v[68:71], a[148:151], a[164:167], v[68:71]\n"
    "v_mfma_f32_16x16x32_bf16 v[64:67], a[148:151], a[160:163], v[64:67]\n"
    "ds_read_b128 a[252:255], v4 offset:14336\n"
    "v_mfma_f32_16x16x32_bf16 v[96:99], a[152:155], a[160:163], v[96:99]\n"
    "v_mfma_f32_16x16x32_bf16 v[100:103], a[152:155], a[164:167], v[100:103]\n"
    "v_mfma_f32_16x16x32_bf16 v[104:107], a[152:155], a[168:171], v[104:107]\n"
    "v_mfma_f32_16x16x32_bf16 v[108:111], a[152:155], a[172:175], v[108:111]\n"
    "v_mfma_f32_16x16x32_bf16 v[112:115], a[152:155], a[176:179], v[112:115]\n"
    "v_mfma_f32_16x16x32_bf16 v[116:119], a[152:155], a[180:183], v[116:119]\n"
    "v_mfma_f32_16x16x32_bf16 v[120:123], a[152:155], a[184:187], v[120:123]\n"
    "s_add_u32 s27, s27, 32768\n"
    "v_mfma_f32_16x16x32_bf16 v[124:127], a[152:155], a[188:191], v[124:127]\n"
    "s_cmp_ge_u32 s27, 98304\n"
    "v_mfma_f32_16x16x32_bf16 a[28:31], a[156:159], a[188:191], a[28:31]\n"
    "s_cselect_b32 s31, 98304, 0\n"
    "v_mfma_f32_16x16x32_bf16 a[24:27], a[156:159], a[184:187], a[24:27]\n"
    "s_sub_u32 s27, s27, s31\n"
    "v_mfma_f32_16x16x32_bf16 a[20:23], a[156:159], a[180:183], a[20:23]\n"
    "v_mfma_f32_16x16x32_bf16 a[16:19], a[156:159], a[176:179], a[16:19]\n"
    "v_mfma_f32_16x16x32_bf16 a[12:15], a[156:159], a[172:175], a[12:15]\n"
    "v_mfma_f32_16x16x32_bf16 a[8:11], a[156:159], a[168:171], a[8:11]\n"
    "v_mfma_f32_16x16x32_bf16 a[4:7], a[156:159], a[164:167], a[4:7]\n"
    "v_mfma_f32_16x16x32_bf16 a[0:3], a[156:159], a[160:163], a[0:3]\n"
    "s_waitcnt lgkmcnt(0)\n"
    "s_barrier\n"
    "v_mfma_f32_16x16x32_bf16 v[128:131], a[192:195], a[224:227], v[128:131]\n"
    "v_mfma_f32_16x16x32_bf16 v[132:135], a[192:195], a[228:231], v[132:135]\n"
    "buffer_load_dwordx4 a[54:57], v30, s[80:83], s68 offen offset:0\n"
    "v_mfma_f32_16x16x32_bf16 v[136:139], a[192:195], a[232:235], v[136:139]\n"
    "v_mfma_f32_16x16x32_bf16 v[140:143], a[192:195], a[236:239], v[140:143]\n"
    "v_mfma_f32_16x16x32_bf16 v[144:147], a[192:195], a[240:243], v[144:147]\n"
    "buffer_load_dwordx2 a[52:53], v31, s[80:83], s68 offen offset:0\n"
    "v_mfma_f32_16x16x32_bf16 v[148:151], a[192:195], a[244:247], v[148:151]\n"
    "v_mfma_f32_16x16x32_bf16 v[152:155], a[192:195], a[248:251], v[152:155]\n"
    "v_mfma_f32_16x16x32_bf16 v[156:159], a[192:195], a[252:255], v[156:159]\n"
    "buffer_load_dwordx4 a[60:63], v30, s[80:83], s68 offen offset:1024\n"
    "v_mfma_f32_16x16x32_bf16 v[188:191], a[196:199], a[252:255], v[188:191]\n"
    "v_mfma_f32_16x16x32_bf16 v[184:187], a[196:199], a[248:251], v[184:187]\n"
    "v_mfma_f32_16x16x32_bf16 v[180:183], a[196:199], a[244:247], v[180:183]\n"
    "buffer_load_dwordx2 a[58:59], v30, s[80:83], s68 offen offset:1016\n"
    "v_mfma_f32_16x16x32_bf16 v[176:179], a[196:199], a[240:243], v[176:179]\n"
    "v_mfma_f32_16x16x32_bf16 v[172:175], a[196:199], a[236:239], v[172:175]\n"
    "v_mfma_f32_16x16x32_bf16 v[168:171], a[196:199], a[232:235], v[168:171]\n"
    "buffer_load_dwordx4 a[66:69], v30, s[80:83], s68 offen offset:2048\n"
    "v_mfma_f32_16x16x32_bf16 v[164:167], a[196:199], a[228:231], v[164:167]\n"
    "v_mfma_f32_16x16x32_bf16 v[160:163], a[196:199], a[224:227], v[160:163]\n"
    "v_mfma_f32_16x16x32_bf16 v[192:195], a[200:203], a[224:227], v[192:195]\n"
    "buffer_load_dwordx2 a[64:65], v30, s[80:83], s68 offen offset:2040\n"
    "v_mfma_f32_16x16x32_bf16 v[196:199], a[200:203], a[228:231], v[196:199]\n"
    "v_mfma_f32_16x16x32_bf16 v[200:203], a[200:203], a[232:235], v[200:203]\n"
    "v_mfma_f32_16x16x32_bf16 v[204:207], a[200:203], a[236:239], v[204:207]\n"
    "buffer_load_dwordx4 a[72:75], v30, s[80:83], s68 offen offset:3072\n"
    "v_mfma_f32_16x16x32_bf16 v[208:211], a[200:203], a[240:243], v[208:211]\n"
    "v_mfma_f32_16x16x32_bf16 v[212:215], a[200:203], a[244:247], v[212:215]\n"
    "v_mfma_f32_16x16x32_bf16 v[216:219], a[200:203], a[248:251], v[216:219]\n"
    "buffer_load_dwordx2 a[70:71], v30, s[80:83], s68 offen offset:3064\n"
    "v_mfma_f32_16x16x32_bf16 v[220:223], a[200:203], a[252:255], v[220:223]\n"
    "v_mfma_f32_16x16x32_bf16 v[252:255], a[204:207], a[252:255], v[252:255]\n"
    "v_mfma_f32_16x16x32_bf16 v[248:251], a[204:207], a[248:251], v[248:251]\n"
    "buffer_load_dwordx4 a[32:35], v30, s[20:23], s66 offen offset:0 sc1\n"
    "v_mfma_f32_16x16x32_bf16 v[244:247], a[204:207], a[244:247], v[244:247]\n"
    "v_mfma_f32_16x16x32_bf16 v[240:243], a[204:207], a[240:243], v[240:243]\n"
    "v_mfma_f32_16x16x32_bf16 v[236:239], a[204:207], a[236:239], v[236:239]\n"
    "buffer_load_dwordx4 a[36:39], v30, s[20:23], s66 offen offset:1024 sc1\n"
    "v_mfma_f32_16x16x32_bf16 v[232:235], a[204:207], a[232:235], v[232:235]\n"
    "v_mfma_f32_16x16x32_bf16 v[228:231], a[204:207], a[228:231], v[228:231]\n"
    "v_mfma_f32_16x16x32_bf16 v[224:227], a[204:207], a[224:227], v[224:227]\n"
    "s_lshl_b32 s30, s39, 2\n"
    "v_mfma_f32_16x16x32_bf16 v[32:35], a[208:211], a[224:227], v[32:35]\n"
    "v_mfma_f32_16x16x32_bf16 v[36:39], a[208:211], a[228:231], v[36:39]\n"
    "v_mfma_f32_16x16x32_bf16 v[40:43], a[208:211], a[232:235], v[40:43]\n"
    "s_add_u32 s30, s30, s34\n"
    "v_mfma_f32_16x16x32_bf16 v[44:47], a[208:211], a[236:239], v[44:47]\n"
    "v_mfma_f32_16x16x32_bf16 v[48:51], a[208:211], a[240:243], v[48:51]\n"
    "v_mfma_f32_16x16x32_bf16 v[52:55], a[208:211], a[244:247], v[52:55]\n"
    "s_lshl_b32 s30, s30, 12\n"
    "v_mfma_f32_16x16x32_bf16 v[56:59], a[208:211], a[248:251], v[56:59]\n"
    "v_mfma_f32_16x16x32_bf16 v[60:63], a[208:211], a[252:255], v[60:63]\n"
    "v_mfma_f32_16x16x32_bf16 v[92:95], a[212:215], a[252:255], v[92:95]\n"
    "buffer_load_dwordx4 a[76:79], v30, s[8:11], s30 offen offset:0\n"
    "v_mfma_f32_16x16x32_bf16 v[88:91], a[212:215], a[248:251], v[88:91]\n"
    "v_mfma_f32_16x16x32_bf16 v[84:87], a[212:215], a[244:247], v[84:87]\n"
    "v_mfma_f32_16x16x32_bf16 v[80:83], a[212:215], a[240:243], v[80:83]\n"
    "buffer_load_dwordx4 a[80:83], v30, s[8:11], s30 offen offset:1024\n"
    "v_mfma_f32_16x16x32_bf16 v[76:79], a[212:215], a[236:239], v[76:79]\n"
    "v_mfma_f32_16x16x32_bf16 v[72:75], a[212:215], a[232:235], v[72:75]\n"
    "v_mfma_f32_16x16x32_bf16 v[68:71], a[212:215], a[228:231], v[68:71]\n"
    "buffer_load_dwordx4 a[84:87], v30, s[8:11], s30 offen offset:2048\n"
    "v_mfma_f32_16x16x32_bf16 v[64:67], a[212:215], a[224:227], v[64:67]\n"
    "v_mfma_f32_16x16x32_bf16 v[96:99], a[216:219], a[224:227], v[96:99]\n"
    "v_mfma_f32_16x16x32_bf16 v[100:103], a[216:219], a[228:231], v[100:103]\n"
    "buffer_load_dwordx4 a[88:91], v30, s[8:11], s30 offen offset:3072\n"
    "v_mfma_f32_16x16x32_bf16 v[104:107], a[216:219], a[232:235], v[104:107]\n"
    "v_mfma_f32_16x16x32_bf16 v[108:111], a[216:219], a[236:239], v[108:111]\n"
    "v_mfma_f32_16x16x32_bf16 v[112:115], a[216:219], a[240:243], v[112:115]\n"
    "global_load_dwordx4 a[92:95], v30, s[90:91] offset:0\n"
    "v_mfma_f32_16x16x32_bf16 v[116:119], a[216:219], a[244:247], v[116:119]\n"
    "v_mfma_f32_16x16x32_bf16 v[120:123], a[216:219], a[248:251], v[120:123]\n"
    "v_mfma_f32_16x16x32_bf16 v[124:127], a[216:219], a[252:255], v[124:127]\n"
    "global_load_dwordx4 a[96:99], v30, s[90:91] offset:1024\n"
    "v_mfma_f32_16x16x32_bf16 a[28:31], a[220:223], a[252:255], a[28:31]\n"
    "v_mfma_f32_16x16x32_bf16 a[24:27], a[220:223], a[248:251], a[24:27]\n"
    "v_mfma_f32_16x16x32_bf16 a[20:23], a[220:223], a[244:247], a[20:23]\n"
    "v_mfma_f32_16x16x32_bf16 a[16:19], a[220:223], a[240:243], a[16:19]\n"
    "v_mfma_f32_16x16x32_bf16 a[12:15], a[220:223], a[236:239], a[12:15]\n"
    "v_mfma_f32_16x16x32_bf16 a[8:11], a[220:223], a[232:235], a[8:11]\n"
    "v_mfma_f32_16x16x32_bf16 a[4:7], a[220:223], a[228:231], a[4:7]\n"
    "v_mfma_f32_16x16x32_bf16 a[0:3], a[220:223], a[224:227], a[0:3]\n"
    "s_nop 7\n"
    "v_lshlrev_b32 v2, 4, v1\n"
    "s_mov_b32 s66, 0xbfb8aa3b\n"
    "s_mov_b32 s67, 0xbfb8aa3b\n"
    "v_pk_mul_f32 v[4:5], v[128:129], s[66:67]\n"
    "v_pk_mul_f32 v[6:7], v[130:131], s[66:67]\n"
    "v_pk_mul_f32 v[8:9], v[160:161], s[66:67]\n"
    "v_pk_mul_f32 v[10:11], v[162:163], s[66:67]\n"
    "v_exp_f32 v4, v4\n"
    "v_exp_f32 v5, v5\n"
    "v_exp_f32 v6, v6\n"
    "v_exp_f32 v7, v7\n"
    "v_exp_f32 v8, v8\n"
    "v_exp_f32 v9, v9\n"
    "v_exp_f32 v10, v10\n"
    "v_exp_f32 v11, v11\n"
    "v_pk_mul_f32 v[128:129], v[128:129], v[32:33]\n"
    "v_pk_mul_f32 v[130:131], v[130:131], v[34:35]\n"
    "v_pk_mul_f32 v[160:161], v[160:161], v[64:65]\n"
    "v_pk_mul_f32 v[162:163], v[162:163], v[66:67]\n"
    "v_pk_add_f32 v[4:5], v[4:5], 1.0 op_sel_hi:[1,0]\n"
    "v_pk_add_f32 v[6:7], v[6:7], 1.0 op_sel_hi:[1,0]\n"
    "v_pk_add_f32 v[8:9], v[8:9], 1.0 op_sel_hi:[1,0]\n"
    "v_pk_add_f32 v[10:11], v[10:11], 1.0 op_sel_hi:[1,0]\n"
    "v_rcp_f32 v4, v4\n"
    "v_rcp_f32 v5, v5\n"
    "v_rcp_f32 v6, v6\n"
    "v_rcp_f32 v7, v7\n"
    "v_rcp_f32 v8, v8\n"
    "v_rcp_f32 v9, v9\n"
    "v_rcp_f32 v10, v10\n"
    "v_rcp_f32 v11, v11\n"
    "s_nop 0\n"
    "v_pk_mul_f32 v[4:5], v[4:5], v[128:129]\n"
    "v_pk_mul_f32 v[6:7], v[6:7], v[130:131]\n"
    "v_pk_mul_f32 v[8:9], v[8:9], v[160:161]\n"
    "v_pk_mul_f32 v[10:11], v[10:11], v[162:163]\n"
    "v_cvt_pk_bf16_f32 v12, v4, v5\n"
    "v_cvt_pk_bf16_f32 v13, v6, v7\n"
    "v_cvt_pk_bf16_f32 v14, v8, v9\n"
    "v_cvt_pk_bf16_f32 v15, v10, v11\n"
    "buffer_store_dwordx4 v[12:15], v2, s[20:23], s96 offen offset:0 sc1\n"
    "v_pk_mul_f32 v[16:17], v[192:193], s[66:67]\n"
    "v_pk_mul_f32 v[18:19], v[194:195], s[66:67]\n"
    "v_pk_mul_f32 v[20:21], v[224:225], s[66:67]\n"
    "v_pk_mul_f32 v[22:23], v[226:227], s[66:67]\n"
    "v_exp_f32 v16, v16\n"
    "v_exp_f32 v17, v17\n"
    "v_exp_f32 v18, v18\n"
    "v_exp_f32 v19, v19\n"
    "v_exp_f32 v20, v20\n"
    "v_exp_f32 v21, v21\n"
    "v_exp_f32 v22, v22\n"
    "v_exp_f32 v23, v23\n"
    "v_pk_mul_f32 v[192:193], v[192:193], v[96:97]\n"
    "v_pk_mul_f32 v[194:195], v[194:195], v[98:99]\n"
    "v_accvgpr_read_b32 v28, a0\n"
    "v_accvgpr_read_b32 v29, a1\n"
    "v_pk_mul_f32 v[224:225], v[224:225], v[28:29]\n"
    "v_accvgpr_read_b32 v28, a2\n"
    "v_accvgpr_read_b32 v29, a3\n"
    "v_pk_mul_f32 v[226:227], v[226:227], v[28:29]\n"
    "v_pk_add_f32 v[16:17], v[16:17], 1.0 op_sel_hi:[1,0]\n"
    "v_pk_add_f32 v[18:19], v[18:19], 1.0 op_sel_hi:[1,0]\n"
    "v_pk_add_f32 v[20:21], v[20:21], 1.0 op_sel_hi:[1,0]\n"
    "v_pk_add_f32 v[22:23], v[22:23], 1.0 op_sel_hi:[1,0]\n"
    "v_rcp_f32 v16, v16\n"
    "v_rcp_f32 v17, v17\n"
    "v_rcp_f32 v18, v18\n"
    "v_rcp_f32 v19, v19\n"
    "v_rcp_f32 v20, v20\n"
    "v_rcp_f32 v21, v21\n"
    "v_rcp_f32 v22, v22\n"
    "v_rcp_f32 v23, v23\n"
    "s_nop 0\n"
    "v_pk_mul_f32 v[16:17], v[16:17], v[192:193]\n"
    "v_pk_mul_f32 v[18:19], v[18:19], v[194:195]\n"
    "v_pk_mul_f32 v[20:21], v[20:21], v[224:225]\n"
    "v_pk_mul_f32 v[22:23], v[22:23], v[226:227]\n"
    "v_cvt_pk_bf16_f32 v24, v16, v17\n"
    "v_cvt_pk_bf16_f32 v25, v18, v19\n"
    "v_cvt_pk_bf16_f32 v26, v20, v21\n"
    "v_cvt_pk_bf16_f32 v27, v22, v23\n"
    "buffer_store_dwordx4 v[24:27], v2, s[20:23], s96 offen offset:1024 sc1\n"
    "s_add_u32 s96, s96, 114688\n"
    "v_pk_mul_f32 v[4:5], v[132:133], s[66:67]\n"
    "v_pk_mul_f32 v[6:7], v[134:135], s[66:67]\n"
    "v_pk_mul_f32 v[8:9], v[164:165], s[66:67]\n"
    "v_pk_mul_f32 v[10:11], v[166:167], s[66:67]\n"
    "v_exp_f32 v4, v4\n"
    "v_exp_f32 v5, v5\n"
    "v_exp_f32 v6, v6\n"
    "v_exp_f32 v7, v7\n"
    "v_exp_f32 v8, v8\n"
    "v_exp_f32 v9, v9\n"
    "v_exp_f32 v10, v10\n"
    "v_exp_f32 v11, v11\n"
    "v_pk_mul_f32 v[132:133], v[132:133], v[36:37]\n"
    "v_pk_mul_f32 v[134:135], v[134:135], v[38:39]\n"
    "v_pk_mul_f32 v[164:165], v[164:165], v[68:69]\n"
    "v_pk_mul_f32 v[166:167], v[166:167], v[70:71]\n"
    "v_pk_add_f32 v[4:5], v[4:5], 1.0 op_sel_hi:[1,0]\n"
    "v_pk_add_f32 v[6:7], v[6:7], 1.0 op_sel_hi:[1,0]\n"
    "v_pk_add_f32 v[8:9], v[8:9], 1.0 op_sel_hi:[1,0]\n"
    "v_pk_add_f32 v[10:11], v[10:11], 1.0 op_sel_hi:[1,0]\n"
    "v_rcp_f32 v4, v4\n"
    "v_rcp_f32 v5, v5\n"
    "v_rcp_f32 v6, v6\n"
    "v_rcp_f32 v7, v7\n"
    "v_rcp_f32 v8, v8\n"
    "v_rcp_f32 v9, v9\n"
    "v_rcp_f32 v10, v10\n"
    "v_rcp_f32 v11, v11\n"
    "s_nop 0\n"
    "v_pk_mul_f32 v[4:5], v[4:5], v[132:133]\n"
    "v_pk_mul_f32 v[6:7], v[6:7], v[134:135]\n"
    "v_pk_mul_f32 v[8:9], v[8:9], v[164:165]\n"
    "v_pk_mul_f32 v[10:11], v[10:11], v[166:167]\n"
    "v_cvt_pk_bf16_f32 v12, v4, v5\n"
    "v_cvt_pk_bf16_f32 v13, v6, v7\n"
    "v_cvt_pk_bf16_f32 v14, v8, v9\n"
    "v_cvt_pk_bf16_f32 v15, v10, v11\n"
    "buffer_store_dwordx4 v[12:15], v2, s[20:23], s96 offen offset:0 sc1\n"
    "v_pk_mul_f32 v[16:17], v[196:197], s[66:67]\n"
    "v_pk_mul_f32 v[18:19], v[198:199], s[66:67]\n"
    "v_pk_mul_f32 v[20:21], v[228:229], s[66:67]\n"
    "v_pk_mul_f32 v[22:23], v[230:231], s[66:67]\n"
    "v_exp_f32 v16, v16\n"
    "v_exp_f32 v17, v17\n"
    "v_exp_f32 v18, v18\n"
    "v_exp_f32 v19, v19\n"
    "v_exp_f32 v20, v20\n"
    "v_exp_f32 v21, v21\n"
    "v_exp_f32 v22, v22\n"
    "v_exp_f32 v23, v23\n"
    "v_pk_mul_f32 v[196:197], v[196:197], v[100:101]\n"
    "v_pk_mul_f32 v[198:199], v[198:199], v[102:103]\n"
    "v_accvgpr_read_b32 v28, a4\n"
    "v_accvgpr_read_b32 v29, a5\n"
    "v_pk_mul_f32 v[228:229], v[228:229], v[28:29]\n"
    "v_accvgpr_read_b32 v28, a6\n"
    "v_accvgpr_read_b32 v29, a7\n"
    "v_pk_mul_f32 v[230:231], v[230:231], v[28:29]\n"
    "v_pk_add_f32 v[16:17], v[16:17], 1.0 op_sel_hi:[1,0]\n"
    "v_pk_add_f32 v[18:19], v[18:19], 1.0 op_sel_hi:[1,0]\n"
    "v_pk_add_f32 v[20:21], v[20:21], 1.0 op_sel_hi:[1,0]\n"
    "v_pk_add_f32 v[22:23], v[22:23], 1.0 op_sel_hi:[1,0]\n"
    "v_rcp_f32 v16, v16\n"
    "v_rcp_f32 v17, v17\n"
    "v_rcp_f32 v18, v18\n"
    "v_rcp_f32 v19, v19\n"
    "v_rcp_f32 v20, v20\n"
    "v_rcp_f32 v21, v21\n"
    "v_rcp_f32 v22, v22\n"
    "v_rcp_f32 v23, v23\n"
    "s_nop 0\n"
    "v_pk_mul_f32 v[16:17], v[16:17], v[196:197]\n"
    "v_pk_mul_f32 v[18:19], v[18:19], v[198:199]\n"
    "v_pk_mul_f32 v[20:21], v[20:21], v[228:229]\n"
    "v_pk_mul_f32 v[22:23], v[22:23], v[230:231]\n"
    "v_cvt_pk_bf16_f32 v24, v16, v17\n"
    "v_cvt_pk_bf16_f32 v25, v18, v19\n"
    "v_cvt_pk_bf16_f32 v26, v20, v21\n"
    "v_cvt_pk_bf16_f32 v27, v22, v23\n"
    "buffer_store_dwordx4 v[24:27], v2, s[20:23], s96 offen offset:1024 sc1\n"
    "s_add_u32 s96, s96, 114688\n"
    "v_pk_mul_f32 v[4:5], v[136:137], s[66:67]\n"
    "v_pk_mul_f32 v[6:7], v[138:139], s[66:67]\n"
    "v_pk_mul_f32 v[8:9], v[168:169], s[66:67]\n"
    "v_pk_mul_f32 v[10:11], v[170:171], s[66:67]\n"
    "v_exp_f32 v4, v4\n"
    "v_exp_f32 v5, v5\n"
    "v_exp_f32 v6, v6\n"
    "v_exp_f32 v7, v7\n"
    "v_exp_f32 v8, v8\n"
    "v_exp_f32 v9, v9\n"
    "v_exp_f32 v10, v10\n"
    "v_exp_f32 v11, v11\n"
    "v_pk_mul_f32 v[136:137], v[136:137], v[40:41]\n"
    "v_pk_mul_f32 v[138:139], v[138:139], v[42:43]\n"
    "v_pk_mul_f32 v[168:169], v[168:169], v[72:73]\n"
    "v_pk_mul_f32 v[170:171], v[170:171], v[74:75]\n"
    "v_pk_add_f32 v[4:5], v[4:5], 1.0 op_sel_hi:[1,0]\n"
    "v_pk_add_f32 v[6:7], v[6:7], 1.0 op_sel_hi:[1,0]\n"
    "v_pk_add_f32 v[8:9], v[8:9], 1.0 op_sel_hi:[1,0]\n"
    "v_pk_add_f32 v[10:11], v[10:11], 1.0 op_sel_hi:[1,0]\n"
    "v_rcp_f32 v4, v4\n"
    "v_rcp_f32 v5, v5\n"
    "v_rcp_f32 v6, v6\n"
    "v_rcp_f32 v7, v7\n"
    "v_rcp_f32 v8, v8\n"
    "v_rcp_f32 v9, v9\n"
    "v_rcp_f32 v10, v10\n"
    "v_rcp_f32 v11, v11\n"
    "s_nop 0\n"
    "v_pk_mul_f32 v[4:5], v[4:5], v[136:137]\n"
    "v_pk_mul_f32 v[6:7], v[6:7], v[138:139]\n"
    "v_pk_mul_f32 v[8:9], v[8:9], v[168:169]\n"
    "v_pk_mul_f32 v[10:11], v[10:11], v[170:171]\n"
    "v_cvt_pk_bf16_f32 v12, v4, v5\n"
    "v_cvt_pk_bf16_f32 v13, v6, v7\n"
    "v_cvt_pk_bf16_f32 v14, v8, v9\n"
    "v_cvt_pk_bf16_f32 v15, v10, v11\n"
    "buffer_store_dwordx4 v[12:15], v2, s[20:23], s96 offen offset:0 sc1\n"
    "v_pk_mul_f32 v[16:17], v[200:201], s[66:67]\n"
    "v_pk_mul_f32 v[18:19], v[202:203], s[66:67]\n"
    "v_pk_mul_f32 v[20:21], v[232:233], s[66:67]\n"
    "v_pk_mul_f32 v[22:23], v[234:235], s[66:67]\n"
    "v_exp_f32 v16, v16\n"
    "v_exp_f32 v17, v17\n"
    "v_exp_f32 v18, v18\n"
    "v_exp_f32 v19, v19\n"
    "v_exp_f32 v20, v20\n"
    "v_exp_f32 v21, v21\n"
    "v_exp_f32 v22, v22\n"
    "v_exp_f32 v23, v23\n"
    "v_pk_mul_f32 v[200:201], v[200:201], v[104:105]\n"
    "v_pk_mul_f32 v[202:203], v[202:203], v[106:107]\n"
    "v_accvgpr_read_b32 v28, a8\n"
    "v_accvgpr_read_b32 v29, a9\n"
    "v_pk_mul_f32 v[232:233], v[232:233], v[28:29]\n"
    "v_accvgpr_read_b32 v28, a10\n"
    "v_accvgpr_read_b32 v29, a11\n"
    "v_pk_mul_f32 v[234:235], v[234:235], v[28:29]\n"
    "v_pk_add_f32 v[16:17], v[16:17], 1.0 op_sel_hi:[1,0]\n"
    "v_pk_add_f32 v[18:19], v[18:19], 1.0 op_sel_hi:[1,0]\n"
    "v_pk_add_f32 v[20:21], v[20:21], 1.0 op_sel_hi:[1,0]\n"
    "v_pk_add_f32 v[22:23], v[22:23], 1.0 op_sel_hi:[1,0]\n"
    "v_rcp_f32 v16, v16\n"
    "v_rcp_f32 v17, v17\n"
    "v_rcp_f32 v18, v18\n"
    "v_rcp_f32 v19, v19\n"
    "v_rcp_f32 v20, v20\n"
    "v_rcp_f32 v21, v21\n"
    "v_rcp_f32 v22, v22\n"
    "v_rcp_f32 v23, v23\n"
    "s_nop 0\n"
    "v_pk_mul_f32 v[16:17], v[16:17], v[200:201]\n"
    "v_pk_mul_f32 v[18:19], v[18:19], v[202:203]\n"
    "v_pk_mul_f32 v[20:21], v[20:21], v[232:233]\n"
    "v_pk_mul_f32 v[22:23], v[22:23], v[234:235]\n"
    "v_cvt_pk_bf16_f32 v24, v16, v17\n"
    "v_cvt_pk_bf16_f32 v25, v18, v19\n"
    "v_cvt_pk_bf16_f32 v26, v20, v21\n"
    "v_cvt_pk_bf16_f32 v27, v22, v23\n"
    "buffer_store_dwordx4 v[24:27], v2, s[20:23], s96 offen offset:1024 sc1\n"
    "s_add_u32 s96, s96, 114688\n"
    "v_pk_mul_f32 v[4:5], v[140:141], s[66:67]\n"
    "v_pk_mul_f32 v[6:7], v[142:143], s[66:67]\n"
    "v_pk_mul_f32 v[8:9], v[172:173], s[66:67]\n"
    "v_pk_mul_f32 v[10:11], v[174:175], s[66:67]\n"
    "v_exp_f32 v4, v4\n"
    "v_exp_f32 v5, v5\n"
    "v_exp_f32 v6, v6\n"
    "v_exp_f32 v7, v7\n"
    "v_exp_f32 v8, v8\n"
    "v_exp_f32 v9, v9\n"
    "v_exp_f32 v10, v10\n"
    "v_exp_f32 v11, v11\n"
    "v_pk_mul_f32 v[140:141], v[140:141], v[44:45]\n"
    "v_pk_mul_f32 v[142:143], v[142:143], v[46:47]\n"
    "v_pk_mul_f32 v[172:173], v[172:173], v[76:77]\n"
    "v_pk_mul_f32 v[174:175], v[174:175], v[78:79]\n"
    "v_pk_add_f32 v[4:5], v[4:5], 1.0 op_sel_hi:[1,0]\n"
    "v_pk_add_f32 v[6:7], v[6:7], 1.0 op_sel_hi:[1,0]\n"
    "v_pk_add_f32 v[8:9], v[8:9], 1.0 op_sel_hi:[1,0]\n"
    "v_pk_add_f32 v[10:11], v[10:11], 1.0 op_sel_hi:[1,0]\n"
    "v_rcp_f32 v4, v4\n"
    "v_rcp_f32 v5, v5\n"
    "v_rcp_f32 v6, v6\n"
    "v_rcp_f32 v7, v7\n"
    "v_rcp_f32 v8, v8\n"
    "v_rcp_f32 v9, v9\n"
    "v_rcp_f32 v10, v10\n"
    "v_rcp_f32 v11, v11\n"
    "s_nop 0\n"
    "v_pk_mul_f32 v[4:5], v[4:5], v[140:141]\n"
    "v_pk_mul_f32 v[6:7], v[6:7], v[142:143]\n"
    "v_pk_mul_f32 v[8:9], v[8:9], v[172:173]\n"
    "v_pk_mul_f32 v[10:11], v[10:11], v[174:175]\n"
    "v_cvt_pk_bf16_f32 v12, v4, v5\n"
    "v_cvt_pk_bf16_f32 v13, v6, v7\n"
    "v_cvt_pk_bf16_f32 v14, v8, v9\n"
    "v_cvt_pk_bf16_f32 v15, v10, v11\n"
    "buffer_store_dwordx4 v[12:15], v2, s[20:23], s96 offen offset:0 sc1\n"
    "v_pk_mul_f32 v[16:17], v[204:205], s[66:67]\n"
    "v_pk_mul_f32 v[18:19], v[206:207], s[66:67]\n"
    "v_pk_mul_f32 v[20:21], v[236:237], s[66:67]\n"
    "v_pk_mul_f32 v[22:23], v[238:239], s[66:67]\n"
    "v_exp_f32 v16, v16\n"
    "v_exp_f32 v17, v17\n"
    "v_exp_f32 v18, v18\n"
    "v_exp_f32 v19, v19\n"
    "v_exp_f32 v20, v20\n"
    "v_exp_f32 v21, v21\n"
    "v_exp_f32 v22, v22\n"
    "v_exp_f32 v23, v23\n"
    "v_pk_mul_f32 v[204:205], v[204:205], v[108:109]\n"
    "v_pk_mul_f32 v[206:207], v[206:207], v[110:111]\n"
    "v_accvgpr_read_b32 v28, a12\n"
    "v_accvgpr_read_b32 v29, a13\n"
    "v_pk_mul_f32 v[236:237], v[236:237], v[28:29]\n"
    "v_accvgpr_read_b32 v28, a14\n"
    "v_accvgpr_read_b32 v29, a15\n"
    "v_pk_mul_f32 v[238:239], v[238:239], v[28:29]\n"
    "v_pk_add_f32 v[16:17], v[16:17], 1.0 op_sel_hi:[1,0]\n"
    "v_pk_add_f32 v[18:19], v[18:19], 1.0 op_sel_hi:[1,0]\n"
    "v_pk_add_f32 v[20:21], v[20:21], 1.0 op_sel_hi:[1,0]\n"
    "v_pk_add_f32 v[22:23], v[22:23], 1.0 op_sel_hi:[1,0]\n"
    "v_rcp_f32 v16, v16\n"
    "v_rcp_f32 v17, v17\n"
    "v_rcp_f32 v18, v18\n"
    "v_rcp_f32 v19, v19\n"
    "v_rcp_f32 v20, v20\n"
    "v_rcp_f32 v21, v21\n"
    "v_rcp_f32 v22, v22\n"
    "v_rcp_f32 v23, v23\n"
    "s_nop 0\n"
    "v_pk_mul_f32 v[16:17], v[16:17], v[204:205]\n"
    "v_pk_mul_f32 v[18:19], v[18:19], v[206:207]\n"
    "v_pk_mul_f32 v[20:21], v[20:21], v[236:237]\n"
    "v_pk_mul_f32 v[22:23], v[22:23], v[238:239]\n"
    "v_cvt_pk_bf16_f32 v24, v16, v17\n"
    "v_cvt_pk_bf16_f32 v25, v18, v19\n"
    "v_cvt_pk_bf16_f32 v26, v20, v21\n"
    "v_cvt_pk_bf16_f32 v27, v22, v23\n"
    "buffer_store_dwordx4 v[24:27], v2, s[20:23], s96 offen offset:1024 sc1\n"
    "s_add_u32 s96, s96, 114688\n"
    "v_pk_mul_f32 v[4:5], v[144:145], s[66:67]\n"
    "v_pk_mul_f32 v[6:7], v[146:147], s[66:67]\n"
    "v_pk_mul_f32 v[8:9], v[176:177], s[66:67]\n"
    "v_pk_mul_f32 v[10:11], v[178:179], s[66:67]\n"
    "v_exp_f32 v4, v4\n"
    "v_exp_f32 v5, v5\n"
    "v_exp_f32 v6, v6\n"
    "v_exp_f32 v7, v7\n"
    "v_exp_f32 v8, v8\n"
    "v_exp_f32 v9, v9\n"
    "v_exp_f32 v10, v10\n"
    "v_exp_f32 v11, v11\n"
    "v_pk_mul_f32 v[144:145], v[144:145], v[48:49]\n"
    "v_pk_mul_f32 v[146:147], v[146:147], v[50:51]\n"
    "v_pk_mul_f32 v[176:177], v[176:177], v[80:81]\n"
    "v_pk_mul_f32 v[178:179], v[178:179], v[82:83]\n"
    "v_pk_add_f32 v[4:5], v[4:5], 1.0 op_sel_hi:[1,0]\n"
    "v_pk_add_f32 v[6:7], v[6:7], 1.0 op_sel_hi:[1,0]\n"
    "v_pk_add_f32 v[8:9], v[8:9], 1.0 op_sel_hi:[1,0]\n"
    "v_pk_add_f32 v[10:11], v[10:11], 1.0 op_sel_hi:[1,0]\n"
    "v_rcp_f32 v4, v4\n"
    "v_rcp_f32 v5, v5\n"
    "v_rcp_f32 v6, v6\n"
    "v_rcp_f32 v7, v7\n"
    "v_rcp_f32 v8, v8\n"
    "v_rcp_f32 v9, v9\n"
    "v_rcp_f32 v10, v10\n"
    "v_rcp_f32 v11, v11\n"
    "s_nop 0\n"
    "v_pk_mul_f32 v[4:5], v[4:5], v[144:145]\n"
    "v_pk_mul_f32 v[6:7], v[6:7], v[146:147]\n"
    "v_pk_mul_f32 v[8:9], v[8:9], v[176:177]\n"
    "v_pk_mul_f32 v[10:11], v[10:11], v[178:179]\n"
    "v_cvt_pk_bf16_f32 v12, v4, v5\n"
    "v_cvt_pk_bf16_f32 v13, v6, v7\n"
    "v_cvt_pk_bf16_f32 v14, v8, v9\n"
    "v_cvt_pk_bf16_f32 v15, v10, v11\n"
    "buffer_store_dwordx4 v[12:15], v2, s[20:23], s96 offen offset:0 sc1\n"
    "v_pk_mul_f32 v[16:17], v[208:209], s[66:67]\n"
    "v_pk_mul_f32 v[18:19], v[210:211], s[66:67]\n"
    "v_pk_mul_f32 v[20:21], v[240:241], s[66:67]\n"
    "v_pk_mul_f32 v[22:23], v[242:243], s[66:67]\n"
    "v_exp_f32 v16, v16\n"
    "v_exp_f32 v17, v17\n"
    "v_exp_f32 v18, v18\n"
    "v_exp_f32 v19, v19\n"
    "v_exp_f32 v20, v20\n"
    "v_exp_f32 v21, v21\n"
    "v_exp_f32 v22, v22\n"
    "v_exp_f32 v23, v23\n"
    "v_pk_mul_f32 v[208:209], v[208:209], v[112:113]\n"
    "v_pk_mul_f32 v[210:211], v[210:211], v[114:115]\n"
    "v_accvgpr_read_b32 v28, a16\n"
    "v_accvgpr_read_b32 v29, a17\n"
    "v_pk_mul_f32 v[240:241], v[240:241], v[28:29]\n"
    "v_accvgpr_read_b32 v28, a18\n"
    "v_accvgpr_read_b32 v29, a19\n"
    "v_pk_mul_f32 v[242:243], v[242:243], v[28:29]\n"
    "v_pk_add_f32 v[16:17], v[16:17], 1.0 op_sel_hi:[1,0]\n"
    "v_pk_add_f32 v[18:19], v[18:19], 1.0 op_sel_hi:[1,0]\n"
    "v_pk_add_f32 v[20:21], v[20:21], 1.0 op_sel_hi:[1,0]\n"
    "v_pk_add_f32 v[22:23], v[22:23], 1.0 op_sel_hi:[1,0]\n"
    "v_rcp_f32 v16, v16\n"
    "v_rcp_f32 v17, v17\n"
    "v_rcp_f32 v18, v18\n"
    "v_rcp_f32 v19, v19\n"
    "v_rcp_f32 v20, v20\n"
    "v_rcp_f32 v21, v21\n"
    "v_rcp_f32 v22, v22\n"
    "v_rcp_f32 v23, v23\n"
    "s_nop 0\n"
    "v_pk_mul_f32 v[16:17], v[16:17], v[208:209]\n"
    "v_pk_mul_f32 v[18:19], v[18:19], v[210:211]\n"
    "v_pk_mul_f32 v[20:21], v[20:21], v[240:241]\n"
    "v_pk_mul_f32 v[22:23], v[22:23], v[242:243]\n"
    "v_cvt_pk_bf16_f32 v24, v16, v17\n"
    "v_cvt_pk_bf16_f32 v25, v18, v19\n"
    "v_cvt_pk_bf16_f32 v26, v20, v21\n"
    "v_cvt_pk_bf16_f32 v27, v22, v23\n"
    "buffer_store_dwordx4 v[24:27], v2, s[20:23], s96 offen offset:1024 sc1\n"
    "s_add_u32 s96, s96, 114688\n"
    "v_pk_mul_f32 v[4:5], v[148:149], s[66:67]\n"
    "v_pk_mul_f32 v[6:7], v[150:151], s[66:67]\n"
    "v_pk_mul_f32 v[8:9], v[180:181], s[66:67]\n"
    "v_pk_mul_f32 v[10:11], v[182:183], s[66:67]\n"
    "v_exp_f32 v4, v4\n"
    "v_exp_f32 v5, v5\n"
    "v_exp_f32 v6, v6\n"
    "v_exp_f32 v7, v7\n"
    "v_exp_f32 v8, v8\n"
    "v_exp_f32 v9, v9\n"
    "v_exp_f32 v10, v10\n"
    "v_exp_f32 v11, v11\n"
    "v_pk_mul_f32 v[148:149], v[148:149], v[52:53]\n"
    "v_pk_mul_f32 v[150:151], v[150:151], v[54:55]\n"
    "v_pk_mul_f32 v[180:181], v[180:181], v[84:85]\n"
    "v_pk_mul_f32 v[182:183], v[182:183], v[86:87]\n"
    "v_pk_add_f32 v[4:5], v[4:5], 1.0 op_sel_hi:[1,0]\n"
    "v_pk_add_f32 v[6:7], v[6:7], 1.0 op_sel_hi:[1,0]\n"
    "v_pk_add_f32 v[8:9], v[8:9], 1.0 op_sel_hi:[1,0]\n"
    "v_pk_add_f32 v[10:11], v[10:11], 1.0 op_sel_hi:[1,0]\n"
    "v_rcp_f32 v4, v4\n"
    "v_rcp_f32 v5, v5\n"
    "v_rcp_f32 v6, v6\n"
    "v_rcp_f32 v7, v7\n"
    "v_rcp_f32 v8, v8\n"
    "v_rcp_f32 v9, v9\n"
    "v_rcp_f32 v10, v10\n"
    "v_rcp_f32 v11, v11\n"
    "s_nop 0\n"
    "v_pk_mul_f32 v[4:5], v[4:5], v[148:149]\n"
    "v_pk_mul_f32 v[6:7], v[6:7], v[150:151]\n"
    "v_pk_mul_f32 v[8:9], v[8:9], v[180:181]\n"
    "v_pk_mul_f32 v[10:11], v[10:11], v[182:183]\n"
    "v_cvt_pk_bf16_f32 v12, v4, v5\n"
    "v_cvt_pk_bf16_f32 v13, v6, v7\n"
    "v_cvt_pk_bf16_f32 v14, v8, v9\n"
    "v_cvt_pk_bf16_f32 v15, v10, v11\n"
    "buffer_store_dwordx4 v[12:15], v2, s[20:23], s96 offen offset:0 sc1\n"
    "v_pk_mul_f32 v[16:17], v[212:213], s[66:67]\n"
    "v_pk_mul_f32 v[18:19], v[214:215], s[66:67]\n"
    "v_pk_mul_f32 v[20:21], v[244:245], s[66:67]\n"
    "v_pk_mul_f32 v[22:23], v[246:247], s[66:67]\n"
    "v_exp_f32 v16, v16\n"
    "v_exp_f32 v17, v17\n"
    "v_exp_f32 v18, v18\n"
    "v_exp_f32 v19, v19\n"
    "v_exp_f32 v20, v20\n"
    "v_exp_f32 v21, v21\n"
    "v_exp_f32 v22, v22\n"
    "v_exp_f32 v23, v23\n"
    "v_pk_mul_f32 v[212:213], v[212:213], v[116:117]\n"
    "v_pk_mul_f32 v[214:215], v[214:215], v[118:119]\n"
    "v_accvgpr_read_b32 v28, a20\n"
    "v_accvgpr_read_b32 v29, a21\n"
    "v_pk_mul_f32 v[244:245], v[244:245], v[28:29]\n"
    "v_accvgpr_read_b32 v28, a22\n"
    "v_accvgpr_read_b32 v29, a23\n"
    "v_pk_mul_f32 v[246:247], v[246:247], v[28:29]\n"
    "v_pk_add_f32 v[16:17], v[16:17], 1.0 op_sel_hi:[1,0]\n"
    "v_pk_add_f32 v[18:19], v[18:19], 1.0 op_sel_hi:[1,0]\n"
    "v_pk_add_f32 v[20:21], v[20:21], 1.0 op_sel_hi:[1,0]\n"
    "v_pk_add_f32 v[22:23], v[22:23], 1.0 op_sel_hi:[1,0]\n"
    "v_rcp_f32 v16, v16\n"
    "v_rcp_f32 v17, v17\n"
    "v_rcp_f32 v18, v18\n"
    "v_rcp_f32 v19, v19\n"
    "v_rcp_f32 v20, v20\n"
    "v_rcp_f32 v21, v21\n"
    "v_rcp_f32 v22, v22\n"
    "v_rcp_f32 v23, v23\n"
    "s_nop 0\n"
    "v_pk_mul_f32 v[16:17], v[16:17], v[212:213]\n"
    "v_pk_mul_f32 v[18:19], v[18:19], v[214:215]\n"
    "v_pk_mul_f32 v[20:21], v[20:21], v[244:245]\n"
    "v_pk_mul_f32 v[22:23], v[22:23], v[246:247]\n"
    "v_cvt_pk_bf16_f32 v24, v16, v17\n"
    "v_cvt_pk_bf16_f32 v25, v18, v19\n"
    "v_cvt_pk_bf16_f32 v26, v20, v21\n"
    "v_cvt_pk_bf16_f32 v27, v22, v23\n"
    "buffer_store_dwordx4 v[24:27], v2, s[20:23], s96 offen offset:1024 sc1\n"
    "s_add_u32 s96, s96, 114688\n"
    "v_pk_mul_f32 v[4:5], v[152:153], s[66:67]\n"
    "v_pk_mul_f32 v[6:7], v[154:155], s[66:67]\n"
    "v_pk_mul_f32 v[8:9], v[184:185], s[66:67]\n"
    "v_pk_mul_f32 v[10:11], v[186:187], s[66:67]\n"
    "v_exp_f32 v4, v4\n"
    "v_exp_f32 v5, v5\n"
    "v_exp_f32 v6, v6\n"
    "v_exp_f32 v7, v7\n"
    "v_exp_f32 v8, v8\n"
    "v_exp_f32 v9, v9\n"
    "v_exp_f32 v10, v10\n"
    "v_exp_f32 v11, v11\n"
    "v_pk_mul_f32 v[152:153], v[152:153], v[56:57]\n"
    "v_pk_mul_f32 v[154:155], v[154:155], v[58:59]\n"
    "v_pk_mul_f32 v[184:185], v[184:185], v[88:89]\n"
    "v_pk_mul_f32 v[186:187], v[186:187], v[90:91]\n"
    "v_pk_add_f32 v[4:5], v[4:5], 1.0 op_sel_hi:[1,0]\n"
    "v_pk_add_f32 v[6:7], v[6:7], 1.0 op_sel_hi:[1,0]\n"
    "v_pk_add_f32 v[8:9], v[8:9], 1.0 op_sel_hi:[1,0]\n"
    "v_pk_add_f32 v[10:11], v[10:11], 1.0 op_sel_hi:[1,0]\n"
    "v_rcp_f32 v4, v4\n"
    "v_rcp_f32 v5, v5\n"
    "v_rcp_f32 v6, v6\n"
    "v_rcp_f32 v7, v7\n"
    "v_rcp_f32 v8, v8\n"
    "v_rcp_f32 v9, v9\n"
    "v_rcp_f32 v10, v10\n"
    "v_rcp_f32 v11, v11\n"
    "s_nop 0\n"
    "v_pk_mul_f32 v[4:5], v[4:5], v[152:153]\n"
    "v_pk_mul_f32 v[6:7], v[6:7], v[154:155]\n"
    "v_pk_mul_f32 v[8:9], v[8:9], v[184:185]\n"
    "v_pk_mul_f32 v[10:11], v[10:11], v[186:187]\n"
    "v_cvt_pk_bf16_f32 v12, v4, v5\n"
    "v_cvt_pk_bf16_f32 v13, v6, v7\n"
    "v_cvt_pk_bf16_f32 v14, v8, v9\n"
    "v_cvt_pk_bf16_f32 v15, v10, v11\n"
    "buffer_store_dwordx4 v[12:15], v2, s[20:23], s96 offen offset:0 sc1\n"
    "v_pk_mul_f32 v[16:17], v[216:217], s[66:67]\n"
    "v_pk_mul_f32 v[18:19], v[218:219], s[66:67]\n"
    "v_pk_mul_f32 v[20:21], v[248:249], s[66:67]\n"
    "v_pk_mul_f32 v[22:23], v[250:251], s[66:67]\n"
    "v_exp_f32 v16, v16\n"
    "v_exp_f32 v17, v17\n"
    "v_exp_f32 v18, v18\n"
    "v_exp_f32 v19, v19\n"
    "v_exp_f32 v20, v20\n"
    "v_exp_f32 v21, v21\n"
    "v_exp_f32 v22, v22\n"
    "v_exp_f32 v23, v23\n"
    "v_pk_mul_f32 v[216:217], v[216:217], v[120:121]\n"
    "v_pk_mul_f32 v[218:219], v[218:219], v[122:123]\n"
    "v_accvgpr_read_b32 v28, a24\n"
    "v_accvgpr_read_b32 v29, a25\n"
    "v_pk_mul_f32 v[248:249], v[248:249], v[28:29]\n"
    "v_accvgpr_read_b32 v28, a26\n"
    "v_accvgpr_read_b32 v29, a27\n"
    "v_pk_mul_f32 v[250:251], v[250:251], v[28:29]\n"
    "v_pk_add_f32 v[16:17], v[16:17], 1.0 op_sel_hi:[1,0]\n"
    "v_pk_add_f32 v[18:19], v[18:19], 1.0 op_sel_hi:[1,0]\n"
    "v_pk_add_f32 v[20:21], v[20:21], 1.0 op_sel_hi:[1,0]\n"
    "v_pk_add_f32 v[22:23], v[22:23], 1.0 op_sel_hi:[1,0]\n"
    "v_rcp_f32 v16, v16\n"
    "v_rcp_f32 v17, v17\n"
    "v_rcp_f32 v18, v18\n"
    "v_rcp_f32 v19, v19\n"
    "v_rcp_f32 v20, v20\n"
    "v_rcp_f32 v21, v21\n"
    "v_rcp_f32 v22, v22\n"
    "v_rcp_f32 v23, v23\n"
    "s_nop 0\n"
    "v_pk_mul_f32 v[16:17], v[16:17], v[216:217]\n"
    "v_pk_mul_f32 v[18:19], v[18:19], v[218:219]\n"
    "v_pk_mul_f32 v[20:21], v[20:21], v[248:249]\n"
    "v_pk_mul_f32 v[22:23], v[22:23], v[250:251]\n"
    "v_cvt_pk_bf16_f32 v24, v16, v17\n"
    "v_cvt_pk_bf16_f32 v25, v18, v19\n"
    "v_cvt_pk_bf16_f32 v26, v20, v21\n"
    "v_cvt_pk_bf16_f32 v27, v22, v23\n"
    "buffer_store_dwordx4 v[24:27], v2, s[20:23], s96 offen offset:1024 sc1\n"
    "s_add_u32 s96, s96, 114688\n"
    "v_pk_mul_f32 v[4:5], v[156:157], s[66:67]\n"
    "v_pk_mul_f32 v[6:7], v[158:159], s[66:67]\n"
    "v_pk_mul_f32 v[8:9], v[188:189], s[66:67]\n"
    "v_pk_mul_f32 v[10:11], v[190:191], s[66:67]\n"
    "v_exp_f32 v4, v4\n"
    "v_exp_f32 v5, v5\n"
    "v_exp_f32 v6, v6\n"
    "v_exp_f32 v7, v7\n"
    "v_exp_f32 v8, v8\n"
    "v_exp_f32 v9, v9\n"
    "v_exp_f32 v10, v10\n"
    "v_exp_f32 v11, v11\n"
    "v_pk_mul_f32 v[156:157], v[156:157], v[60:61]\n"
    "v_pk_mul_f32 v[158:159], v[158:159], v[62:63]\n"
    "v_pk_mul_f32 v[188:189], v[188:189], v[92:93]\n"
    "v_pk_mul_f32 v[190:191], v[190:191], v[94:95]\n"
    "v_pk_add_f32 v[4:5], v[4:5], 1.0 op_sel_hi:[1,0]\n"
    "v_pk_add_f32 v[6:7], v[6:7], 1.0 op_sel_hi:[1,0]\n"
    "v_pk_add_f32 v[8:9], v[8:9], 1.0 op_sel_hi:[1,0]\n"
    "v_pk_add_f32 v[10:11], v[10:11], 1.0 op_sel_hi:[1,0]\n"
    "v_rcp_f32 v4, v4\n"
    "v_rcp_f32 v5, v5\n"
    "v_rcp_f32 v6, v6\n"
    "v_rcp_f32 v7, v7\n"
    "v_rcp_f32 v8, v8\n"
    "v_rcp_f32 v9, v9\n"
    "v_rcp_f32 v10, v10\n"
    "v_rcp_f32 v11, v11\n"
    "s_nop 0\n"
    "v_pk_mul_f32 v[4:5], v[4:5], v[156:157]\n"
    "v_pk_mul_f32 v[6:7], v[6:7], v[158:159]\n"
    "v_pk_mul_f32 v[8:9], v[8:9], v[188:189]\n"
    "v_pk_mul_f32 v[10:11], v[10:11], v[190:191]\n"
    "v_cvt_pk_bf16_f32 v12, v4, v5\n"
    "v_cvt_pk_bf16_f32 v13, v6, v7\n"
    "v_cvt_pk_bf16_f32 v14, v8, v9\n"
    "v_cvt_pk_bf16_f32 v15, v10, v11\n"
    "buffer_store_dwordx4 v[12:15], v2, s[20:23], s96 offen offset:0 sc1\n"
    "v_pk_mul_f32 v[16:17], v[220:221], s[66:67]\n"
    "v_pk_mul_f32 v[18:19], v[222:223], s[66:67]\n"
    "v_pk_mul_f32 v[20:21], v[252:253], s[66:67]\n"
    "v_pk_mul_f32 v[22:23], v[254:255], s[66:67]\n"
    "v_exp_f32 v16, v16\n"
    "v_exp_f32 v17, v17\n"
    "v_exp_f32 v18, v18\n"
    "v_exp_f32 v19, v19\n"
    "v_exp_f32 v20, v20\n"
    "v_exp_f32 v21, v21\n"
    "v_exp_f32 v22, v22\n"
    "v_exp_f32 v23, v23\n"
    "v_pk_mul_f32 v[220:221], v[220:221], v[124:125]\n"
    "v_pk_mul_f32 v[222:223], v[222:223], v[126:127]\n"
    "v_accvgpr_read_b32 v28, a28\n"
    "v_accvgpr_read_b32 v29, a29\n"
    "v_pk_mul_f32 v[252:253], v[252:253], v[28:29]\n"
    "v_accvgpr_read_b32 v28, a30\n"
    "v_accvgpr_read_b32 v29, a31\n"
    "v_pk_mul_f32 v[254:255], v[254:255], v[28:29]\n"
    "v_pk_add_f32 v[16:17], v[16:17], 1.0 op_sel_hi:[1,0]\n"
    "v_pk_add_f32 v[18:19], v[18:19], 1.0 op_sel_hi:[1,0]\n"
    "v_pk_add_f32 v[20:21], v[20:21], 1.0 op_sel_hi:[1,0]\n"
    "v_pk_add_f32 v[22:23], v[22:23], 1.0 op_sel_hi:[1,0]\n"
    "v_rcp_f32 v16, v16\n"
    "v_rcp_f32 v17, v17\n"
    "v_rcp_f32 v18, v18\n"
    "v_rcp_f32 v19, v19\n"
    "v_rcp_f32 v20, v20\n"
    "v_rcp_f32 v21, v21\n"
    "v_rcp_f32 v22, v22\n"
    "v_rcp_f32 v23, v23\n"
    "s_nop 0\n"
    "v_pk_mul_f32 v[16:17], v[16:17], v[220:221]\n"
    "v_pk_mul_f32 v[18:19], v[18:19], v[222:223]\n"
    "v_pk_mul_f32 v[20:21], v[20:21], v[252:253]\n"
    "v_pk_mul_f32 v[22:23], v[22:23], v[254:255]\n"
    "v_cvt_pk_bf16_f32 v24, v16, v17\n"
    "v_cvt_pk_bf16_f32 v25, v18, v19\n"
    "v_cvt_pk_bf16_f32 v26, v20, v21\n"
    "v_cvt_pk_bf16_f32 v27, v22, v23\n"
    "buffer_store_dwordx4 v[24:27], v2, s[20:23], s96 offen offset:1024 sc1\n"
    "s_add_u32 s96, s96, 114688\n"
    "JOIN_gated_mix__ssm_conv_h3__swiglu_ffn__prompt_kernel:\n"
    "s_waitcnt vmcnt(16)\n"
    "s_and_b32 s68, s34, 1\n"
    "v_and_b32 v72, 7, v1\n"
    "v_bfe_u32 v73, v1, 3, 1\n"
    "s_mul_i32 s30, s68, 8320\n"
    "s_add_u32 s30, s30, s73\n"
    "v_mul_u32_u24 v73, 4160, v73\n"
    "v_add_u32 v73, s30, v73\n"
    "v_lshl_add_u32 v75, v72, 2, v73\n"
    "v_lshlrev_b32 v74, 4, v1\n"
    "v_add_u32 v74, s30, v74\n"
    "v_min_u32 v76, 59, v1\n"
    "v_lshlrev_b32 v76, 3, v76\n"
    "s_add_u32 s31, s73, 16640\n"
    "v_add_u32 v76, s31, v76\n"
    "v_mov_b32 v78, 0\n"
    "v_mov_b32 v79, 0\n"
    "ds_write_b32 v75, v78\n"
    "ds_write_b64 v76, v[78:79]\n"
    "s_lshl_b32 s30, s68, 11\n"
    "s_add_u32 s30, s30, s31\n"
    "v_lshlrev_b32 v77, 4, v1\n"
    "v_add_u32 v77, s30, v77\n"
    "ds_write_b128 v77, a[32:35] offset:480\n"
    "ds_write_b128 v77, a[36:39] offset:1504\n"
    "v_accvgpr_read_b32 v124, a52\n"
    "v_accvgpr_read_b32 v125, a53\n"
    "v_accvgpr_read_b32 v126, a54\n"
    "v_accvgpr_read_b32 v127, a55\n"
    "v_accvgpr_read_b32 v128, a56\n"
    "v_accvgpr_read_b32 v129, a57\n"
    "v_accvgpr_read_b32 v130, a58\n"
    "v_accvgpr_read_b32 v131, a59\n"
    "v_accvgpr_read_b32 v132, a60\n"
    "v_accvgpr_read_b32 v133, a61\n"
    "v_accvgpr_read_b32 v134, a62\n"
    "v_accvgpr_read_b32 v135, a63\n"
    "v_accvgpr_read_b32 v136, a64\n"
    "v_accvgpr_read_b32 v137, a65\n"
    "v_accvgpr_read_b32 v138, a66\n"
    "v_accvgpr_read_b32 v139, a67\n"
    "v_accvgpr_read_b32 v140, a68\n"
    "v_accvgpr_read_b32 v141, a69\n"
    "v_accvgpr_read_b32 v142, a70\n"
    "v_accvgpr_read_b32 v143, a71\n"
    "v_accvgpr_read_b32 v144, a72\n"
    "v_accvgpr_read_b32 v145, a73\n"
    "v_accvgpr_read_b32 v146, a74\n"
    "v_accvgpr_read_b32 v147, a75\n"
    "s_cmp_lg_u32 s68, 0\n"
    "s_cbranch_scc1 BC1_gated_mix__ssm_conv_h3__swiglu_ffn__prompt_kernel\n"
    "ds_write_b128 v74, v[126:129] offset:32\n"
    "v_alignbit_b32 v32, v126, v125, 16\n"
    "v_alignbit_b32 v33, v127, v126, 16\n"
    "v_alignbit_b32 v34, v128, v127, 16\n"
    "v_alignbit_b32 v35, v129, v128, 16\n"
    "ds_write_b128 v74, v[32:35] offset:4192\n"
    "ds_write_b128 v74, v[132:135] offset:1056\n"
    "v_alignbit_b32 v36, v132, v131, 16\n"
    "v_alignbit_b32 v37, v133, v132, 16\n"
    "v_alignbit_b32 v38, v134, v133, 16\n"
    "v_alignbit_b32 v39, v135, v134, 16\n"
    "ds_write_b128 v74, v[36:39] offset:5216\n"
    "ds_write_b128 v74, v[138:141] offset:2080\n"
    "v_alignbit_b32 v40, v138, v137, 16\n"
    "v_alignbit_b32 v41, v139, v138, 16\n"
    "v_alignbit_b32 v42, v140, v139, 16\n"
    "v_alignbit_b32 v43, v141, v140, 16\n"
    "ds_write_b128 v74, v[40:43] offset:6240\n"
    "ds_write_b128 v74, v[144:147] offset:3104\n"
    "v_alignbit_b32 v44, v144, v143, 16\n"
    "v_alignbit_b32 v45, v145, v144, 16\n"
    "v_alignbit_b32 v46, v146, v145, 16\n"
    "v_alignbit_b32 v47, v147, v146, 16\n"
    "ds_write_b128 v74, v[44:47] offset:7264\n"
    "s_branch BC2_gated_mix__ssm_conv_h3__swiglu_ffn__prompt_kernel\n"
    "BC1_gated_mix__ssm_conv_h3__swiglu_ffn__prompt_kernel:\n"
    "v_mov_b32 v32, v125\n"
    "v_mov_b32 v33, v126\n"
    "v_mov_b32 v34, v127\n"
    "v_mov_b32 v35, v128\n"
    "ds_write_b128 v74, v[32:35] offset:32\n"
    "v_alignbit_b32 v36, v125, v124, 16\n"
    "v_alignbit_b32 v37, v126, v125, 16\n"
    "v_alignbit_b32 v38, v127, v126, 16\n"
    "v_alignbit_b32 v39, v128, v127, 16\n"
    "ds_write_b128 v74, v[36:39] offset:4192\n"
    "v_mov_b32 v40, v131\n"
    "v_mov_b32 v41, v132\n"
    "v_mov_b32 v42, v133\n"
    "v_mov_b32 v43, v134\n"
    "ds_write_b128 v74, v[40:43] offset:1056\n"
    "v_alignbit_b32 v44, v131, v130, 16\n"
    "v_alignbit_b32 v45, v132, v131, 16\n"
    "v_alignbit_b32 v46, v133, v132, 16\n"
    "v_alignbit_b32 v47, v134, v133, 16\n"
    "ds_write_b128 v74, v[44:47] offset:5216\n"
    "v_mov_b32 v48, v137\n"
    "v_mov_b32 v49, v138\n"
    "v_mov_b32 v50, v139\n"
    "v_mov_b32 v51, v140\n"
    "ds_write_b128 v74, v[48:51] offset:2080\n"
    "v_alignbit_b32 v52, v137, v136, 16\n"
    "v_alignbit_b32 v53, v138, v137, 16\n"
    "v_alignbit_b32 v54, v139, v138, 16\n"
    "v_alignbit_b32 v55, v140, v139, 16\n"
    "ds_write_b128 v74, v[52:55] offset:6240\n"
    "v_mov_b32 v56, v143\n"
    "v_mov_b32 v57, v144\n"
    "v_mov_b32 v58, v145\n"
    "v_mov_b32 v59, v146\n"
    "ds_write_b128 v74, v[56:59] offset:3104\n"
    "v_alignbit_b32 v60, v143, v142, 16\n"
    "v_alignbit_b32 v61, v144, v143, 16\n"
    "v_alignbit_b32 v62, v145, v144, 16\n"
    "v_alignbit_b32 v63, v146, v145, 16\n"
    "ds_write_b128 v74, v[60:63] offset:7264\n"
    "BC2_gated_mix__ssm_conv_h3__swiglu_ffn__prompt_kernel:\n"
    "s_lshl_b32 s30, s68, 8\n"
    "s_add_u32 s66, s73, s30\n"
    "s_sub_u32 s67, s31, s30\n"
    "v_bfe_u32 v72, v1, 2, 2\n"
    "v_mul_u32_u24 v64, 4160, v72\n"
    "v_and_b32 v72, 3, v1\n"
    "v_lshl_add_u32 v64, v72, 3, v64\n"
    "v_lshrrev_b32 v72, 4, v1\n"
    "v_lshlrev_b32 v72, 4, v72\n"
    "v_sub_u32 v64, v64, v72\n"
    "v_add_u32 v64, s66, v64\n"
    "v_add_u32 v64, 56, v64\n"
    "v_and_b32 v65, 15, v1\n"
    "v_lshl_add_u32 v65, v65, 5, v72\n"
    "v_add_u32 v65, s67, v65\n"
    "v_lshrrev_b32 v72, 4, v1\n"
    "v_bfe_u32 v73, v1, 3, 1\n"
    "v_add_u32 v73, 2, v73\n"
    "v_cmp_eq_u32 vcc, v72, v73\n"
    "v_bfe_u32 v72, v1, 1, 2\n"
    "v_mov_b32 v73, 9\n"
    "v_cndmask_b32 v72, v73, v72, vcc\n"
    "v_and_b32 v73, 1, v1\n"
    "v_lshlrev_b32 v73, 4, v73\n"
    "v_mov_b32 v74, 0xffff\n"
    "v_lshlrev_b32 v73, v73, v74\n"
    "v_cmp_eq_u32 vcc, 0, v72\n"
    "s_nop 1\n"
    "v_cndmask_b32 v68, 0, v73, vcc\n"
    "v_cmp_eq_u32 vcc, 1, v72\n"
    "s_nop 1\n"
    "v_cndmask_b32 v69, 0, v73, vcc\n"
    "v_cmp_eq_u32 vcc, 2, v72\n"
    "s_nop 1\n"
    "v_cndmask_b32 v70, 0, v73, vcc\n"
    "v_cmp_eq_u32 vcc, 3, v72\n"
    "s_nop 1\n"
    "v_cndmask_b32 v71, 0, v73, vcc\n"
    "s_bitcmp1_b32 s72, 0\n"
    "s_cselect_b32 s30, 16, 0\n"
    "s_lshr_b32 s69, s75, s30\n"
    "s_and_b32 s69, s69, 0xffff\n"
    "s_mul_i32 s69, s69, 0x10001\n"
    "s_cmp_eq_u32 s68, 0\n"
    "s_cselect_b32 s69, s69, 0\n"
    "v_and_b32 v68, s69, v68\n"
    "v_and_b32 v69, s69, v69\n"
    "v_and_b32 v70, s69, v70\n"
    "v_and_b32 v71, s69, v71\n"
    "s_waitcnt vmcnt(0) lgkmcnt(0)\n"
    "s_barrier\n"
    "s_cmp_lg_u32 s34, 0\n"
    "s_cbranch_scc1 NOHF_gated_mix__ssm_conv_h3__swiglu_ffn__prompt_kernel\n"
    "v_mov_b32 v72, s70\n"
    "v_mov_b32 v73, s71\n"
    "v_mov_b32 v74, 0\n"
    "s_mov_b64 exec, 1\n"
    "buffer_store_dwordx2 v[72:73], v74, s[20:23], s74 offen sc1\n"
    "s_mov_b64 exec, -1\n"
    "NOHF_gated_mix__ssm_conv_h3__swiglu_ffn__prompt_kernel:\n"
    "ds_read_b64_tr_b16 a[128:129], v64 offset:8\n"
    "ds_read_b64_tr_b16 a[130:131], v64 offset:0\n"
    "ds_read_b128 a[160:163], v65 offset:448\n"
    "ds_read_b128 a[164:167], v65 offset:960\n"
    "ds_read_b128 a[168:171], v65 offset:1472\n"
    "ds_read_b128 a[172:175], v65 offset:1984\n"
    "ds_read_b128 a[176:179], v65 offset:2496\n"
    "ds_read_b128 a[180:183], v65 offset:3008\n"
    "ds_read_b128 a[184:187], v65 offset:3520\n"
    "ds_read_b128 a[188:191], v65 offset:4032\n"
    "ds_read_b64_tr_b16 a[132:133], v64 offset:520\n"
    "ds_read_b64_tr_b16 a[134:135], v64 offset:512\n"
    "ds_read_b64_tr_b16 a[136:137], v64 offset:1032\n"
    "ds_read_b64_tr_b16 a[138:139], v64 offset:1024\n"
    "ds_read_b64_tr_b16 a[140:141], v64 offset:1544\n"
    "ds_read_b64_tr_b16 a[142:143], v64 offset:1536\n"
    "ds_read_b64_tr_b16 a[144:145], v64 offset:2056\n"
    "ds_read_b64_tr_b16 a[146:147], v64 offset:2048\n"
    "ds_read_b64_tr_b16 a[148:149], v64 offset:2568\n"
    "ds_read_b64_tr_b16 a[150:151], v64 offset:2560\n"
    "ds_read_b64_tr_b16 a[152:153], v64 offset:3080\n"
    "ds_read_b64_tr_b16 a[154:155], v64 offset:3072\n"
    "ds_read_b64_tr_b16 a[156:157], v64 offset:3592\n"
    "ds_read_b64_tr_b16 a[158:159], v64 offset:3584\n"
    "s_bfe_u32 s37, s39, 0x20001\n"
    "s_and_b32 s35, s39, 1\n"
    "s_lshl_b32 s35, s35, 2\n"
    "s_bfe_u32 s30, s39, 0x20003\n"
    "s_add_u32 s35, s35, s30\n"
    "s_lshr_b32 s36, s39, 5\n"
    "s_mul_i32 s25, s37, 1835008\n"
    "s_lshl_b32 s30, s36, 8\n"
    "s_add_u32 s25, s25, s30\n"
    "s_add_u32 s96, s25, 1835008\n"
    "s_cmp_eq_u32 s36, 7\n"
    "s_cselect_b32 s27, 3, s36\n"
    "s_mul_i32 s27, s27, 0\n"
    "s_lshl_b32 s30, s27, 18\n"
    "s_add_u32 s25, s25, s30\n"
    "s_lshl_b32 s28, s34, 12\n"
    "v_lshrrev_b32 v38, 4, v1\n"
    "s_lshl_b32 s30, s34, 4\n"
    "v_add_u32 v39, s30, v38\n"
    "v_lshlrev_b32 v39, 11, v39\n"
    "v_and_b32 v40, 1, v1\n"
    "v_lshl_add_u32 v39, v40, 4, v39\n"
    "v_bfe_u32 v40, v1, 1, 3\n"
    "v_xor_b32 v38, v38, v40\n"
    "v_lshl_add_u32 v22, v38, 5, v39\n"
    "v_xor_b32 v38, 4, v38\n"
    "v_lshl_add_u32 v23, v38, 5, v39\n"
    "v_add_u32 v24, 14336, v22\n"
    "v_add_u32 v25, 21504, v23\n"
    "v_add_u32 v23, 7168, v23\n"
    "s_waitcnt lgkmcnt(0)\n"
    "v_mfma_f32_16x16x32_bf16 v[128:131], v[68:71], a[160:163], 0\n"
    "v_mfma_f32_16x16x32_bf16 v[132:135], v[68:71], a[164:167], 0\n"
    "v_mfma_f32_16x16x32_bf16 v[136:139], v[68:71], a[168:171], 0\n"
    "v_mfma_f32_16x16x32_bf16 v[140:143], v[68:71], a[172:175], 0\n"
    "v_mfma_f32_16x16x32_bf16 v[144:147], v[68:71], a[176:179], 0\n"
    "v_mfma_f32_16x16x32_bf16 v[148:151], v[68:71], a[180:183], 0\n"
    "v_mfma_f32_16x16x32_bf16 v[152:155], v[68:71], a[184:187], 0\n"
    "v_mfma_f32_16x16x32_bf16 v[156:159], v[68:71], a[188:191], 0\n"
    "v_mfma_f32_16x16x32_bf16 v[128:131], a[128:131], a[160:163], v[128:131]\n"
    "ds_read_b64_tr_b16 a[192:193], v64 offset:72\n"
    "v_mfma_f32_16x16x32_bf16 v[132:135], a[128:131], a[164:167], v[132:135]\n"
    "ds_read_b64_tr_b16 a[194:195], v64 offset:64\n"
    "v_mfma_f32_16x16x32_bf16 v[136:139], a[128:131], a[168:171], v[136:139]\n"
    "ds_read_b128 a[224:227], v65 offset:384\n"
    "v_mfma_f32_16x16x32_bf16 v[140:143], a[128:131], a[172:175], v[140:143]\n"
    "s_mul_i32 s30, s35, 28\n"
    "s_add_u32 m0, s28, 0\n"
    "s_nop 0\n"
    "ds_read_b128 a[228:231], v65 offset:896\n"
    "v_mfma_f32_16x16x32_bf16 v[144:147], a[128:131], a[176:179], v[144:147]\n"
    "ds_read_b128 a[232:235], v65 offset:1408\n"
    "v_mfma_f32_16x16x32_bf16 v[148:151], a[128:131], a[180:183], v[148:151]\n"
    "s_mul_i32 s31, s37, 7\n"
    "s_add_u32 s30, s30, s31\n"
    "ds_read_b128 a[236:239], v65 offset:1920\n"
    "v_mfma_f32_16x16x32_bf16 v[152:155], a[128:131], a[184:187], v[152:155]\n"
    "ds_read_b128 a[240:243], v65 offset:2432\n"
    "v_mfma_f32_16x16x32_bf16 v[156:159], a[128:131], a[188:191], v[156:159]\n"
    "buffer_load_dwordx4 v22, s[76:79], s25 offen offset:0 lds\n"
    "s_lshl_b32 s30, s30, 7\n"
    "ds_read_b128 a[244:247], v65 offset:2944\n"
    "v_mfma_f32_16x16x32_bf16 v[132:135], a[132:135], a[160:163], v[132:135]\n"
    "ds_read_b128 a[248:251], v65 offset:3456\n"
    "v_mfma_f32_16x16x32_bf16 v[136:139], a[132:135], a[164:167], v[136:139]\n"
    "s_add_u32 s30, s30, 40968192\n"
    "buffer_load_dwordx4 v23, s[76:79], s25 offen offset:1024 lds\n"
    "ds_read_b128 a[252:255], v65 offset:3968\n"
    "v_mfma_f32_16x16x32_bf16 v[140:143], a[132:135], a[168:171], v[140:143]\n"
    "ds_read_b64_tr_b16 a[196:197], v64 offset:584\n"
    "v_mfma_f32_16x16x32_bf16 v[144:147], a[132:135], a[172:175], v[144:147]\n"
    "v_min_u32 v80, 6, v1\n"
    "ds_read_b64_tr_b16 a[198:199], v64 offset:576\n"
    "v_mfma_f32_16x16x32_bf16 v[148:151], a[132:135], a[176:179], v[148:151]\n"
    "ds_read_b64_tr_b16 a[200:201], v64 offset:1096\n"
    "v_mfma_f32_16x16x32_bf16 v[152:155], a[132:135], a[180:183], v[152:155]\n"
    "v_lshlrev_b32 v80, 7, v80\n"
    "buffer_load_dwordx4 v24, s[76:79], s25 offen offset:2048 lds\n"
    "v_add_u32 v80, s30, v80\n"
    "ds_read_b64_tr_b16 a[202:203], v64 offset:1088\n"
    "v_mfma_f32_16x16x32_bf16 v[156:159], a[132:135], a[184:187], v[156:159]\n"
    "ds_read_b64_tr_b16 a[204:205], v64 offset:1608\n"
    "v_mfma_f32_16x16x32_bf16 v[136:139], a[136:139], a[160:163], v[136:139]\n"
    "v_lshlrev_b32 v2, 4, v1\n"
    "ds_read_b64_tr_b16 a[206:207], v64 offset:1600\n"
    "v_mfma_f32_16x16x32_bf16 v[140:143], a[136:139], a[164:167], v[140:143]\n"
    "ds_read_b64_tr_b16 a[208:209], v64 offset:2120\n"
    "v_mfma_f32_16x16x32_bf16 v[144:147], a[136:139], a[168:171], v[144:147]\n"
    "buffer_load_dwordx4 v25, s[76:79], s25 offen offset:3072 lds\n"
    "v_add_u32 v3, 114688, v2\n"
    "ds_read_b64_tr_b16 a[210:211], v64 offset:2112\n"
    "v_mfma_f32_16x16x32_bf16 v[148:151], a[136:139], a[172:175], v[148:151]\n"
    "ds_read_b64_tr_b16 a[212:213], v64 offset:2632\n"
    "v_mfma_f32_16x16x32_bf16 v[152:155], a[136:139], a[176:179], v[152:155]\n"
    "v_add_u32 v4, 229376, v2\n"
    "s_add_u32 s25, s25, 131072\n"
    "v_add_u32 v5, 344064, v2\n"
    "ds_read_b64_tr_b16 a[214:215], v64 offset:2624\n"
    "v_mfma_f32_16x16x32_bf16 v[156:159], a[136:139], a[180:183], v[156:159]\n"
    "ds_read_b64_tr_b16 a[216:217], v64 offset:3144\n"
    "v_mfma_f32_16x16x32_bf16 v[140:143], a[140:143], a[160:163], v[140:143]\n"
    "s_lshl_b32 s30, s35, 4\n"
    "ds_read_b64_tr_b16 a[218:219], v64 offset:3136\n"
    "v_mfma_f32_16x16x32_bf16 v[144:147], a[140:143], a[164:167], v[144:147]\n"
    "ds_read_b64_tr_b16 a[220:221], v64 offset:3656\n"
    "v_mfma_f32_16x16x32_bf16 v[148:151], a[140:143], a[168:171], v[148:151]\n"
    "s_add_u32 m0, s28, 16384\n"
    "s_nop 0\n"
    "s_lshl_b32 s31, s34, 2\n"
    "ds_read_b64_tr_b16 a[222:223], v64 offset:3648\n"
    "v_mfma_f32_16x16x32_bf16 v[152:155], a[140:143], a[172:175], v[152:155]\n"
    "s_add_u32 s30, s30, s31\n"
    "buffer_load_dwordx4 v22, s[76:79], s25 offen offset:0 lds\n"
    "v_mfma_f32_16x16x32_bf16 v[156:159], a[140:143], a[176:179], v[156:159]\n"
    "s_mul_i32 s30, s30, 112\n"
    "s_mul_i32 s31, s37, 28\n"
    "v_mfma_f32_16x16x32_bf16 v[144:147], a[144:147], a[160:163], v[144:147]\n"
    "buffer_load_dwordx4 v23, s[76:79], s25 offen offset:1024 lds\n"
    "s_add_u32 s30, s30, s31\n"
    "v_mfma_f32_16x16x32_bf16 v[148:151], a[144:147], a[164:167], v[148:151]\n"
    "s_lshl_b32 s38, s30, 10\n"
    "v_mfma_f32_16x16x32_bf16 v[152:155], a[144:147], a[168:171], v[152:155]\n"
    "buffer_load_dwordx4 v24, s[76:79], s25 offen offset:2048 lds\n"
    "s_add_u32 s38, s38, 24117248\n"
    "s_add_u32 s97, s38, 28672\n"
    "v_mfma_f32_16x16x32_bf16 v[156:159], a[144:147], a[172:175], v[156:159]\n"
    "buffer_load_dwordx4 v25, s[76:79], s25 offen offset:3072 lds\n"
    "s_lshl_b32 s30, s27, 12\n"
    "v_mfma_f32_16x16x32_bf16 v[148:151], a[148:151], a[160:163], v[148:151]\n"
    "s_add_u32 s38, s38, s30\n"
    "v_mfma_f32_16x16x32_bf16 v[152:155], a[148:151], a[164:167], v[152:155]\n"
    "s_add_u32 s25, s25, 131072\n"
    "v_lshrrev_b32 v38, 2, v1\n"
    "v_mfma_f32_16x16x32_bf16 v[156:159], a[148:151], a[168:171], v[156:159]\n"
    "v_lshlrev_b32 v39, 8, v38\n"
    "s_add_u32 m0, s28, 32768\n"
    "s_nop 0\n"
    "v_and_b32 v40, 3, v1\n"
    "v_mfma_f32_16x16x32_bf16 v[152:155], a[152:155], a[160:163], v[152:155]\n"
    "v_lshl_add_u32 v39, v40, 3, v39\n"
    "v_mfma_f32_16x16x32_bf16 v[156:159], a[152:155], a[164:167], v[156:159]\n"
    "buffer_load_dwordx4 v22, s[76:79], s25 offen offset:0 lds\n"
    "v_and_b32 v38, 7, v38\n"
    "v_mfma_f32_16x16x32_bf16 v[156:159], a[156:159], a[160:163], v[156:159]\n"
    "s_lshl_b32 s30, s37, 1\n"
    "buffer_load_dwordx4 v23, s[76:79], s25 offen offset:1024 lds\n"
    "s_waitcnt lgkmcnt(0)\n"
    "v_mfma_f32_16x16x32_bf16 v[128:131], a[192:195], a[224:227], v[128:131]\n"
    "ds_read_b64_tr_b16 a[128:129], v64 offset:136\n"
    "v_mfma_f32_16x16x32_bf16 v[132:135], a[192:195], a[228:231], v[132:135]\n"
    "ds_read_b64_tr_b16 a[130:131], v64 offset:128\n"
    "v_mfma_f32_16x16x32_bf16 v[136:139], a[192:195], a[232:235], v[136:139]\n"
    "ds_read_b128 a[160:163], v65 offset:320\n"
    "v_mfma_f32_16x16x32_bf16 v[140:143], a[192:195], a[236:239], v[140:143]\n"
    "v_xor_b32 v38, s30, v38\n"
    "v_xor_b32 v40, 0, v38\n"
    "ds_read_b128 a[164:167], v65 offset:832\n"
    "v_mfma_f32_16x16x32_bf16 v[144:147], a[192:195], a[240:243], v[144:147]\n"
    "ds_read_b128 a[168:171], v65 offset:1344\n"
    "v_mfma_f32_16x16x32_bf16 v[148:151], a[192:195], a[244:247], v[148:151]\n"
    "buffer_load_dwordx4 v24, s[76:79], s25 offen offset:2048 lds\n"
    "v_lshl_add_u32 v6, v40, 5, v39\n"
    "ds_read_b128 a[172:175], v65 offset:1856\n"
    "v_mfma_f32_16x16x32_bf16 v[152:155], a[192:195], a[248:251], v[152:155]\n"
    "ds_read_b128 a[176:179], v65 offset:2368\n"
    "v_mfma_f32_16x16x32_bf16 v[156:159], a[192:195], a[252:255], v[156:159]\n"
    "v_xor_b32 v40, 1, v38\n"
    "ds_read_b128 a[180:183], v65 offset:2880\n"
    "v_mfma_f32_16x16x32_bf16 v[132:135], a[196:199], a[224:227], v[132:135]\n"
    "ds_read_b128 a[184:187], v65 offset:3392\n"
    "v_mfma_f32_16x16x32_bf16 v[136:139], a[196:199], a[228:231], v[136:139]\n"
    "v_lshl_add_u32 v7, v40, 5, v39\n"
    "buffer_load_dwordx4 v25, s[76:79], s25 offen offset:3072 lds\n"
    "v_xor_b32 v40, 2, v38\n"
    "ds_read_b128 a[188:191], v65 offset:3904\n"
    "v_mfma_f32_16x16x32_bf16 v[140:143], a[196:199], a[232:235], v[140:143]\n"
    "ds_read_b64_tr_b16 a[132:133], v64 offset:648\n"
    "v_mfma_f32_16x16x32_bf16 v[144:147], a[196:199], a[236:239], v[144:147]\n"
    "v_lshl_add_u32 v8, v40, 5, v39\n"
    "s_add_u32 s25, s25, 131072\n"
    "ds_read_b64_tr_b16 a[134:135], v64 offset:640\n"
    "v_mfma_f32_16x16x32_bf16 v[148:151], a[196:199], a[240:243], v[148:151]\n"
    "ds_read_b64_tr_b16 a[136:137], v64 offset:1160\n"
    "v_mfma_f32_16x16x32_bf16 v[152:155], a[196:199], a[244:247], v[152:155]\n"
    "v_xor_b32 v40, 3, v38\n"
    "ds_read_b64_tr_b16 a[138:139], v64 offset:1152\n"
    "v_mfma_f32_16x16x32_bf16 v[156:159], a[196:199], a[248:251], v[156:159]\n"
    "ds_read_b64_tr_b16 a[140:141], v64 offset:1672\n"
    "v_mfma_f32_16x16x32_bf16 v[136:139], a[200:203], a[224:227], v[136:139]\n"
    "v_lshl_add_u32 v9, v40, 5, v39\n"
    "ds_read_b64_tr_b16 a[142:143], v64 offset:1664\n"
    "v_mfma_f32_16x16x32_bf16 v[140:143], a[200:203], a[228:231], v[140:143]\n"
    "ds_read_b64_tr_b16 a[144:145], v64 offset:2184\n"
    "v_mfma_f32_16x16x32_bf16 v[144:147], a[200:203], a[232:235], v[144:147]\n"
    "v_xor_b32 v40, 4, v38\n"
    "buffer_load_dwordx2 v[84:85], v80, s[20:23], 0 offen sc1\n"
    "v_lshl_add_u32 v10, v40, 5, v39\n"
    "ds_read_b64_tr_b16 a[146:147], v64 offset:2176\n"
    "v_mfma_f32_16x16x32_bf16 v[148:151], a[200:203], a[236:239], v[148:151]\n"
    "ds_read_b64_tr_b16 a[148:149], v64 offset:2696\n"
    "v_mfma_f32_16x16x32_bf16 v[152:155], a[200:203], a[240:243], v[152:155]\n"
    "v_xor_b32 v40, 5, v38\n"
    "ds_read_b64_tr_b16 a[150:151], v64 offset:2688\n"
    "v_mfma_f32_16x16x32_bf16 v[156:159], a[200:203], a[244:247], v[156:159]\n"
    "ds_read_b64_tr_b16 a[152:153], v64 offset:3208\n"
    "v_mfma_f32_16x16x32_bf16 v[140:143], a[204:207], a[224:227], v[140:143]\n"
    "v_lshl_add_u32 v11, v40, 5, v39\n"
    "ds_read_b64_tr_b16 a[154:155], v64 offset:3200\n"
    "v_mfma_f32_16x16x32_bf16 v[144:147], a[204:207], a[228:231], v[144:147]\n"
    "ds_read_b64_tr_b16 a[156:157], v64 offset:3720\n"
    "v_mfma_f32_16x16x32_bf16 v[148:151], a[204:207], a[232:235], v[148:151]\n"
    "v_xor_b32 v40, 6, v38\n"
    "s_add_u32 m0, s28, 49152\n"
    "s_nop 0\n"
    "v_lshl_add_u32 v12, v40, 5, v39\n"
    "ds_read_b64_tr_b16 a[158:159], v64 offset:3712\n"
    "v_mfma_f32_16x16x32_bf16 v[152:155], a[204:207], a[236:239], v[152:155]\n"
    "v_xor_b32 v40, 7, v38\n"
    "v_mfma_f32_16x16x32_bf16 v[156:159], a[204:207], a[240:243], v[156:159]\n"
    "buffer_load_dwordx4 v22, s[76:79], s25 offen offset:0 lds\n"
    "v_lshl_add_u32 v13, v40, 5, v39\n"
    "v_mfma_f32_16x16x32_bf16 v[144:147], a[208:211], a[224:227], v[144:147]\n"
    "v_and_b32 v34, 15, v1\n"
    "v_mfma_f32_16x16x32_bf16 v[148:151], a[208:211], a[228:231], v[148:151]\n"
    "v_lshlrev_b32 v34, 5, v34\n"
    "buffer_load_dwordx4 v23, s[76:79], s25 offen offset:1024 lds\n"
    "v_bfe_u32 v72, v1, 4, 1\n"
    "v_mfma_f32_16x16x32_bf16 v[152:155], a[208:211], a[232:235], v[152:155]\n"
    "v_lshl_add_u32 v34, v72, 9, v34\n"
    "v_mfma_f32_16x16x32_bf16 v[156:159], a[208:211], a[236:239], v[156:159]\n"
    "v_lshrrev_b32 v72, 5, v1\n"
    "buffer_load_dwordx4 v24, s[76:79], s25 offen offset:2048 lds\n"
    "v_mfma_f32_16x16x32_bf16 v[148:151], a[212:215], a[224:227], v[148:151]\n"
    "v_lshl_add_u32 v34, v72, 4, v34\n"
    "s_lshl_b32 s30, s34, 12\n"
    "v_mfma_f32_16x16x32_bf16 v[152:155], a[212:215], a[228:231], v[152:155]\n"
    "s_add_u32 s30, s30, 147456\n"
    "buffer_load_dwordx4 v25, s[76:79], s25 offen offset:3072 lds\n"
    "v_mfma_f32_16x16x32_bf16 v[156:159], a[212:215], a[232:235], v[156:159]\n"
    "v_add_u32 v82, s30, v2\n"
    "v_mfma_f32_16x16x32_bf16 v[152:155], a[216:219], a[224:227], v[152:155]\n"
    "v_xor_b32 v35, 0x1000, v82\n"
    "v_mfma_f32_16x16x32_bf16 v[156:159], a[216:219], a[228:231], v[156:159]\n"
    "s_add_u32 s25, s25, 131072\n"
    "v_mfma_f32_16x16x32_bf16 v[156:159], a[220:223], a[224:227], v[156:159]\n"
    "s_waitcnt lgkmcnt(0)\n"
    "s_waitcnt vmcnt(4)\n"
    "v_cmp_ne_u64 vcc, s[70:71], v[84:85]\n"
    "s_cbranch_vccnz PHSA_gated_mix__ssm_conv_h3__swiglu_ffn__prompt_kernel\n"
    "PHRA_gated_mix__ssm_conv_h3__swiglu_ffn__prompt_kernel:\n"
    "v_mfma_f32_16x16x32_bf16 v[128:131], a[128:131], a[160:163], v[128:131]\n"
    "ds_read_b64_tr_b16 a[192:193], v64 offset:200\n"
    "v_mfma_f32_16x16x32_bf16 v[132:135], a[128:131], a[164:167], v[132:135]\n"
    "ds_read_b64_tr_b16 a[194:195], v64 offset:192\n"
    "v_mfma_f32_16x16x32_bf16 v[136:139], a[128:131], a[168:171], v[136:139]\n"
    "buffer_load_dwordx4 a[0:3], v2, s[20:23], s38 offen offset:0 sc1\n"
    "ds_read_b128 a[224:227], v65 offset:256\n"
    "v_mfma_f32_16x16x32_bf16 v[140:143], a[128:131], a[172:175], v[140:143]\n"
    "ds_read_b128 a[228:231], v65 offset:768\n"
    "v_mfma_f32_16x16x32_bf16 v[144:147], a[128:131], a[176:179], v[144:147]\n"
    "ds_read_b128 a[232:235], v65 offset:1280\n"
    "v_mfma_f32_16x16x32_bf16 v[148:151], a[128:131], a[180:183], v[148:151]\n"
    "buffer_load_dwordx4 a[4:7], v3, s[20:23], s38 offen offset:0 sc1\n"
    "ds_read_b128 a[236:239], v65 offset:1792\n"
    "v_mfma_f32_16x16x32_bf16 v[152:155], a[128:131], a[184:187], v[152:155]\n"
    "ds_read_b128 a[240:243], v65 offset:2304\n"
    "v_mfma_f32_16x16x32_bf16 v[156:159], a[128:131], a[188:191], v[156:159]\n"
    "ds_read_b128 a[244:247], v65 offset:2816\n"
    "v_mfma_f32_16x16x32_bf16 v[132:135], a[132:135], a[160:163], v[132:135]\n"
    "ds_read_b128 a[248:251], v65 offset:3328\n"
    "v_mfma_f32_16x16x32_bf16 v[136:139], a[132:135], a[164:167], v[136:139]\n"
    "buffer_load_dwordx4 a[8:11], v4, s[20:23], s38 offen offset:0 sc1\n"
    "ds_read_b128 a[252:255], v65 offset:3840\n"
    "v_mfma_f32_16x16x32_bf16 v[140:143], a[132:135], a[168:171], v[140:143]\n"
    "ds_read_b64_tr_b16 a[196:197], v64 offset:712\n"
    "v_mfma_f32_16x16x32_bf16 v[144:147], a[132:135], a[172:175], v[144:147]\n"
    "ds_read_b64_tr_b16 a[198:199], v64 offset:704\n"
    "v_mfma_f32_16x16x32_bf16 v[148:151], a[132:135], a[176:179], v[148:151]\n"
    "ds_read_b64_tr_b16 a[200:201], v64 offset:1224\n"
    "v_mfma_f32_16x16x32_bf16 v[152:155], a[132:135], a[180:183], v[152:155]\n"
    "buffer_load_dwordx4 a[12:15], v5, s[20:23], s38 offen offset:0 sc1\n"
    "ds_read_b64_tr_b16 a[202:203], v64 offset:1216\n"
    "v_mfma_f32_16x16x32_bf16 v[156:159], a[132:135], a[184:187], v[156:159]\n"
    "ds_read_b64_tr_b16 a[204:205], v64 offset:1736\n"
    "v_mfma_f32_16x16x32_bf16 v[136:139], a[136:139], a[160:163], v[136:139]\n"
    "ds_read_b64_tr_b16 a[206:207], v64 offset:1728\n"
    "v_mfma_f32_16x16x32_bf16 v[140:143], a[136:139], a[164:167], v[140:143]\n"
    "ds_read_b64_tr_b16 a[208:209], v64 offset:2248\n"
    "v_mfma_f32_16x16x32_bf16 v[144:147], a[136:139], a[168:171], v[144:147]\n"
    "buffer_load_dwordx4 a[16:19], v2, s[20:23], s38 offen offset:1024 sc1\n"
    "ds_read_b64_tr_b16 a[210:211], v64 offset:2240\n"
    "v_mfma_f32_16x16x32_bf16 v[148:151], a[136:139], a[172:175], v[148:151]\n"
    "ds_read_b64_tr_b16 a[212:213], v64 offset:2760\n"
    "v_mfma_f32_16x16x32_bf16 v[152:155], a[136:139], a[176:179], v[152:155]\n"
    "ds_read_b64_tr_b16 a[214:215], v64 offset:2752\n"
    "v_mfma_f32_16x16x32_bf16 v[156:159], a[136:139], a[180:183], v[156:159]\n"
    "buffer_load_dwordx4 a[20:23], v3, s[20:23], s38 offen offset:1024 sc1\n"
    "ds_read_b64_tr_b16 a[216:217], v64 offset:3272\n"
    "v_mfma_f32_16x16x32_bf16 v[140:143], a[140:143], a[160:163], v[140:143]\n"
    "ds_read_b64_tr_b16 a[218:219], v64 offset:3264\n"
    "v_mfma_f32_16x16x32_bf16 v[144:147], a[140:143], a[164:167], v[144:147]\n"
    "ds_read_b64_tr_b16 a[220:221], v64 offset:3784\n"
    "v_mfma_f32_16x16x32_bf16 v[148:151], a[140:143], a[168:171], v[148:151]\n"
    "ds_read_b64_tr_b16 a[222:223], v64 offset:3776\n"
    "v_mfma_f32_16x16x32_bf16 v[152:155], a[140:143], a[172:175], v[152:155]\n"
    "buffer_load_dwordx4 a[24:27], v4, s[20:23], s38 offen offset:1024 sc1\n"
    "v_mfma_f32_16x16x32_bf16 v[156:159], a[140:143], a[176:179], v[156:159]\n"
    "v_mfma_f32_16x16x32_bf16 v[144:147], a[144:147], a[160:163], v[144:147]\n"
    "v_mfma_f32_16x16x32_bf16 v[148:151], a[144:147], a[164:167], v[148:151]\n"
    "v_mfma_f32_16x16x32_bf16 v[152:155], a[144:147], a[168:171], v[152:155]\n"
    "buffer_load_dwordx4 a[28:31], v5, s[20:23], s38 offen offset:1024 sc1\n"
    "v_mfma_f32_16x16x32_bf16 v[156:159], a[144:147], a[172:175], v[156:159]\n"
    "v_mfma_f32_16x16x32_bf16 v[148:151], a[148:151], a[160:163], v[148:151]\n"
    "v_mfma_f32_16x16x32_bf16 v[152:155], a[148:151], a[164:167], v[152:155]\n"
    "v_mfma_f32_16x16x32_bf16 v[156:159], a[148:151], a[168:171], v[156:159]\n"
    "buffer_load_dwordx4 a[32:35], v2, s[20:23], s38 offen offset:2048 sc1\n"
    "v_mfma_f32_16x16x32_bf16 v[152:155], a[152:155], a[160:163], v[152:155]\n"
    "v_mfma_f32_16x16x32_bf16 v[156:159], a[152:155], a[164:167], v[156:159]\n"
    "v_mfma_f32_16x16x32_bf16 v[156:159], a[156:159], a[160:163], v[156:159]\n"
    "s_waitcnt lgkmcnt(0)\n"
    "v_mfma_f32_16x16x32_bf16 v[128:131], a[192:195], a[224:227], v[128:131]\n"
    "v_accvgpr_read_b32 v88, a76\n"
    "v_mfma_f32_16x16x32_bf16 v[132:135], a[192:195], a[228:231], v[132:135]\n"
    "v_accvgpr_read_b32 v89, a77\n"
    "v_mfma_f32_16x16x32_bf16 v[136:139], a[192:195], a[232:235], v[136:139]\n"
    "v_accvgpr_read_b32 v90, a78\n"
    "v_mfma_f32_16x16x32_bf16 v[140:143], a[192:195], a[236:239], v[140:143]\n"
    "buffer_load_dwordx4 a[36:39], v3, s[20:23], s38 offen offset:2048 sc1\n"
    "v_mfma_f32_16x16x32_bf16 v[144:147], a[192:195], a[240:243], v[144:147]\n"
    "v_accvgpr_read_b32 v91, a79\n"
    "v_mfma_f32_16x16x32_bf16 v[148:151], a[192:195], a[244:247], v[148:151]\n"
    "v_accvgpr_read_b32 v92, a80\n"
    "v_mfma_f32_16x16x32_bf16 v[152:155], a[192:195], a[248:251], v[152:155]\n"
    "v_accvgpr_read_b32 v93, a81\n"
    "v_mfma_f32_16x16x32_bf16 v[156:159], a[192:195], a[252:255], v[156:159]\n"
    "buffer_load_dwordx4 a[40:43], v4, s[20:23], s38 offen offset:2048 sc1\n"
    "v_mfma_f32_16x16x32_bf16 v[132:135], a[196:199], a[224:227], v[132:135]\n"
    "v_accvgpr_read_b32 v94, a82\n"
    "v_mfma_f32_16x16x32_bf16 v[136:139], a[196:199], a[228:231], v[136:139]\n"
    "v_accvgpr_read_b32 v95, a83\n"
    "v_mfma_f32_16x16x32_bf16 v[140:143], a[196:199], a[232:235], v[140:143]\n"
    "v_accvgpr_read_b32 v96, a84\n"
    "v_mfma_f32_16x16x32_bf16 v[144:147], a[196:199], a[236:239], v[144:147]\n"
    "buffer_load_dwordx4 a[44:47], v5, s[20:23], s38 offen offset:2048 sc1\n"
    "v_mfma_f32_16x16x32_bf16 v[148:151], a[196:199], a[240:243], v[148:151]\n"
    "v_accvgpr_read_b32 v97, a85\n"
    "v_mfma_f32_16x16x32_bf16 v[152:155], a[196:199], a[244:247], v[152:155]\n"
    "v_accvgpr_read_b32 v98, a86\n"
    "v_mfma_f32_16x16x32_bf16 v[156:159], a[196:199], a[248:251], v[156:159]\n"
    "v_accvgpr_read_b32 v99, a87\n"
    "v_mfma_f32_16x16x32_bf16 v[136:139], a[200:203], a[224:227], v[136:139]\n"
    "buffer_load_dwordx4 a[48:51], v2, s[20:23], s38 offen offset:3072 sc1\n"
    "v_mfma_f32_16x16x32_bf16 v[140:143], a[200:203], a[228:231], v[140:143]\n"
    "v_accvgpr_read_b32 v100, a88\n"
    "v_mfma_f32_16x16x32_bf16 v[144:147], a[200:203], a[232:235], v[144:147]\n"
    "v_accvgpr_read_b32 v101, a89\n"
    "v_mfma_f32_16x16x32_bf16 v[148:151], a[200:203], a[236:239], v[148:151]\n"
    "buffer_load_dwordx4 a[52:55], v3, s[20:23], s38 offen offset:3072 sc1\n"
    "v_mfma_f32_16x16x32_bf16 v[152:155], a[200:203], a[240:243], v[152:155]\n"
    "v_accvgpr_read_b32 v102, a90\n"
    "v_mfma_f32_16x16x32_bf16 v[156:159], a[200:203], a[244:247], v[156:159]\n"
    "v_accvgpr_read_b32 v103, a91\n"
    "v_mfma_f32_16x16x32_bf16 v[140:143], a[204:207], a[224:227], v[140:143]\n"
    "v_accvgpr_read_b32 v104, a92\n"
    "v_mfma_f32_16x16x32_bf16 v[144:147], a[204:207], a[228:231], v[144:147]\n"
    "buffer_load_dwordx4 a[56:59], v4, s[20:23], s38 offen offset:3072 sc1\n"
    "v_mfma_f32_16x16x32_bf16 v[148:151], a[204:207], a[232:235], v[148:151]\n"
    "v_accvgpr_read_b32 v105, a93\n"
    "v_mfma_f32_16x16x32_bf16 v[152:155], a[204:207], a[236:239], v[152:155]\n"
    "v_accvgpr_read_b32 v106, a94\n"
    "v_mfma_f32_16x16x32_bf16 v[156:159], a[204:207], a[240:243], v[156:159]\n"
    "v_accvgpr_read_b32 v107, a95\n"
    "v_mfma_f32_16x16x32_bf16 v[144:147], a[208:211], a[224:227], v[144:147]\n"
    "buffer_load_dwordx4 a[60:63], v5, s[20:23], s38 offen offset:3072 sc1\n"
    "v_mfma_f32_16x16x32_bf16 v[148:151], a[208:211], a[228:231], v[148:151]\n"
    "v_accvgpr_read_b32 v108, a96\n"
    "v_mfma_f32_16x16x32_bf16 v[152:155], a[208:211], a[232:235], v[152:155]\n"
    "v_accvgpr_read_b32 v109, a97\n"
    "v_mfma_f32_16x16x32_bf16 v[156:159], a[208:211], a[236:239], v[156:159]\n"
    "v_accvgpr_read_b32 v110, a98\n"
    "v_mfma_f32_16x16x32_bf16 v[148:151], a[212:215], a[224:227], v[148:151]\n"
    "s_add_u32 s38, s38, 4096\n"
    "v_mfma_f32_16x16x32_bf16 v[152:155], a[212:215], a[228:231], v[152:155]\n"
    "v_accvgpr_read_b32 v111, a99\n"
    "v_mfma_f32_16x16x32_bf16 v[156:159], a[212:215], a[232:235], v[156:159]\n"
    "v_mfma_f32_16x16x32_bf16 v[152:155], a[216:219], a[224:227], v[152:155]\n"
    "v_mfma_f32_16x16x32_bf16 v[156:159], a[216:219], a[228:231], v[156:159]\n"
    "v_mfma_f32_16x16x32_bf16 v[156:159], a[220:223], a[224:227], v[156:159]\n"
    "s_waitcnt lgkmcnt(0)\n"
    "BEND_gated_mix__ssm_conv_h3__swiglu_ffn__prompt_kernel:\n"
    "s_cmp_eq_u32 s68, 0\n"
    "s_nop 5\n"
    "s_cbranch_scc1 BW0_gated_mix__ssm_conv_h3__swiglu_ffn__prompt_kernel\n"
    "ds_write_b128 v82, v[128:131] offset:0\n"
    "ds_write_b128 v82, v[132:135] offset:1024\n"
    "ds_write_b128 v82, v[136:139] offset:2048\n"
    "ds_write_b128 v82, v[140:143] offset:3072\n"
    "s_branch BW1_gated_mix__ssm_conv_h3__swiglu_ffn__prompt_kernel\n"
    "BW0_gated_mix__ssm_conv_h3__swiglu_ffn__prompt_kernel:\n"
    "ds_write_b128 v82, v[144:147] offset:0\n"
    "ds_write_b128 v82, v[148:151] offset:1024\n"
    "ds_write_b128 v82, v[152:155] offset:2048\n"
    "ds_write_b128 v82, v[156:159] offset:3072\n"
    "BW1_gated_mix__ssm_conv_h3__swiglu_ffn__prompt_kernel:\n"
    "s_waitcnt lgkmcnt(0)\n"
    "s_barrier\n"
    "ds_read_b128 v[160:163], v35 offset:0\n"
    "ds_read_b128 v[164:167], v35 offset:1024\n"
    "ds_read_b128 v[168:171], v35 offset:2048\n"
    "ds_read_b128 v[172:175], v35 offset:3072\n"
    "ds_read_b64_tr_b16 a[128:129], v6 offset:0\n"
    "ds_read_b64_tr_b16 a[130:131], v6 offset:4096\n"
    "ds_read_b64_tr_b16 a[132:133], v7 offset:0\n"
    "ds_read_b64_tr_b16 a[134:135], v7 offset:4096\n"
    "ds_read_b64_tr_b16 a[136:137], v8 offset:0\n"
    "ds_read_b64_tr_b16 a[138:139], v8 offset:4096\n"
    "ds_read_b64_tr_b16 a[140:141], v9 offset:0\n"
    "ds_read_b64_tr_b16 a[142:143], v9 offset:4096\n"
    "ds_read_b64_tr_b16 a[144:145], v10 offset:0\n"
    "ds_read_b64_tr_b16 a[146:147], v10 offset:4096\n"
    "ds_read_b64_tr_b16 a[148:149], v11 offset:0\n"
    "ds_read_b64_tr_b16 a[150:151], v11 offset:4096\n"
    "ds_read_b64_tr_b16 a[152:153], v12 offset:0\n"
    "ds_read_b64_tr_b16 a[154:155], v12 offset:4096\n"
    "ds_read_b64_tr_b16 a[156:157], v13 offset:0\n"
    "ds_read_b64_tr_b16 a[158:159], v13 offset:4096\n"
    "buffer_load_dwordx4 a[64:67], v2, s[20:23], s38 offen offset:0 sc1\n"
    "buffer_load_dwordx4 a[68:71], v3, s[20:23], s38 offen offset:0 sc1\n"
    "buffer_load_dwordx4 a[72:75], v4, s[20:23], s38 offen offset:0 sc1\n"
    "buffer_load_dwordx4 a[76:79], v5, s[20:23], s38 offen offset:0 sc1\n"
    "buffer_load_dwordx4 a[80:83], v2, s[20:23], s38 offen offset:1024 sc1\n"
    "buffer_load_dwordx4 a[84:87], v3, s[20:23], s38 offen offset:1024 sc1\n"
    "buffer_load_dwordx4 a[88:91], v4, s[20:23], s38 offen offset:1024 sc1\n"
    "buffer_load_dwordx4 a[92:95], v5, s[20:23], s38 offen offset:1024 sc1\n"
    "s_mul_i32 s30, s72, 4224\n"
    "s_lshl_b32 s31, s68, 11\n"
    "s_add_u32 s30, s30, s31\n"
    "s_add_u32 s30, s30, 13631488\n"
    "s_waitcnt lgkmcnt(15)\n"
    "s_cmp_eq_u32 s68, 0\n"
    "s_cbranch_scc1 BA0_gated_mix__ssm_conv_h3__swiglu_ffn__prompt_kernel\n"
    "v_pk_add_f32 v[160:161], v[144:145], v[160:161]\n"
    "v_pk_add_f32 v[162:163], v[146:147], v[162:163]\n"
    "v_pk_add_f32 v[164:165], v[148:149], v[164:165]\n"
    "v_pk_add_f32 v[166:167], v[150:151], v[166:167]\n"
    "v_pk_add_f32 v[168:169], v[152:153], v[168:169]\n"
    "v_pk_add_f32 v[170:171], v[154:155], v[170:171]\n"
    "v_pk_add_f32 v[172:173], v[156:157], v[172:173]\n"
    "v_pk_add_f32 v[174:175], v[158:159], v[174:175]\n"
    "s_branch BA1_gated_mix__ssm_conv_h3__swiglu_ffn__prompt_kernel\n"
    "BA0_gated_mix__ssm_conv_h3__swiglu_ffn__prompt_kernel:\n"
    "v_pk_add_f32 v[160:161], v[128:129], v[160:161]\n"
    "v_pk_add_f32 v[162:163], v[130:131], v[162:163]\n"
    "v_pk_add_f32 v[164:165], v[132:133], v[164:165]\n"
    "v_pk_add_f32 v[166:167], v[134:135], v[166:167]\n"
    "v_pk_add_f32 v[168:169], v[136:137], v[168:169]\n"
    "v_pk_add_f32 v[170:171], v[138:139], v[170:171]\n"
    "v_pk_add_f32 v[172:173], v[140:141], v[172:173]\n"
    "v_pk_add_f32 v[174:175], v[142:143], v[174:175]\n"
    "BA1_gated_mix__ssm_conv_h3__swiglu_ffn__prompt_kernel:\n"
    "v_cvt_pk_bf16_f32 v40, v160, v161\n"
    "v_cvt_pk_bf16_f32 v41, v162, v163\n"
    "v_cvt_pk_bf16_f32 v42, v164, v165\n"
    "v_cvt_pk_bf16_f32 v43, v166, v167\n"
    "v_cvt_pk_bf16_f32 v44, v168, v169\n"
    "v_cvt_pk_bf16_f32 v45, v170, v171\n"
    "v_cvt_pk_bf16_f32 v46, v172, v173\n"
    "v_cvt_pk_bf16_f32 v47, v174, v175\n"
    "s_nop 0\n"
    "v_permlane16_swap_b32 v40, v42\n"
    "v_permlane16_swap_b32 v41, v43\n"
    "v_permlane16_swap_b32 v44, v46\n"
    "v_permlane16_swap_b32 v45, v47\n"
    "s_mov_b32 s69, s30\n"
    "s_waitcnt vmcnt(20)\n"
    "s_waitcnt lgkmcnt(12)\n"
    "v_mfma_f32_16x16x32_bf16 v[128:131], a[128:131], a[0:3], 0\n"
    "ds_read_b64_tr_b16 a[160:161], v6 offset:8192\n"
    "v_mfma_f32_16x16x32_bf16 v[132:135], a[128:131], a[4:7], 0\n"
    "s_add_u32 m0, s28, 65536\n"
    "s_nop 0\n"
    "buffer_load_dwordx4 v22, s[76:79], s25 offen offset:0 lds\n"
    "v_mfma_f32_16x16x32_bf16 v[136:139], a[128:131], a[8:11], 0\n"
    "ds_read_b64_tr_b16 a[162:163], v6 offset:12288\n"
    "v_mfma_f32_16x16x32_bf16 v[140:143], a[128:131], a[12:15], 0\n"
    "buffer_store_dwordx4 v[40:43], v34, s[20:23], s69 offen offset:0 sc1\n"
    "buffer_store_dwordx4 v[44:47], v34, s[20:23], s69 offen offset:1024 sc1\n"
    "v_mfma_f32_16x16x32_bf16 v[156:159], a[132:135], a[12:15], 0\n"
    "ds_read_b64_tr_b16 a[164:165], v7 offset:8192\n"
    "v_mfma_f32_16x16x32_bf16 v[152:155], a[132:135], a[8:11], 0\n"
    "buffer_load_dwordx4 v23, s[76:79], s25 offen offset:1024 lds\n"
    "v_dot2_f32_bf16 v112, v88, v104, 0\n"
    "v_mfma_f32_16x16x32_bf16 v[148:151], a[132:135], a[4:7], 0\n"
    "ds_read_b64_tr_b16 a[166:167], v7 offset:12288\n"
    "v_mfma_f32_16x16x32_bf16 v[144:147], a[132:135], a[0:3], 0\n"
    "v_dot2_f32_bf16 v113, v96, v104, 0\n"
    "s_waitcnt lgkmcnt(12)\n"
    "v_mfma_f32_16x16x32_bf16 v[160:163], a[136:139], a[0:3], 0\n"
    "ds_read_b64_tr_b16 a[168:169], v8 offset:8192\n"
    "v_mfma_f32_16x16x32_bf16 v[164:167], a[136:139], a[4:7], 0\n"
    "buffer_load_dwordx4 v24, s[76:79], s25 offen offset:2048 lds\n"
    "v_dot2_f32_bf16 v112, v89, v105, v112\n"
    "v_mfma_f32_16x16x32_bf16 v[168:171], a[136:139], a[8:11], 0\n"
    "ds_read_b64_tr_b16 a[170:171], v8 offset:12288\n"
    "v_mfma_f32_16x16x32_bf16 v[172:175], a[136:139], a[12:15], 0\n"
    "v_dot2_f32_bf16 v113, v97, v105, v113\n"
    "v_mfma_f32_16x16x32_bf16 v[188:191], a[140:143], a[12:15], 0\n"
    "ds_read_b64_tr_b16 a[172:173], v9 offset:8192\n"
    "v_mfma_f32_16x16x32_bf16 v[184:187], a[140:143], a[8:11], 0\n"
    "buffer_load_dwordx4 v25, s[76:79], s25 offen offset:3072 lds\n"
    "v_dot2_f32_bf16 v112, v90, v106, v112\n"
    "v_mfma_f32_16x16x32_bf16 v[180:183], a[140:143], a[4:7], 0\n"
    "ds_read_b64_tr_b16 a[174:175], v9 offset:12288\n"
    "v_mfma_f32_16x16x32_bf16 v[176:179], a[140:143], a[0:3], 0\n"
    "v_dot2_f32_bf16 v113, v98, v106, v113\n"
    "s_waitcnt lgkmcnt(12)\n"
    "v_mfma_f32_16x16x32_bf16 v[192:195], a[144:147], a[0:3], 0\n"
    "ds_read_b64_tr_b16 a[176:177], v10 offset:8192\n"
    "v_mfma_f32_16x16x32_bf16 v[196:199], a[144:147], a[4:7], 0\n"
    "s_add_u32 s25, s25, 131072\n"
    "s_add_u32 m0, s28, 81920\n"
    "s_nop 0\n"
    "buffer_load_dwordx4 v22, s[76:79], s25 offen offset:0 lds\n"
    "v_dot2_f32_bf16 v112, v91, v107, v112\n"
    "v_mfma_f32_16x16x32_bf16 v[200:203], a[144:147], a[8:11], 0\n"
    "ds_read_b64_tr_b16 a[178:179], v10 offset:12288\n"
    "v_mfma_f32_16x16x32_bf16 v[204:207], a[144:147], a[12:15], 0\n"
    "v_dot2_f32_bf16 v113, v99, v107, v113\n"
    "v_mfma_f32_16x16x32_bf16 v[220:223], a[148:151], a[12:15], 0\n"
    "ds_read_b64_tr_b16 a[180:181], v11 offset:8192\n"
    "v_mfma_f32_16x16x32_bf16 v[216:219], a[148:151], a[8:11], 0\n"
    "buffer_load_dwordx4 v23, s[76:79], s25 offen offset:1024 lds\n"
    "v_dot2_f32_bf16 v112, v92, v108, v112\n"
    "v_mfma_f32_16x16x32_bf16 v[212:215], a[148:151], a[4:7], 0\n"
    "ds_read_b64_tr_b16 a[182:183], v11 offset:12288\n"
    "v_mfma_f32_16x16x32_bf16 v[208:211], a[148:151], a[0:3], 0\n"
    "v_dot2_f32_bf16 v113, v100, v108, v113\n"
    "s_waitcnt lgkmcnt(12)\n"
    "v_mfma_f32_16x16x32_bf16 v[224:227], a[152:155], a[0:3], 0\n"
    "ds_read_b64_tr_b16 a[184:185], v12 offset:8192\n"
    "v_mfma_f32_16x16x32_bf16 v[228:231], a[152:155], a[4:7], 0\n"
    "buffer_load_dwordx4 v24, s[76:79], s25 offen offset:2048 lds\n"
    "v_dot2_f32_bf16 v112, v93, v109, v112\n"
    "v_mfma_f32_16x16x32_bf16 v[232:235], a[152:155], a[8:11], 0\n"
    "ds_read_b64_tr_b16 a[186:187], v12 offset:12288\n"
    "v_mfma_f32_16x16x32_bf16 v[236:239], a[152:155], a[12:15], 0\n"
    "v_dot2_f32_bf16 v113, v101, v109, v113\n"
    "v_mfma_f32_16x16x32_bf16 v[252:255], a[156:159], a[12:15], 0\n"
    "ds_read_b64_tr_b16 a[188:189], v13 offset:8192\n"
    "v_mfma_f32_16x16x32_bf16 v[248:251], a[156:159], a[8:11], 0\n"
    "v_dot2_f32_bf16 v112, v94, v110, v112\n"
    "v_mfma_f32_16x16x32_bf16 v[244:247], a[156:159], a[4:7], 0\n"
    "ds_read_b64_tr_b16 a[190:191], v13 offset:12288\n"
    "v_mfma_f32_16x16x32_bf16 v[240:243], a[156:159], a[0:3], 0\n"
    "s_waitcnt vmcnt(25)\n"
    "s_waitcnt lgkmcnt(12)\n"
    "v_mfma_f32_16x16x32_bf16 v[128:131], a[160:163], a[16:19], v[128:131]\n"
    "ds_read_b64_tr_b16 a[128:129], v6 offset:16384\n"
    "v_mfma_f32_16x16x32_bf16 v[132:135], a[160:163], a[20:23], v[132:135]\n"
    "buffer_load_dwordx4 v25, s[76:79], s25 offen offset:3072 lds\n"
    "v_mfma_f32_16x16x32_bf16 v[136:139], a[160:163], a[24:27], v[136:139]\n"
    "ds_read_b64_tr_b16 a[130:131], v6 offset:20480\n"
    "v_mfma_f32_16x16x32_bf16 v[140:143], a[160:163], a[28:31], v[140:143]\n"
    "v_dot2_f32_bf16 v113, v102, v110, v113\n"
    "v_mfma_f32_16x16x32_bf16 v[156:159], a[164:167], a[28:31], v[156:159]\n"
    "ds_read_b64_tr_b16 a[132:133], v7 offset:16384\n"
    "v_mfma_f32_16x16x32_bf16 v[152:155], a[164:167], a[24:27], v[152:155]\n"
    "s_add_u32 s25, s25, 131072\n"
    "buffer_load_dwordx4 a[96:99], v2, s[20:23], s38 offen offset:2048 sc1\n"
    "v_dot2_f32_bf16 v112, v95, v111, v112\n"
    "v_mfma_f32_16x16x32_bf16 v[148:151], a[164:167], a[20:23], v[148:151]\n"
    "ds_read_b64_tr_b16 a[134:135], v7 offset:20480\n"
    "v_mfma_f32_16x16x32_bf16 v[144:147], a[164:167], a[16:19], v[144:147]\n"
    "v_dot2_f32_bf16 v113, v103, v111, v113\n"
    "s_waitcnt lgkmcnt(12)\n"
    "v_mfma_f32_16x16x32_bf16 v[160:163], a[168:171], a[16:19], v[160:163]\n"
    "ds_read_b64_tr_b16 a[136:137], v8 offset:16384\n"
    "v_mfma_f32_16x16x32_bf16 v[164:167], a[168:171], a[20:23], v[164:167]\n"
    "buffer_load_dwordx4 a[100:103], v3, s[20:23], s38 offen offset:2048 sc1\n"
    "v_add_f32_dpp v112, v112, v112 row_shr:1 row_mask:0xf bank_mask:0xf bound_ctrl:0\n"
    "v_mfma_f32_16x16x32_bf16 v[168:171], a[168:171], a[24:27], v[168:171]\n"
    "ds_read_b64_tr_b16 a[138:139], v8 offset:20480\n"
    "v_mfma_f32_16x16x32_bf16 v[172:175], a[168:171], a[28:31], v[172:175]\n"
    "v_add_f32_dpp v113, v113, v113 row_shr:1 row_mask:0xf bank_mask:0xf bound_ctrl:0\n"
    "v_mfma_f32_16x16x32_bf16 v[188:191], a[172:175], a[28:31], v[188:191]\n"
    "ds_read_b64_tr_b16 a[140:141], v9 offset:16384\n"
    "v_mfma_f32_16x16x32_bf16 v[184:187], a[172:175], a[24:27], v[184:187]\n"
    "buffer_load_dwordx4 a[104:107], v4, s[20:23], s38 offen offset:2048 sc1\n"
    "v_add_f32_dpp v112, v112, v112 row_shr:2 row_mask:0xf bank_mask:0xf bound_ctrl:0\n"
    "v_mfma_f32_16x16x32_bf16 v[180:183], a[172:175], a[20:23], v[180:183]\n"
    "ds_read_b64_tr_b16 a[142:143], v9 offset:20480\n"
    "v_mfma_f32_16x16x32_bf16 v[176:179], a[172:175], a[16:19], v[176:179]\n"
    "v_add_f32_dpp v113, v113, v113 row_shr:2 row_mask:0xf bank_mask:0xf bound_ctrl:0\n"
    "s_waitcnt lgkmcnt(12)\n"
    "v_mfma_f32_16x16x32_bf16 v[192:195], a[176:179], a[16:19], v[192:195]\n"
    "ds_read_b64_tr_b16 a[144:145], v10 offset:16384\n"
    "v_mfma_f32_16x16x32_bf16 v[196:199], a[176:179], a[20:23], v[196:199]\n"
    "buffer_load_dwordx4 a[108:111], v5, s[20:23], s38 offen offset:2048 sc1\n"
    "v_add_f32_dpp v112, v112, v112 row_shr:4 row_mask:0xf bank_mask:0xf bound_ctrl:0\n"
    "v_mfma_f32_16x16x32_bf16 v[200:203], a[176:179], a[24:27], v[200:203]\n"
    "ds_read_b64_tr_b16 a[146:147], v10 offset:20480\n"
    "v_mfma_f32_16x16x32_bf16 v[204:207], a[176:179], a[28:31], v[204:207]\n"
    "v_add_f32_dpp v113, v113, v113 row_shr:4 row_mask:0xf bank_mask:0xf bound_ctrl:0\n"
    "v_mfma_f32_16x16x32_bf16 v[220:223], a[180:183], a[28:31], v[220:223]\n"
    "ds_read_b64_tr_b16 a[148:149], v11 offset:16384\n"
    "v_mfma_f32_16x16x32_bf16 v[216:219], a[180:183], a[24:27], v[216:219]\n"
    "buffer_load_dwordx4 a[112:115], v2, s[20:23], s38 offen offset:3072 sc1\n"
    "v_add_f32_dpp v112, v112, v112 row_shr:8 row_mask:0xf bank_mask:0xf bound_ctrl:0\n"
    "v_mfma_f32_16x16x32_bf16 v[212:215], a[180:183], a[20:23], v[212:215]\n"
    "ds_read_b64_tr_b16 a[150:151], v11 offset:20480\n"
    "v_mfma_f32_16x16x32_bf16 v[208:211], a[180:183], a[16:19], v[208:211]\n"
    "v_add_f32_dpp v113, v113, v113 row_shr:8 row_mask:0xf bank_mask:0xf bound_ctrl:0\n"
    "s_waitcnt lgkmcnt(12)\n"
    "v_mfma_f32_16x16x32_bf16 v[224:227], a[184:187], a[16:19], v[224:227]\n"
    "ds_read_b64_tr_b16 a[152:153], v12 offset:16384\n"
    "v_mfma_f32_16x16x32_bf16 v[228:231], a[184:187], a[20:23], v[228:231]\n"
    "buffer_load_dwordx4 a[116:119], v3, s[20:23], s38 offen offset:3072 sc1\n"
    "v_add_f32_dpp v112, v112, v112 row_bcast:15 row_mask:0xa bank_mask:0xf\n"
    "v_mfma_f32_16x16x32_bf16 v[232:235], a[184:187], a[24:27], v[232:235]\n"
    "ds_read_b64_tr_b16 a[154:155], v12 offset:20480\n"
    "v_mfma_f32_16x16x32_bf16 v[236:239], a[184:187], a[28:31], v[236:239]\n"
    "v_add_f32_dpp v113, v113, v113 row_bcast:15 row_mask:0xa bank_mask:0xf\n"
    "v_mfma_f32_16x16x32_bf16 v[252:255], a[188:191], a[28:31], v[252:255]\n"
    "ds_read_b64_tr_b16 a[156:157], v13 offset:16384\n"
    "v_mfma_f32_16x16x32_bf16 v[248:251], a[188:191], a[24:27], v[248:251]\n"
    "v_mfma_f32_16x16x32_bf16 v[244:247], a[188:191], a[20:23], v[244:247]\n"
    "ds_read_b64_tr_b16 a[158:159], v13 offset:20480\n"
    "v_mfma_f32_16x16x32_bf16 v[240:243], a[188:191], a[16:19], v[240:243]\n"
    "s_waitcnt vmcnt(28)\n"
    "s_waitcnt lgkmcnt(12)\n"
    "v_mfma_f32_16x16x32_bf16 v[128:131], a[128:131], a[32:35], v[128:131]\n"
    "ds_read_b64_tr_b16 a[160:161], v6 offset:24576\n"
    "v_mfma_f32_16x16x32_bf16 v[132:135], a[128:131], a[36:39], v[132:135]\n"
    "buffer_load_dwordx4 a[120:123], v4, s[20:23], s38 offen offset:3072 sc1\n"
    "v_mfma_f32_16x16x32_bf16 v[136:139], a[128:131], a[40:43], v[136:139]\n"
    "ds_read_b64_tr_b16 a[162:163], v6 offset:28672\n"
    "v_mfma_f32_16x16x32_bf16 v[140:143], a[128:131], a[44:47], v[140:143]\n"
    "v_add_f32_dpp v112, v112, v112 row_bcast:31 row_mask:0xc bank_mask:0xf\n"
    "v_mfma_f32_16x16x32_bf16 v[156:159], a[132:135], a[44:47], v[156:159]\n"
    "ds_read_b64_tr_b16 a[164:165], v7 offset:24576\n"
    "v_mfma_f32_16x16x32_bf16 v[152:155], a[132:135], a[40:43], v[152:155]\n"
    "buffer_load_dwordx4 a[124:127], v5, s[20:23], s38 offen offset:3072 sc1\n"
    "v_add_f32_dpp v113, v113, v113 row_bcast:31 row_mask:0xc bank_mask:0xf\n"
    "v_mfma_f32_16x16x32_bf16 v[148:151], a[132:135], a[36:39], v[148:151]\n"
    "ds_read_b64_tr_b16 a[166:167], v7 offset:28672\n"
    "v_mfma_f32_16x16x32_bf16 v[144:147], a[132:135], a[32:35], v[144:147]\n"
    "v_mul_f32 v112, 0xbfb8aa3b, v112\n"
    "s_waitcnt lgkmcnt(12)\n"
    "v_mfma_f32_16x16x32_bf16 v[160:163], a[136:139], a[32:35], v[160:163]\n"
    "ds_read_b64_tr_b16 a[168:169], v8 offset:24576\n"
    "v_mfma_f32_16x16x32_bf16 v[164:167], a[136:139], a[36:39], v[164:167]\n"
    "s_add_u32 s38, s38, 4096\n"
    "buffer_load_dwordx4 a[192:195], v2, s[20:23], s38 offen offset:0 sc1\n"
    "v_mul_f32 v113, 0xbfb8aa3b, v113\n"
    "v_mfma_f32_16x16x32_bf16 v[168:171], a[136:139], a[40:43], v[168:171]\n"
    "ds_read_b64_tr_b16 a[170:171], v8 offset:28672\n"
    "v_mfma_f32_16x16x32_bf16 v[172:175], a[136:139], a[44:47], v[172:175]\n"
    "v_exp_f32 v112, v112\n"
    "v_mfma_f32_16x16x32_bf16 v[188:191], a[140:143], a[44:47], v[188:191]\n"
    "ds_read_b64_tr_b16 a[172:173], v9 offset:24576\n"
    "v_mfma_f32_16x16x32_bf16 v[184:187], a[140:143], a[40:43], v[184:187]\n"
    "buffer_load_dwordx4 a[196:199], v3, s[20:23], s38 offen offset:0 sc1\n"
    "v_exp_f32 v113, v113\n"
    "v_mfma_f32_16x16x32_bf16 v[180:183], a[140:143], a[36:39], v[180:183]\n"
    "ds_read_b64_tr_b16 a[174:175], v9 offset:28672\n"
    "v_mfma_f32_16x16x32_bf16 v[176:179], a[140:143], a[32:35], v[176:179]\n"
    "v_add_f32 v112, 1.0, v112\n"
    "s_waitcnt lgkmcnt(12)\n"
    "v_mfma_f32_16x16x32_bf16 v[192:195], a[144:147], a[32:35], v[192:195]\n"
    "ds_read_b64_tr_b16 a[176:177], v10 offset:24576\n"
    "v_mfma_f32_16x16x32_bf16 v[196:199], a[144:147], a[36:39], v[196:199]\n"
    "buffer_load_dwordx4 a[200:203], v4, s[20:23], s38 offen offset:0 sc1\n"
    "v_add_f32 v113, 1.0, v113\n"
    "v_mfma_f32_16x16x32_bf16 v[200:203], a[144:147], a[40:43], v[200:203]\n"
    "ds_read_b64_tr_b16 a[178:179], v10 offset:28672\n"
    "v_mfma_f32_16x16x32_bf16 v[204:207], a[144:147], a[44:47], v[204:207]\n"
    "v_rcp_f32 v112, v112\n"
    "v_mfma_f32_16x16x32_bf16 v[220:223], a[148:151], a[44:47], v[220:223]\n"
    "ds_read_b64_tr_b16 a[180:181], v11 offset:24576\n"
    "v_mfma_f32_16x16x32_bf16 v[216:219], a[148:151], a[40:43], v[216:219]\n"
    "buffer_load_dwordx4 a[204:207], v5, s[20:23], s38 offen offset:0 sc1\n"
    "v_rcp_f32 v113, v113\n"
    "v_mfma_f32_16x16x32_bf16 v[212:215], a[148:151], a[36:39], v[212:215]\n"
    "ds_read_b64_tr_b16 a[182:183], v11 offset:28672\n"
    "v_mfma_f32_16x16x32_bf16 v[208:211], a[148:151], a[32:35], v[208:211]\n"
    "s_lshl_b32 s24, s39, 2\n"
    "s_add_u32 s24, s24, s34\n"
    "s_lshl_b32 s24, s24, 3\n"
    "s_add_u32 s24, s24, 40894464\n"
    "s_cmp_ge_u32 s39, 224\n"
    "s_cselect_b32 s24, 2146435072, s24\n"
    "s_waitcnt lgkmcnt(12)\n"
    "v_mfma_f32_16x16x32_bf16 v[224:227], a[152:155], a[32:35], v[224:227]\n"
    "ds_read_b64_tr_b16 a[184:185], v12 offset:24576\n"
    "v_mfma_f32_16x16x32_bf16 v[228:231], a[152:155], a[36:39], v[228:231]\n"
    "buffer_load_dwordx4 a[208:211], v2, s[20:23], s38 offen offset:1024 sc1\n"
    "v_mov_b32 v114, 0\n"
    "v_mfma_f32_16x16x32_bf16 v[232:235], a[152:155], a[40:43], v[232:235]\n"
    "ds_read_b64_tr_b16 a[186:187], v12 offset:28672\n"
    "v_mfma_f32_16x16x32_bf16 v[236:239], a[152:155], a[44:47], v[236:239]\n"
    "s_mov_b32 exec_lo, 0\n"
    "s_mov_b32 exec_hi, 0x80000000\n"
    "buffer_store_dwordx2 v[112:113], v114, s[20:23], s24 offen sc1\n"
    "s_mov_b64 exec, -1\n"
    "v_mfma_f32_16x16x32_bf16 v[252:255], a[156:159], a[44:47], v[252:255]\n"
    "ds_read_b64_tr_b16 a[188:189], v13 offset:24576\n"
    "v_mfma_f32_16x16x32_bf16 v[248:251], a[156:159], a[40:43], v[248:251]\n"
    "v_mfma_f32_16x16x32_bf16 v[244:247], a[156:159], a[36:39], v[244:247]\n"
    "ds_read_b64_tr_b16 a[190:191], v13 offset:28672\n"
    "v_mfma_f32_16x16x32_bf16 v[240:243], a[156:159], a[32:35], v[240:243]\n"
    "s_waitcnt vmcnt(32)\n"
    "s_barrier\n"
    "s_waitcnt lgkmcnt(12)\n"
    "v_mfma_f32_16x16x32_bf16 v[128:131], a[160:163], a[48:51], v[128:131]\n"
    "ds_read_b64_tr_b16 a[128:129], v6 offset:32768\n"
    "v_mfma_f32_16x16x32_bf16 v[132:135], a[160:163], a[52:55], v[132:135]\n"
    "buffer_load_dwordx4 a[212:215], v3, s[20:23], s38 offen offset:1024 sc1\n"
    "v_mfma_f32_16x16x32_bf16 v[136:139], a[160:163], a[56:59], v[136:139]\n"
    "ds_read_b64_tr_b16 a[130:131], v6 offset:36864\n"
    "v_mfma_f32_16x16x32_bf16 v[140:143], a[160:163], a[60:63], v[140:143]\n"
    "v_add_u32 v14, 0x10000, v6\n"
    "v_mfma_f32_16x16x32_bf16 v[156:159], a[164:167], a[60:63], v[156:159]\n"
    "ds_read_b64_tr_b16 a[132:133], v7 offset:32768\n"
    "v_mfma_f32_16x16x32_bf16 v[152:155], a[164:167], a[56:59], v[152:155]\n"
    "buffer_load_dwordx4 a[216:219], v4, s[20:23], s38 offen offset:1024 sc1\n"
    "v_mfma_f32_16x16x32_bf16 v[148:151], a[164:167], a[52:55], v[148:151]\n"
    "ds_read_b64_tr_b16 a[134:135], v7 offset:36864\n"
    "v_mfma_f32_16x16x32_bf16 v[144:147], a[164:167], a[48:51], v[144:147]\n"
    "v_add_u32 v15, 0x10000, v7\n"
    "s_waitcnt lgkmcnt(12)\n"
    "v_mfma_f32_16x16x32_bf16 v[160:163], a[168:171], a[48:51], v[160:163]\n"
    "ds_read_b64_tr_b16 a[136:137], v8 offset:32768\n"
    "v_mfma_f32_16x16x32_bf16 v[164:167], a[168:171], a[52:55], v[164:167]\n"
    "buffer_load_dwordx4 a[220:223], v5, s[20:23], s38 offen offset:1024 sc1\n"
    "v_mfma_f32_16x16x32_bf16 v[168:171], a[168:171], a[56:59], v[168:171]\n"
    "ds_read_b64_tr_b16 a[138:139], v8 offset:36864\n"
    "v_mfma_f32_16x16x32_bf16 v[172:175], a[168:171], a[60:63], v[172:175]\n"
    "v_add_u32 v16, 0x10000, v8\n"
    "v_mfma_f32_16x16x32_bf16 v[188:191], a[172:175], a[60:63], v[188:191]\n"
    "ds_read_b64_tr_b16 a[140:141], v9 offset:32768\n"
    "v_mfma_f32_16x16x32_bf16 v[184:187], a[172:175], a[56:59], v[184:187]\n"
    "s_add_u32 m0, s28, 98304\n"
    "s_nop 0\n"
    "buffer_load_dwordx4 v22, s[76:79], s25 offen offset:0 lds\n"
    "v_mfma_f32_16x16x32_bf16 v[180:183], a[172:175], a[52:55], v[180:183]\n"
    "ds_read_b64_tr_b16 a[142:143], v9 offset:36864\n"
    "v_mfma_f32_16x16x32_bf16 v[176:179], a[172:175], a[48:51], v[176:179]\n"
    "v_add_u32 v17, 0x10000, v9\n"
    "s_waitcnt lgkmcnt(12)\n"
    "v_mfma_f32_16x16x32_bf16 v[192:195], a[176:179], a[48:51], v[192:195]\n"
    "ds_read_b64_tr_b16 a[144:145], v10 offset:32768\n"
    "v_mfma_f32_16x16x32_bf16 v[196:199], a[176:179], a[52:55], v[196:199]\n"
    "buffer_load_dwordx4 v23, s[76:79], s25 offen offset:1024 lds\n"
    "v_mfma_f32_16x16x32_bf16 v[200:203], a[176:179], a[56:59], v[200:203]\n"
    "ds_read_b64_tr_b16 a[146:147], v10 offset:36864\n"
    "v_mfma_f32_16x16x32_bf16 v[204:207], a[176:179], a[60:63], v[204:207]\n"
    "v_add_u32 v18, 0x10000, v10\n"
    "v_mfma_f32_16x16x32_bf16 v[220:223], a[180:183], a[60:63], v[220:223]\n"
    "ds_read_b64_tr_b16 a[148:149], v11 offset:32768\n"
    "v_mfma_f32_16x16x32_bf16 v[216:219], a[180:183], a[56:59], v[216:219]\n"
    "buffer_load_dwordx4 v24, s[76:79], s25 offen offset:2048 lds\n"
    "v_mfma_f32_16x16x32_bf16 v[212:215], a[180:183], a[52:55], v[212:215]\n"
    "ds_read_b64_tr_b16 a[150:151], v11 offset:36864\n"
    "v_mfma_f32_16x16x32_bf16 v[208:211], a[180:183], a[48:51], v[208:211]\n"
    "v_add_u32 v19, 0x10000, v11\n"
    "s_waitcnt lgkmcnt(12)\n"
    "v_mfma_f32_16x16x32_bf16 v[224:227], a[184:187], a[48:51], v[224:227]\n"
    "ds_read_b64_tr_b16 a[152:153], v12 offset:32768\n"
    "v_mfma_f32_16x16x32_bf16 v[228:231], a[184:187], a[52:55], v[228:231]\n"
    "buffer_load_dwordx4 v25, s[76:79], s25 offen offset:3072 lds\n"
    "v_mfma_f32_16x16x32_bf16 v[232:235], a[184:187], a[56:59], v[232:235]\n"
    "ds_read_b64_tr_b16 a[154:155], v12 offset:36864\n"
    "v_mfma_f32_16x16x32_bf16 v[236:239], a[184:187], a[60:63], v[236:239]\n"
    "v_add_u32 v20, 0x10000, v12\n"
    "v_mfma_f32_16x16x32_bf16 v[252:255], a[188:191], a[60:63], v[252:255]\n"
    "ds_read_b64_tr_b16 a[156:157], v13 offset:32768\n"
    "v_mfma_f32_16x16x32_bf16 v[248:251], a[188:191], a[56:59], v[248:251]\n"
    "v_mfma_f32_16x16x32_bf16 v[244:247], a[188:191], a[52:55], v[244:247]\n"
    "ds_read_b64_tr_b16 a[158:159], v13 offset:36864\n"
    "v_mfma_f32_16x16x32_bf16 v[240:243], a[188:191], a[48:51], v[240:243]\n"
    "s_add_u32 s25, s25, 131072\n"
    "s_add_u32 m0, s28, 0\n"
    "s_nop 0\n"
    "s_waitcnt vmcnt(35)\n"
    "s_waitcnt lgkmcnt(12)\n"
    "v_mfma_f32_16x16x32_bf16 v[128:131], a[128:131], a[64:67], v[128:131]\n"
    "ds_read_b64_tr_b16 a[160:161], v6 offset:40960\n"
    "v_mfma_f32_16x16x32_bf16 v[132:135], a[128:131], a[68:71], v[132:135]\n"
    "buffer_load_dwordx4 v22, s[76:79], s25 offen offset:0 lds\n"
    "v_mfma_f32_16x16x32_bf16 v[136:139], a[128:131], a[72:75], v[136:139]\n"
    "ds_read_b64_tr_b16 a[162:163], v6 offset:45056\n"
    "v_mfma_f32_16x16x32_bf16 v[140:143], a[128:131], a[76:79], v[140:143]\n"
    "v_add_u32 v21, 0x10000, v13\n"
    "v_mfma_f32_16x16x32_bf16 v[156:159], a[132:135], a[76:79], v[156:159]\n"
    "ds_read_b64_tr_b16 a[164:165], v7 offset:40960\n"
    "v_mfma_f32_16x16x32_bf16 v[152:155], a[132:135], a[72:75], v[152:155]\n"
    "buffer_load_dwordx4 v23, s[76:79], s25 offen offset:1024 lds\n"
    "v_mfma_f32_16x16x32_bf16 v[148:151], a[132:135], a[68:71], v[148:151]\n"
    "ds_read_b64_tr_b16 a[166:167], v7 offset:45056\n"
    "v_mfma_f32_16x16x32_bf16 v[144:147], a[132:135], a[64:67], v[144:147]\n"
    "s_lshr_b32 s30, s39, 3\n"
    "s_waitcnt lgkmcnt(12)\n"
    "v_mfma_f32_16x16x32_bf16 v[160:163], a[136:139], a[64:67], v[160:163]\n"
    "ds_read_b64_tr_b16 a[168:169], v8 offset:40960\n"
    "v_mfma_f32_16x16x32_bf16 v[164:167], a[136:139], a[68:71], v[164:167]\n"
    "buffer_load_dwordx4 v24, s[76:79], s25 offen offset:2048 lds\n"
    "v_mfma_f32_16x16x32_bf16 v[168:171], a[136:139], a[72:75], v[168:171]\n"
    "ds_read_b64_tr_b16 a[170:171], v8 offset:45056\n"
    "v_mfma_f32_16x16x32_bf16 v[172:175], a[136:139], a[76:79], v[172:175]\n"
    "s_lshl_b32 s30, s30, 2\n"
    "v_mfma_f32_16x16x32_bf16 v[188:191], a[140:143], a[76:79], v[188:191]\n"
    "ds_read_b64_tr_b16 a[172:173], v9 offset:40960\n"
    "v_mfma_f32_16x16x32_bf16 v[184:187], a[140:143], a[72:75], v[184:187]\n"
    "buffer_load_dwordx4 v25, s[76:79], s25 offen offset:3072 lds\n"
    "v_mfma_f32_16x16x32_bf16 v[180:183], a[140:143], a[68:71], v[180:183]\n"
    "ds_read_b64_tr_b16 a[174:175], v9 offset:45056\n"
    "v_mfma_f32_16x16x32_bf16 v[176:179], a[140:143], a[64:67], v[176:179]\n"
    "s_add_u32 s30, s30, s34\n"
    "s_waitcnt lgkmcnt(12)\n"
    "v_mfma_f32_16x16x32_bf16 v[192:195], a[144:147], a[64:67], v[192:195]\n"
    "ds_read_b64_tr_b16 a[176:177], v10 offset:40960\n"
    "v_mfma_f32_16x16x32_bf16 v[196:199], a[144:147], a[68:71], v[196:199]\n"
    "s_add_u32 s25, s25, 131072\n"
    "buffer_load_dwordx4 a[224:227], v2, s[20:23], s38 offen offset:2048 sc1\n"
    "v_mfma_f32_16x16x32_bf16 v[200:203], a[144:147], a[72:75], v[200:203]\n"
    "ds_read_b64_tr_b16 a[178:179], v10 offset:45056\n"
    "v_mfma_f32_16x16x32_bf16 v[204:207], a[144:147], a[76:79], v[204:207]\n"
    "v_mul_u32_u24 v33, 128, v1\n"
    "v_mfma_f32_16x16x32_bf16 v[220:223], a[148:151], a[76:79], v[220:223]\n"
    "ds_read_b64_tr_b16 a[180:181], v11 offset:40960\n"
    "v_mfma_f32_16x16x32_bf16 v[216:219], a[148:151], a[72:75], v[216:219]\n"
    "buffer_load_dwordx4 a[228:231], v3, s[20:23], s38 offen offset:2048 sc1\n"
    "v_mfma_f32_16x16x32_bf16 v[212:215], a[148:151], a[68:71], v[212:215]\n"
    "ds_read_b64_tr_b16 a[182:183], v11 offset:45056\n"
    "v_mfma_f32_16x16x32_bf16 v[208:211], a[148:151], a[64:67], v[208:211]\n"
    "v_add_lshl_u32 v33, v33, s30, 7\n"
    "s_waitcnt lgkmcnt(12)\n"
    "v_mfma_f32_16x16x32_bf16 v[224:227], a[152:155], a[64:67], v[224:227]\n"
    "ds_read_b64_tr_b16 a[184:185], v12 offset:40960\n"
    "v_mfma_f32_16x16x32_bf16 v[228:231], a[152:155], a[68:71], v[228:231]\n"
    "buffer_load_dwordx4 a[232:235], v4, s[20:23], s38 offen offset:2048 sc1\n"
    "v_mfma_f32_16x16x32_bf16 v[232:235], a[152:155], a[72:75], v[232:235]\n"
    "ds_read_b64_tr_b16 a[186:187], v12 offset:45056\n"
    "v_mfma_f32_16x16x32_bf16 v[236:239], a[152:155], a[76:79], v[236:239]\n"
    "s_lshl_b32 s30, s35, 8\n"
    "v_mfma_f32_16x16x32_bf16 v[252:255], a[156:159], a[76:79], v[252:255]\n"
    "ds_read_b64_tr_b16 a[188:189], v13 offset:40960\n"
    "v_mfma_f32_16x16x32_bf16 v[248:251], a[156:159], a[72:75], v[248:251]\n"
    "v_mfma_f32_16x16x32_bf16 v[244:247], a[156:159], a[68:71], v[244:247]\n"
    "ds_read_b64_tr_b16 a[190:191], v13 offset:45056\n"
    "v_mfma_f32_16x16x32_bf16 v[240:243], a[156:159], a[64:67], v[240:243]\n"
    "s_waitcnt vmcnt(38)\n"
    "s_waitcnt lgkmcnt(12)\n"
    "v_mfma_f32_16x16x32_bf16 v[128:131], a[160:163], a[80:83], v[128:131]\n"
    "ds_read_b64_tr_b16 a[128:129], v6 offset:49152\n"
    "v_mfma_f32_16x16x32_bf16 v[132:135], a[160:163], a[84:87], v[132:135]\n"
    "buffer_load_dwordx4 a[236:239], v5, s[20:23], s38 offen offset:2048 sc1\n"
    "v_mfma_f32_16x16x32_bf16 v[136:139], a[160:163], a[88:91], v[136:139]\n"
    "ds_read_b64_tr_b16 a[130:131], v6 offset:53248\n"
    "v_mfma_f32_16x16x32_bf16 v[140:143], a[160:163], a[92:95], v[140:143]\n"
    "s_lshl_b32 s31, s34, 6\n"
    "v_mfma_f32_16x16x32_bf16 v[156:159], a[164:167], a[92:95], v[156:159]\n"
    "ds_read_b64_tr_b16 a[132:133], v7 offset:49152\n"
    "v_mfma_f32_16x16x32_bf16 v[152:155], a[164:167], a[88:91], v[152:155]\n"
    "buffer_load_dwordx4 a[240:243], v2, s[20:23], s38 offen offset:3072 sc1\n"
    "v_mfma_f32_16x16x32_bf16 v[148:151], a[164:167], a[84:87], v[148:151]\n"
    "ds_read_b64_tr_b16 a[134:135], v7 offset:53248\n"
    "v_mfma_f32_16x16x32_bf16 v[144:147], a[164:167], a[80:83], v[144:147]\n"
    "s_add_u32 s30, s30, s31\n"
    "s_waitcnt lgkmcnt(12)\n"
    "v_mfma_f32_16x16x32_bf16 v[160:163], a[168:171], a[80:83], v[160:163]\n"
    "ds_read_b64_tr_b16 a[136:137], v8 offset:49152\n"
    "v_mfma_f32_16x16x32_bf16 v[164:167], a[168:171], a[84:87], v[164:167]\n"
    "buffer_load_dwordx4 a[244:247], v3, s[20:23], s38 offen offset:3072 sc1\n"
    "v_mfma_f32_16x16x32_bf16 v[168:171], a[168:171], a[88:91], v[168:171]\n"
    "ds_read_b64_tr_b16 a[138:139], v8 offset:53248\n"
    "v_mfma_f32_16x16x32_bf16 v[172:175], a[168:171], a[92:95], v[172:175]\n"
    "v_and_b32 v38, 15, v1\n"
    "v_mfma_f32_16x16x32_bf16 v[188:191], a[172:175], a[92:95], v[188:191]\n"
    "ds_read_b64_tr_b16 a[140:141], v9 offset:49152\n"
    "v_mfma_f32_16x16x32_bf16 v[184:187], a[172:175], a[88:91], v[184:187]\n"
    "buffer_load_dwordx4 a[248:251], v4, s[20:23], s38 offen offset:3072 sc1\n"
    "v_mfma_f32_16x16x32_bf16 v[180:183], a[172:175], a[84:87], v[180:183]\n"
    "ds_read_b64_tr_b16 a[142:143], v9 offset:53248\n"
    "v_mfma_f32_16x16x32_bf16 v[176:179], a[172:175], a[80:83], v[176:179]\n"
    "v_add_u32 v38, s30, v38\n"
    "s_waitcnt lgkmcnt(12)\n"
    "v_mfma_f32_16x16x32_bf16 v[192:195], a[176:179], a[80:83], v[192:195]\n"
    "ds_read_b64_tr_b16 a[144:145], v10 offset:49152\n"
    "v_mfma_f32_16x16x32_bf16 v[196:199], a[176:179], a[84:87], v[196:199]\n"
    "buffer_load_dwordx4 a[252:255], v5, s[20:23], s38 offen offset:3072 sc1\n"
    "v_mfma_f32_16x16x32_bf16 v[200:203], a[176:179], a[88:91], v[200:203]\n"
    "ds_read_b64_tr_b16 a[146:147], v10 offset:53248\n"
    "v_mfma_f32_16x16x32_bf16 v[204:207], a[176:179], a[92:95], v[204:207]\n"
    "v_lshlrev_b32 v31, 2, v38\n"
    "v_mfma_f32_16x16x32_bf16 v[220:223], a[180:183], a[92:95], v[220:223]\n"
    "ds_read_b64_tr_b16 a[148:149], v11 offset:49152\n"
    "v_mfma_f32_16x16x32_bf16 v[216:219], a[180:183], a[88:91], v[216:219]\n"
    "s_add_u32 s38, s38, 4096\n"
    "buffer_load_dwordx4 a[0:3], v2, s[20:23], s38 offen offset:0 sc1\n"
    "v_mfma_f32_16x16x32_bf16 v[212:215], a[180:183], a[84:87], v[212:215]\n"
    "ds_read_b64_tr_b16 a[150:151], v11 offset:53248\n"
    "v_mfma_f32_16x16x32_bf16 v[208:211], a[180:183], a[80:83], v[208:211]\n"
    "v_add_u32 v31, 40894464, v31\n"
    "s_waitcnt lgkmcnt(12)\n"
    "v_mfma_f32_16x16x32_bf16 v[224:227], a[184:187], a[80:83], v[224:227]\n"
    "ds_read_b64_tr_b16 a[152:153], v12 offset:49152\n"
    "v_mfma_f32_16x16x32_bf16 v[228:231], a[184:187], a[84:87], v[228:231]\n"
    "buffer_load_dwordx4 a[4:7], v3, s[20:23], s38 offen offset:0 sc1\n"
    "v_mfma_f32_16x16x32_bf16 v[232:235], a[184:187], a[88:91], v[232:235]\n"
    "ds_read_b64_tr_b16 a[154:155], v12 offset:53248\n"
    "v_mfma_f32_16x16x32_bf16 v[236:239], a[184:187], a[92:95], v[236:239]\n"
    "v_lshlrev_b32 v26, 11, v38\n"
    "v_mfma_f32_16x16x32_bf16 v[252:255], a[188:191], a[92:95], v[252:255]\n"
    "ds_read_b64_tr_b16 a[156:157], v13 offset:49152\n"
    "v_mfma_f32_16x16x32_bf16 v[248:251], a[188:191], a[88:91], v[248:251]\n"
    "v_mfma_f32_16x16x32_bf16 v[244:247], a[188:191], a[84:87], v[244:247]\n"
    "ds_read_b64_tr_b16 a[158:159], v13 offset:53248\n"
    "v_mfma_f32_16x16x32_bf16 v[240:243], a[188:191], a[80:83], v[240:243]\n"
    "s_waitcnt vmcnt(31)\n"
    "s_waitcnt lgkmcnt(12)\n"
    "v_mfma_f32_16x16x32_bf16 v[128:131], a[128:131], a[96:99], v[128:131]\n"
    "ds_read_b64_tr_b16 a[160:161], v6 offset:57344\n"
    "v_mfma_f32_16x16x32_bf16 v[132:135], a[128:131], a[100:103], v[132:135]\n"
    "buffer_load_dwordx4 a[8:11], v4, s[20:23], s38 offen offset:0 sc1\n"
    "v_mfma_f32_16x16x32_bf16 v[136:139], a[128:131], a[104:107], v[136:139]\n"
    "ds_read_b64_tr_b16 a[162:163], v6 offset:61440\n"
    "v_mfma_f32_16x16x32_bf16 v[140:143], a[128:131], a[108:111], v[140:143]\n"
    "s_lshl_b32 s31, s36, 7\n"
    "v_mfma_f32_16x16x32_bf16 v[156:159], a[132:135], a[108:111], v[156:159]\n"
    "ds_read_b64_tr_b16 a[164:165], v7 offset:57344\n"
    "v_mfma_f32_16x16x32_bf16 v[152:155], a[132:135], a[104:107], v[152:155]\n"
    "buffer_load_dwordx4 a[12:15], v5, s[20:23], s38 offen offset:0 sc1\n"
    "v_mfma_f32_16x16x32_bf16 v[148:151], a[132:135], a[100:103], v[148:151]\n"
    "ds_read_b64_tr_b16 a[166:167], v7 offset:61440\n"
    "v_mfma_f32_16x16x32_bf16 v[144:147], a[132:135], a[96:99], v[144:147]\n"
    "s_lshl_b32 s66, s37, 5\n"
    "s_waitcnt lgkmcnt(12)\n"
    "v_mfma_f32_16x16x32_bf16 v[160:163], a[136:139], a[96:99], v[160:163]\n"
    "ds_read_b64_tr_b16 a[168:169], v8 offset:57344\n"
    "v_mfma_f32_16x16x32_bf16 v[164:167], a[136:139], a[100:103], v[164:167]\n"
    "buffer_load_dwordx4 a[16:19], v2, s[20:23], s38 offen offset:1024 sc1\n"
    "v_mfma_f32_16x16x32_bf16 v[168:171], a[136:139], a[104:107], v[168:171]\n"
    "ds_read_b64_tr_b16 a[170:171], v8 offset:61440\n"
    "v_mfma_f32_16x16x32_bf16 v[172:175], a[136:139], a[108:111], v[172:175]\n"
    "s_add_u32 s31, s31, s66\n"
    "v_mfma_f32_16x16x32_bf16 v[188:191], a[140:143], a[108:111], v[188:191]\n"
    "ds_read_b64_tr_b16 a[172:173], v9 offset:57344\n"
    "v_mfma_f32_16x16x32_bf16 v[184:187], a[140:143], a[104:107], v[184:187]\n"
    "buffer_load_dwordx4 a[20:23], v3, s[20:23], s38 offen offset:1024 sc1\n"
    "v_mfma_f32_16x16x32_bf16 v[180:183], a[140:143], a[100:103], v[180:183]\n"
    "ds_read_b64_tr_b16 a[174:175], v9 offset:61440\n"
    "v_mfma_f32_16x16x32_bf16 v[176:179], a[140:143], a[96:99], v[176:179]\n"
    "v_bfe_u32 v38, v1, 4, 1\n"
    "s_waitcnt lgkmcnt(12)\n"
    "v_mfma_f32_16x16x32_bf16 v[192:195], a[144:147], a[96:99], v[192:195]\n"
    "ds_read_b64_tr_b16 a[176:177], v10 offset:57344\n"
    "v_mfma_f32_16x16x32_bf16 v[196:199], a[144:147], a[100:103], v[196:199]\n"
    "buffer_load_dwordx4 a[24:27], v4, s[20:23], s38 offen offset:1024 sc1\n"
    "v_mfma_f32_16x16x32_bf16 v[200:203], a[144:147], a[104:107], v[200:203]\n"
    "ds_read_b64_tr_b16 a[178:179], v10 offset:61440\n"
    "v_mfma_f32_16x16x32_bf16 v[204:207], a[144:147], a[108:111], v[204:207]\n"
    "v_lshl_add_u32 v26, v38, 5, v26\n"
    "v_mfma_f32_16x16x32_bf16 v[220:223], a[148:151], a[108:111], v[220:223]\n"
    "ds_read_b64_tr_b16 a[180:181], v11 offset:57344\n"
    "v_mfma_f32_16x16x32_bf16 v[216:219], a[148:151], a[104:107], v[216:219]\n"
    "buffer_load_dwordx4 a[28:31], v5, s[20:23], s38 offen offset:1024 sc1\n"
    "v_mfma_f32_16x16x32_bf16 v[212:215], a[148:151], a[100:103], v[212:215]\n"
    "ds_read_b64_tr_b16 a[182:183], v11 offset:61440\n"
    "v_mfma_f32_16x16x32_bf16 v[208:211], a[148:151], a[96:99], v[208:211]\n"
    "v_lshrrev_b32 v38, 5, v1\n"
    "s_waitcnt lgkmcnt(12)\n"
    "v_mfma_f32_16x16x32_bf16 v[224:227], a[152:155], a[96:99], v[224:227]\n"
    "ds_read_b64_tr_b16 a[184:185], v12 offset:57344\n"
    "v_mfma_f32_16x16x32_bf16 v[228:231], a[152:155], a[100:103], v[228:231]\n"
    "buffer_load_dwordx4 a[32:35], v2, s[20:23], s38 offen offset:2048 sc1\n"
    "v_mfma_f32_16x16x32_bf16 v[232:235], a[152:155], a[104:107], v[232:235]\n"
    "ds_read_b64_tr_b16 a[186:187], v12 offset:61440\n"
    "v_mfma_f32_16x16x32_bf16 v[236:239], a[152:155], a[108:111], v[236:239]\n"
    "v_lshl_add_u32 v26, v38, 4, v26\n"
    "v_mfma_f32_16x16x32_bf16 v[252:255], a[156:159], a[108:111], v[252:255]\n"
    "ds_read_b64_tr_b16 a[188:189], v13 offset:57344\n"
    "v_mfma_f32_16x16x32_bf16 v[248:251], a[156:159], a[104:107], v[248:251]\n"
    "v_mfma_f32_16x16x32_bf16 v[244:247], a[156:159], a[100:103], v[244:247]\n"
    "ds_read_b64_tr_b16 a[190:191], v13 offset:61440\n"
    "v_mfma_f32_16x16x32_bf16 v[240:243], a[156:159], a[96:99], v[240:243]\n"
    "s_waitcnt vmcnt(28)\n"
    "s_barrier\n"
    "s_cmp_lg_u32 s34, 0\n"
    "s_cbranch_scc1 XC_gated_mix__ssm_conv_h3__swiglu_ffn__prompt_kernel\n"
    "s_lshl_b32 s27, s39, 3\n"
    "s_add_u32 s27, s27, 41000960\n"
    "v_mov_b32 v48, s70\n"
    "v_mov_b32 v49, s71\n"
    "v_mov_b32 v50, 0\n"
    "s_mov_b64 exec, 1\n"
    "buffer_store_dwordx2 v[48:49], v50, s[20:23], s27 offen sc1\n"
    "s_mov_b64 exec, -1\n"
    "XC_gated_mix__ssm_conv_h3__swiglu_ffn__prompt_kernel:\n"
    "s_waitcnt lgkmcnt(12)\n"
    "v_mfma_f32_16x16x32_bf16 v[128:131], a[160:163], a[112:115], v[128:131]\n"
    "ds_read_b64_tr_b16 a[128:129], v14 offset:0\n"
    "v_mfma_f32_16x16x32_bf16 v[132:135], a[160:163], a[116:119], v[132:135]\n"
    "s_add_u32 m0, s28, 16384\n"
    "s_nop 0\n"
    "buffer_load_dwordx4 v22, s[76:79], s25 offen offset:0 lds\n"
    "v_mfma_f32_16x16x32_bf16 v[136:139], a[160:163], a[120:123], v[136:139]\n"
    "ds_read_b64_tr_b16 a[130:131], v14 offset:4096\n"
    "v_mfma_f32_16x16x32_bf16 v[140:143], a[160:163], a[124:127], v[140:143]\n"
    "s_lshl_b32 s67, s31, 1\n"
    "v_mfma_f32_16x16x32_bf16 v[156:159], a[164:167], a[124:127], v[156:159]\n"
    "ds_read_b64_tr_b16 a[132:133], v15 offset:0\n"
    "v_mfma_f32_16x16x32_bf16 v[152:155], a[164:167], a[120:123], v[152:155]\n"
    "buffer_load_dwordx4 v23, s[76:79], s25 offen offset:1024 lds\n"
    "v_mfma_f32_16x16x32_bf16 v[148:151], a[164:167], a[116:119], v[148:151]\n"
    "ds_read_b64_tr_b16 a[134:135], v15 offset:4096\n"
    "v_mfma_f32_16x16x32_bf16 v[144:147], a[164:167], a[112:115], v[144:147]\n"
    "v_add_u32 v26, s67, v26\n"
    "s_waitcnt lgkmcnt(12)\n"
    "v_mfma_f32_16x16x32_bf16 v[160:163], a[168:171], a[112:115], v[160:163]\n"
    "ds_read_b64_tr_b16 a[136:137], v16 offset:0\n"
    "v_mfma_f32_16x16x32_bf16 v[164:167], a[168:171], a[116:119], v[164:167]\n"
    "buffer_load_dwordx4 v24, s[76:79], s25 offen offset:2048 lds\n"
    "v_mfma_f32_16x16x32_bf16 v[168:171], a[168:171], a[120:123], v[168:171]\n"
    "ds_read_b64_tr_b16 a[138:139], v16 offset:4096\n"
    "v_mfma_f32_16x16x32_bf16 v[172:175], a[168:171], a[124:127], v[172:175]\n"
    "v_add_u32 v27, 32768, v26\n"
    "v_mfma_f32_16x16x32_bf16 v[188:191], a[172:175], a[124:127], v[188:191]\n"
    "ds_read_b64_tr_b16 a[140:141], v17 offset:0\n"
    "v_mfma_f32_16x16x32_bf16 v[184:187], a[172:175], a[120:123], v[184:187]\n"
    "buffer_load_dwordx4 v25, s[76:79], s25 offen offset:3072 lds\n"
    "v_mfma_f32_16x16x32_bf16 v[180:183], a[172:175], a[116:119], v[180:183]\n"
    "ds_read_b64_tr_b16 a[142:143], v17 offset:4096\n"
    "v_mfma_f32_16x16x32_bf16 v[176:179], a[172:175], a[112:115], v[176:179]\n"
    "v_add_u32 v28, 65536, v26\n"
    "s_waitcnt lgkmcnt(12)\n"
    "v_mfma_f32_16x16x32_bf16 v[192:195], a[176:179], a[112:115], v[192:195]\n"
    "ds_read_b64_tr_b16 a[144:145], v18 offset:0\n"
    "v_mfma_f32_16x16x32_bf16 v[196:199], a[176:179], a[116:119], v[196:199]\n"
    "s_add_u32 s25, s25, 131072\n"
    "s_add_u32 m0, s28, 32768\n"
    "s_nop 0\n"
    "buffer_load_dwordx4 v22, s[76:79], s25 offen offset:0 lds\n"
    "v_mfma_f32_16x16x32_bf16 v[200:203], a[176:179], a[120:123], v[200:203]\n"
    "ds_read_b64_tr_b16 a[146:147], v18 offset:4096\n"
    "v_mfma_f32_16x16x32_bf16 v[204:207], a[176:179], a[124:127], v[204:207]\n"
    "v_add_u32 v29, 98304, v26\n"
    "v_mfma_f32_16x16x32_bf16 v[220:223], a[180:183], a[124:127], v[220:223]\n"
    "ds_read_b64_tr_b16 a[148:149], v19 offset:0\n"
    "v_mfma_f32_16x16x32_bf16 v[216:219], a[180:183], a[120:123], v[216:219]\n"
    "buffer_load_dwordx4 v23, s[76:79], s25 offen offset:1024 lds\n"
    "v_mfma_f32_16x16x32_bf16 v[212:215], a[180:183], a[116:119], v[212:215]\n"
    "ds_read_b64_tr_b16 a[150:151], v19 offset:4096\n"
    "v_mfma_f32_16x16x32_bf16 v[208:211], a[180:183], a[112:115], v[208:211]\n"
    "v_lshrrev_b32 v30, 3, v1\n"
    "s_waitcnt lgkmcnt(12)\n"
    "v_mfma_f32_16x16x32_bf16 v[224:227], a[184:187], a[112:115], v[224:227]\n"
    "ds_read_b64_tr_b16 a[152:153], v20 offset:0\n"
    "v_mfma_f32_16x16x32_bf16 v[228:231], a[184:187], a[116:119], v[228:231]\n"
    "buffer_load_dwordx4 v24, s[76:79], s25 offen offset:2048 lds\n"
    "v_mfma_f32_16x16x32_bf16 v[232:235], a[184:187], a[120:123], v[232:235]\n"
    "ds_read_b64_tr_b16 a[154:155], v20 offset:4096\n"
    "v_mfma_f32_16x16x32_bf16 v[236:239], a[184:187], a[124:127], v[236:239]\n"
    "v_mul_u32_u24 v30, 4224, v30\n"
    "v_mfma_f32_16x16x32_bf16 v[252:255], a[188:191], a[124:127], v[252:255]\n"
    "ds_read_b64_tr_b16 a[156:157], v21 offset:0\n"
    "v_mfma_f32_16x16x32_bf16 v[248:251], a[188:191], a[120:123], v[248:251]\n"
    "v_mfma_f32_16x16x32_bf16 v[244:247], a[188:191], a[116:119], v[244:247]\n"
    "ds_read_b64_tr_b16 a[158:159], v21 offset:4096\n"
    "v_mfma_f32_16x16x32_bf16 v[240:243], a[188:191], a[112:115], v[240:243]\n"
    "s_waitcnt lgkmcnt(12)\n"
    "v_mfma_f32_16x16x32_bf16 v[128:131], a[128:131], a[192:195], v[128:131]\n"
    "ds_read_b64_tr_b16 a[160:161], v14 offset:8192\n"
    "v_mfma_f32_16x16x32_bf16 v[132:135], a[128:131], a[196:199], v[132:135]\n"
    "buffer_load_dwordx4 v25, s[76:79], s25 offen offset:3072 lds\n"
    "v_mfma_f32_16x16x32_bf16 v[136:139], a[128:131], a[200:203], v[136:139]\n"
    "ds_read_b64_tr_b16 a[162:163], v14 offset:12288\n"
    "v_mfma_f32_16x16x32_bf16 v[140:143], a[128:131], a[204:207], v[140:143]\n"
    "buffer_load_dword v39, v33, s[92:95], 0 offen\n"
    "v_mfma_f32_16x16x32_bf16 v[156:159], a[132:135], a[204:207], v[156:159]\n"
    "ds_read_b64_tr_b16 a[164:165], v15 offset:8192\n"
    "v_mfma_f32_16x16x32_bf16 v[152:155], a[132:135], a[200:203], v[152:155]\n"
    "s_add_u32 s25, s25, 131072\n"
    "buffer_load_dwordx4 a[36:39], v3, s[20:23], s38 offen offset:2048 sc1\n"
    "v_mfma_f32_16x16x32_bf16 v[148:151], a[132:135], a[196:199], v[148:151]\n"
    "ds_read_b64_tr_b16 a[166:167], v15 offset:12288\n"
    "v_mfma_f32_16x16x32_bf16 v[144:147], a[132:135], a[192:195], v[144:147]\n"
    "s_waitcnt lgkmcnt(12)\n"
    "v_mfma_f32_16x16x32_bf16 v[160:163], a[136:139], a[192:195], v[160:163]\n"
    "ds_read_b64_tr_b16 a[168:169], v16 offset:8192\n"
    "v_mfma_f32_16x16x32_bf16 v[164:167], a[136:139], a[196:199], v[164:167]\n"
    "buffer_load_dwordx4 a[40:43], v4, s[20:23], s38 offen offset:2048 sc1\n"
    "v_mfma_f32_16x16x32_bf16 v[168:171], a[136:139], a[200:203], v[168:171]\n"
    "ds_read_b64_tr_b16 a[170:171], v16 offset:12288\n"
    "v_mfma_f32_16x16x32_bf16 v[172:175], a[136:139], a[204:207], v[172:175]\n"
    "v_mfma_f32_16x16x32_bf16 v[188:191], a[140:143], a[204:207], v[188:191]\n"
    "ds_read_b64_tr_b16 a[172:173], v17 offset:8192\n"
    "v_mfma_f32_16x16x32_bf16 v[184:187], a[140:143], a[200:203], v[184:187]\n"
    "buffer_load_dwordx4 a[44:47], v5, s[20:23], s38 offen offset:2048 sc1\n"
    "v_mfma_f32_16x16x32_bf16 v[180:183], a[140:143], a[196:199], v[180:183]\n"
    "ds_read_b64_tr_b16 a[174:175], v17 offset:12288\n"
    "v_mfma_f32_16x16x32_bf16 v[176:179], a[140:143], a[192:195], v[176:179]\n"
    "s_waitcnt lgkmcnt(12)\n"
    "v_mfma_f32_16x16x32_bf16 v[192:195], a[144:147], a[192:195], v[192:195]\n"
    "ds_read_b64_tr_b16 a[176:177], v18 offset:8192\n"
    "v_mfma_f32_16x16x32_bf16 v[196:199], a[144:147], a[196:199], v[196:199]\n"
    "buffer_load_dwordx4 a[48:51], v2, s[20:23], s38 offen offset:3072 sc1\n"
    "v_mfma_f32_16x16x32_bf16 v[200:203], a[144:147], a[200:203], v[200:203]\n"
    "ds_read_b64_tr_b16 a[178:179], v18 offset:12288\n"
    "v_mfma_f32_16x16x32_bf16 v[204:207], a[144:147], a[204:207], v[204:207]\n"
    "v_mfma_f32_16x16x32_bf16 v[220:223], a[148:151], a[204:207], v[220:223]\n"
    "ds_read_b64_tr_b16 a[180:181], v19 offset:8192\n"
    "v_mfma_f32_16x16x32_bf16 v[216:219], a[148:151], a[200:203], v[216:219]\n"
    "buffer_load_dwordx4 a[52:55], v3, s[20:23], s38 offen offset:3072 sc1\n"
    "v_mfma_f32_16x16x32_bf16 v[212:215], a[148:151], a[196:199], v[212:215]\n"
    "ds_read_b64_tr_b16 a[182:183], v19 offset:12288\n"
    "v_mfma_f32_16x16x32_bf16 v[208:211], a[148:151], a[192:195], v[208:211]\n"
    "s_waitcnt lgkmcnt(12)\n"
    "v_mfma_f32_16x16x32_bf16 v[224:227], a[152:155], a[192:195], v[224:227]\n"
    "ds_read_b64_tr_b16 a[184:185], v20 offset:8192\n"
    "v_mfma_f32_16x16x32_bf16 v[228:231], a[152:155], a[196:199], v[228:231]\n"
    "buffer_load_dwordx4 a[56:59], v4, s[20:23], s38 offen offset:3072 sc1\n"
    "v_mfma_f32_16x16x32_bf16 v[232:235], a[152:155], a[200:203], v[232:235]\n"
    "ds_read_b64_tr_b16 a[186:187], v20 offset:12288\n"
    "v_mfma_f32_16x16x32_bf16 v[236:239], a[152:155], a[204:207], v[236:239]\n"
    "v_mfma_f32_16x16x32_bf16 v[252:255], a[156:159], a[204:207], v[252:255]\n"
    "ds_read_b64_tr_b16 a[188:189], v21 offset:8192\n"
    "v_mfma_f32_16x16x32_bf16 v[248:251], a[156:159], a[200:203], v[248:251]\n"
    "v_mfma_f32_16x16x32_bf16 v[244:247], a[156:159], a[196:199], v[244:247]\n"
    "ds_read_b64_tr_b16 a[190:191], v21 offset:12288\n"
    "v_mfma_f32_16x16x32_bf16 v[240:243], a[156:159], a[192:195], v[240:243]\n"
    "s_waitcnt vmcnt(40)\n"
    "s_waitcnt lgkmcnt(12)\n"
    "v_mfma_f32_16x16x32_bf16 v[128:131], a[160:163], a[208:211], v[128:131]\n"
    "ds_read_b64_tr_b16 a[128:129], v14 offset:16384\n"
    "v_mfma_f32_16x16x32_bf16 v[132:135], a[160:163], a[212:215], v[132:135]\n"
    "buffer_load_dwordx4 a[60:63], v5, s[20:23], s38 offen offset:3072 sc1\n"
    "v_mfma_f32_16x16x32_bf16 v[136:139], a[160:163], a[216:219], v[136:139]\n"
    "ds_read_b64_tr_b16 a[130:131], v14 offset:20480\n"
    "v_mfma_f32_16x16x32_bf16 v[140:143], a[160:163], a[220:223], v[140:143]\n"
    "v_and_b32 v38, 7, v1\n"
    "v_mfma_f32_16x16x32_bf16 v[156:159], a[164:167], a[220:223], v[156:159]\n"
    "ds_read_b64_tr_b16 a[132:133], v15 offset:16384\n"
    "v_mfma_f32_16x16x32_bf16 v[152:155], a[164:167], a[216:219], v[152:155]\n"
    "s_add_u32 s38, s38, 4096\n"
    "buffer_load_dwordx4 a[64:67], v2, s[20:23], s38 offen offset:0 sc1\n"
    "v_lshl_add_u32 v30, v38, 4, v30\n"
    "v_mfma_f32_16x16x32_bf16 v[148:151], a[164:167], a[212:215], v[148:151]\n"
    "ds_read_b64_tr_b16 a[134:135], v15 offset:20480\n"
    "v_mfma_f32_16x16x32_bf16 v[144:147], a[164:167], a[208:211], v[144:147]\n"
    "s_waitcnt lgkmcnt(12)\n"
    "v_mfma_f32_16x16x32_bf16 v[160:163], a[168:171], a[208:211], v[160:163]\n"
    "ds_read_b64_tr_b16 a[136:137], v16 offset:16384\n"
    "v_mfma_f32_16x16x32_bf16 v[164:167], a[168:171], a[212:215], v[164:167]\n"
    "buffer_load_dwordx4 a[68:71], v3, s[20:23], s38 offen offset:0 sc1\n"
    "s_mul_i32 s66, s31, 4224\n"
    "v_mfma_f32_16x16x32_bf16 v[168:171], a[168:171], a[216:219], v[168:171]\n"
    "ds_read_b64_tr_b16 a[138:139], v16 offset:20480\n"
    "v_mfma_f32_16x16x32_bf16 v[172:175], a[168:171], a[220:223], v[172:175]\n"
    "v_mfma_f32_16x16x32_bf16 v[188:191], a[172:175], a[220:223], v[188:191]\n"
    "ds_read_b64_tr_b16 a[140:141], v17 offset:16384\n"
    "v_mfma_f32_16x16x32_bf16 v[184:187], a[172:175], a[216:219], v[184:187]\n"
    "buffer_load_dwordx4 a[72:75], v4, s[20:23], s38 offen offset:0 sc1\n"
    "s_lshl_b32 s30, s30, 1\n"
    "v_mfma_f32_16x16x32_bf16 v[180:183], a[172:175], a[212:215], v[180:183]\n"
    "ds_read_b64_tr_b16 a[142:143], v17 offset:20480\n"
    "v_mfma_f32_16x16x32_bf16 v[176:179], a[172:175], a[208:211], v[176:179]\n"
    "s_waitcnt lgkmcnt(12)\n"
    "v_mfma_f32_16x16x32_bf16 v[192:195], a[176:179], a[208:211], v[192:195]\n"
    "ds_read_b64_tr_b16 a[144:145], v18 offset:16384\n"
    "v_mfma_f32_16x16x32_bf16 v[196:199], a[176:179], a[212:215], v[196:199]\n"
    "buffer_load_dwordx4 a[76:79], v5, s[20:23], s38 offen offset:0 sc1\n"
    "s_add_u32 s66, s66, s30\n"
    "v_mfma_f32_16x16x32_bf16 v[200:203], a[176:179], a[216:219], v[200:203]\n"
    "ds_read_b64_tr_b16 a[146:147], v18 offset:20480\n"
    "v_mfma_f32_16x16x32_bf16 v[204:207], a[176:179], a[220:223], v[204:207]\n"
    "s_add_u32 s75, s66, 13631488\n"
    "v_mfma_f32_16x16x32_bf16 v[220:223], a[180:183], a[220:223], v[220:223]\n"
    "ds_read_b64_tr_b16 a[148:149], v19 offset:16384\n"
    "v_mfma_f32_16x16x32_bf16 v[216:219], a[180:183], a[216:219], v[216:219]\n"
    "buffer_load_dwordx4 a[80:83], v2, s[20:23], s38 offen offset:1024 sc1\n"
    "v_mfma_f32_16x16x32_bf16 v[212:215], a[180:183], a[212:215], v[212:215]\n"
    "ds_read_b64_tr_b16 a[150:151], v19 offset:20480\n"
    "v_mfma_f32_16x16x32_bf16 v[208:211], a[180:183], a[208:211], v[208:211]\n"
    "v_lshlrev_b32 v32, 5, v1\n"
    "s_waitcnt lgkmcnt(12)\n"
    "v_mfma_f32_16x16x32_bf16 v[224:227], a[184:187], a[208:211], v[224:227]\n"
    "ds_read_b64_tr_b16 a[152:153], v20 offset:16384\n"
    "v_mfma_f32_16x16x32_bf16 v[228:231], a[184:187], a[212:215], v[228:231]\n"
    "buffer_load_dwordx4 a[84:87], v3, s[20:23], s38 offen offset:1024 sc1\n"
    "v_mfma_f32_16x16x32_bf16 v[232:235], a[184:187], a[216:219], v[232:235]\n"
    "ds_read_b64_tr_b16 a[154:155], v20 offset:20480\n"
    "v_mfma_f32_16x16x32_bf16 v[236:239], a[184:187], a[220:223], v[236:239]\n"
    "v_add_u32 v32, 41000960, v32\n"
    "v_mfma_f32_16x16x32_bf16 v[252:255], a[188:191], a[220:223], v[252:255]\n"
    "ds_read_b64_tr_b16 a[156:157], v21 offset:16384\n"
    "v_mfma_f32_16x16x32_bf16 v[248:251], a[188:191], a[216:219], v[248:251]\n"
    "v_mfma_f32_16x16x32_bf16 v[244:247], a[188:191], a[212:215], v[244:247]\n"
    "ds_read_b64_tr_b16 a[158:159], v21 offset:20480\n"
    "v_mfma_f32_16x16x32_bf16 v[240:243], a[188:191], a[208:211], v[240:243]\n"
    "s_waitcnt vmcnt(35)\n"
    "s_waitcnt lgkmcnt(12)\n"
    "v_mfma_f32_16x16x32_bf16 v[128:131], a[128:131], a[224:227], v[128:131]\n"
    "ds_read_b64_tr_b16 a[160:161], v14 offset:24576\n"
    "v_mfma_f32_16x16x32_bf16 v[132:135], a[128:131], a[228:231], v[132:135]\n"
    "buffer_load_dwordx4 a[88:91], v4, s[20:23], s38 offen offset:1024 sc1\n"
    "v_mfma_f32_16x16x32_bf16 v[136:139], a[128:131], a[232:235], v[136:139]\n"
    "ds_read_b64_tr_b16 a[162:163], v14 offset:28672\n"
    "v_mfma_f32_16x16x32_bf16 v[140:143], a[128:131], a[236:239], v[140:143]\n"
    "buffer_load_dwordx4 a[92:95], v5, s[20:23], s38 offen offset:1024 sc1\n"
    "v_mfma_f32_16x16x32_bf16 v[156:159], a[132:135], a[236:239], v[156:159]\n"
    "ds_read_b64_tr_b16 a[164:165], v15 offset:24576\n"
    "v_mfma_f32_16x16x32_bf16 v[152:155], a[132:135], a[232:235], v[152:155]\n"
    "v_mfma_f32_16x16x32_bf16 v[148:151], a[132:135], a[228:231], v[148:151]\n"
    "ds_read_b64_tr_b16 a[166:167], v15 offset:28672\n"
    "v_mfma_f32_16x16x32_bf16 v[144:147], a[132:135], a[224:227], v[144:147]\n"
    "buffer_load_dwordx4 a[96:99], v2, s[20:23], s38 offen offset:2048 sc1\n"
    "s_waitcnt lgkmcnt(12)\n"
    "v_mfma_f32_16x16x32_bf16 v[160:163], a[136:139], a[224:227], v[160:163]\n"
    "ds_read_b64_tr_b16 a[168:169], v16 offset:24576\n"
    "v_mfma_f32_16x16x32_bf16 v[164:167], a[136:139], a[228:231], v[164:167]\n"
    "v_mfma_f32_16x16x32_bf16 v[168:171], a[136:139], a[232:235], v[168:171]\n"
    "ds_read_b64_tr_b16 a[170:171], v16 offset:28672\n"
    "v_mfma_f32_16x16x32_bf16 v[172:175], a[136:139], a[236:239], v[172:175]\n"
    "buffer_load_dwordx4 a[100:103], v3, s[20:23], s38 offen offset:2048 sc1\n"
    "v_mfma_f32_16x16x32_bf16 v[188:191], a[140:143], a[236:239], v[188:191]\n"
    "ds_read_b64_tr_b16 a[172:173], v17 offset:24576\n"
    "v_mfma_f32_16x16x32_bf16 v[184:187], a[140:143], a[232:235], v[184:187]\n"
    "v_mfma_f32_16x16x32_bf16 v[180:183], a[140:143], a[228:231], v[180:183]\n"
    "ds_read_b64_tr_b16 a[174:175], v17 offset:28672\n"
    "v_mfma_f32_16x16x32_bf16 v[176:179], a[140:143], a[224:227], v[176:179]\n"
    "buffer_load_dwordx4 a[104:107], v4, s[20:23], s38 offen offset:2048 sc1\n"
    "s_waitcnt lgkmcnt(12)\n"
    "v_mfma_f32_16x16x32_bf16 v[192:195], a[144:147], a[224:227], v[192:195]\n"
    "ds_read_b64_tr_b16 a[176:177], v18 offset:24576\n"
    "v_mfma_f32_16x16x32_bf16 v[196:199], a[144:147], a[228:231], v[196:199]\n"
    "v_mfma_f32_16x16x32_bf16 v[200:203], a[144:147], a[232:235], v[200:203]\n"
    "ds_read_b64_tr_b16 a[178:179], v18 offset:28672\n"
    "v_mfma_f32_16x16x32_bf16 v[204:207], a[144:147], a[236:239], v[204:207]\n"
    "buffer_load_dwordx4 a[108:111], v5, s[20:23], s38 offen offset:2048 sc1\n"
    "v_mfma_f32_16x16x32_bf16 v[220:223], a[148:151], a[236:239], v[220:223]\n"
    "ds_read_b64_tr_b16 a[180:181], v19 offset:24576\n"
    "v_mfma_f32_16x16x32_bf16 v[216:219], a[148:151], a[232:235], v[216:219]\n"
    "v_mfma_f32_16x16x32_bf16 v[212:215], a[148:151], a[228:231], v[212:215]\n"
    "ds_read_b64_tr_b16 a[182:183], v19 offset:28672\n"
    "v_mfma_f32_16x16x32_bf16 v[208:211], a[148:151], a[224:227], v[208:211]\n"
    "buffer_load_dwordx4 a[112:115], v2, s[20:23], s38 offen offset:3072 sc1\n"
    "s_waitcnt lgkmcnt(12)\n"
    "v_mfma_f32_16x16x32_bf16 v[224:227], a[152:155], a[224:227], v[224:227]\n"
    "ds_read_b64_tr_b16 a[184:185], v20 offset:24576\n"
    "v_mfma_f32_16x16x32_bf16 v[228:231], a[152:155], a[228:231], v[228:231]\n"
    "v_mfma_f32_16x16x32_bf16 v[232:235], a[152:155], a[232:235], v[232:235]\n"
    "ds_read_b64_tr_b16 a[186:187], v20 offset:28672\n"
    "v_mfma_f32_16x16x32_bf16 v[236:239], a[152:155], a[236:239], v[236:239]\n"
    "buffer_load_dwordx4 a[116:119], v3, s[20:23], s38 offen offset:3072 sc1\n"
    "v_mfma_f32_16x16x32_bf16 v[252:255], a[156:159], a[236:239], v[252:255]\n"
    "ds_read_b64_tr_b16 a[188:189], v21 offset:24576\n"
    "v_mfma_f32_16x16x32_bf16 v[248:251], a[156:159], a[232:235], v[248:251]\n"
    "v_mfma_f32_16x16x32_bf16 v[244:247], a[156:159], a[228:231], v[244:247]\n"
    "ds_read_b64_tr_b16 a[190:191], v21 offset:28672\n"
    "v_mfma_f32_16x16x32_bf16 v[240:243], a[156:159], a[224:227], v[240:243]\n"
    "s_waitcnt vmcnt(39)\n"
    "s_barrier\n"
    "s_waitcnt lgkmcnt(12)\n"
    "v_mfma_f32_16x16x32_bf16 v[128:131], a[160:163], a[240:243], v[128:131]\n"
    "ds_read_b64_tr_b16 a[128:129], v14 offset:32768\n"
    "v_mfma_f32_16x16x32_bf16 v[132:135], a[160:163], a[244:247], v[132:135]\n"
    "s_add_u32 m0, s28, 49152\n"
    "s_nop 0\n"
    "buffer_load_dwordx4 v22, s[76:79], s25 offen offset:0 lds\n"
    "v_mfma_f32_16x16x32_bf16 v[136:139], a[160:163], a[248:251], v[136:139]\n"
    "ds_read_b64_tr_b16 a[130:131], v14 offset:36864\n"
    "v_mfma_f32_16x16x32_bf16 v[140:143], a[160:163], a[252:255], v[140:143]\n"
    "v_mfma_f32_16x16x32_bf16 v[156:159], a[164:167], a[252:255], v[156:159]\n"
    "ds_read_b64_tr_b16 a[132:133], v15 offset:32768\n"
    "v_mfma_f32_16x16x32_bf16 v[152:155], a[164:167], a[248:251], v[152:155]\n"
    "buffer_load_dwordx4 v23, s[76:79], s25 offen offset:1024 lds\n"
    "v_mfma_f32_16x16x32_bf16 v[148:151], a[164:167], a[244:247], v[148:151]\n"
    "ds_read_b64_tr_b16 a[134:135], v15 offset:36864\n"
    "v_mfma_f32_16x16x32_bf16 v[144:147], a[164:167], a[240:243], v[144:147]\n"
    "s_waitcnt lgkmcnt(12)\n"
    "v_mfma_f32_16x16x32_bf16 v[160:163], a[168:171], a[240:243], v[160:163]\n"
    "ds_read_b64_tr_b16 a[136:137], v16 offset:32768\n"
    "v_mfma_f32_16x16x32_bf16 v[164:167], a[168:171], a[244:247], v[164:167]\n"
    "buffer_load_dwordx4 v24, s[76:79], s25 offen offset:2048 lds\n"
    "v_mfma_f32_16x16x32_bf16 v[168:171], a[168:171], a[248:251], v[168:171]\n"
    "ds_read_b64_tr_b16 a[138:139], v16 offset:36864\n"
    "v_mfma_f32_16x16x32_bf16 v[172:175], a[168:171], a[252:255], v[172:175]\n"
    "v_mfma_f32_16x16x32_bf16 v[188:191], a[172:175], a[252:255], v[188:191]\n"
    "ds_read_b64_tr_b16 a[140:141], v17 offset:32768\n"
    "v_mfma_f32_16x16x32_bf16 v[184:187], a[172:175], a[248:251], v[184:187]\n"
    "buffer_load_dwordx4 v25, s[76:79], s25 offen offset:3072 lds\n"
    "v_mfma_f32_16x16x32_bf16 v[180:183], a[172:175], a[244:247], v[180:183]\n"
    "ds_read_b64_tr_b16 a[142:143], v17 offset:36864\n"
    "v_mfma_f32_16x16x32_bf16 v[176:179], a[172:175], a[240:243], v[176:179]\n"
    "s_waitcnt lgkmcnt(12)\n"
    "v_mfma_f32_16x16x32_bf16 v[192:195], a[176:179], a[240:243], v[192:195]\n"
    "ds_read_b64_tr_b16 a[144:145], v18 offset:32768\n"
    "v_mfma_f32_16x16x32_bf16 v[196:199], a[176:179], a[244:247], v[196:199]\n"
    "s_add_u32 s25, s25, 131072\n"
    "s_add_u32 m0, s28, 65536\n"
    "s_nop 0\n"
    "buffer_load_dwordx4 v22, s[76:79], s25 offen offset:0 lds\n"
    "v_mfma_f32_16x16x32_bf16 v[200:203], a[176:179], a[248:251], v[200:203]\n"
    "ds_read_b64_tr_b16 a[146:147], v18 offset:36864\n"
    "v_mfma_f32_16x16x32_bf16 v[204:207], a[176:179], a[252:255], v[204:207]\n"
    "v_mfma_f32_16x16x32_bf16 v[220:223], a[180:183], a[252:255], v[220:223]\n"
    "ds_read_b64_tr_b16 a[148:149], v19 offset:32768\n"
    "v_mfma_f32_16x16x32_bf16 v[216:219], a[180:183], a[248:251], v[216:219]\n"
    "buffer_load_dwordx4 v23, s[76:79], s25 offen offset:1024 lds\n"
    "v_mfma_f32_16x16x32_bf16 v[212:215], a[180:183], a[244:247], v[212:215]\n"
    "ds_read_b64_tr_b16 a[150:151], v19 offset:36864\n"
    "v_mfma_f32_16x16x32_bf16 v[208:211], a[180:183], a[240:243], v[208:211]\n"
    "s_waitcnt lgkmcnt(12)\n"
    "v_mfma_f32_16x16x32_bf16 v[224:227], a[184:187], a[240:243], v[224:227]\n"
    "ds_read_b64_tr_b16 a[152:153], v20 offset:32768\n"
    "v_mfma_f32_16x16x32_bf16 v[228:231], a[184:187], a[244:247], v[228:231]\n"
    "buffer_load_dwordx4 v24, s[76:79], s25 offen offset:2048 lds\n"
    "v_mfma_f32_16x16x32_bf16 v[232:235], a[184:187], a[248:251], v[232:235]\n"
    "ds_read_b64_tr_b16 a[154:155], v20 offset:36864\n"
    "v_mfma_f32_16x16x32_bf16 v[236:239], a[184:187], a[252:255], v[236:239]\n"
    "v_mfma_f32_16x16x32_bf16 v[252:255], a[188:191], a[252:255], v[252:255]\n"
    "ds_read_b64_tr_b16 a[156:157], v21 offset:32768\n"
    "v_mfma_f32_16x16x32_bf16 v[248:251], a[188:191], a[248:251], v[248:251]\n"
    "v_mfma_f32_16x16x32_bf16 v[244:247], a[188:191], a[244:247], v[244:247]\n"
    "ds_read_b64_tr_b16 a[158:159], v21 offset:36864\n"
    "v_mfma_f32_16x16x32_bf16 v[240:243], a[188:191], a[240:243], v[240:243]\n"
    "s_waitcnt vmcnt(42)\n"
    "s_waitcnt lgkmcnt(12)\n"
    "v_mfma_f32_16x16x32_bf16 v[128:131], a[128:131], a[0:3], v[128:131]\n"
    "ds_read_b64_tr_b16 a[160:161], v14 offset:40960\n"
    "v_mfma_f32_16x16x32_bf16 v[132:135], a[128:131], a[4:7], v[132:135]\n"
    "buffer_load_dwordx4 v25, s[76:79], s25 offen offset:3072 lds\n"
    "v_mfma_f32_16x16x32_bf16 v[136:139], a[128:131], a[8:11], v[136:139]\n"
    "ds_read_b64_tr_b16 a[162:163], v14 offset:45056\n"
    "v_mfma_f32_16x16x32_bf16 v[140:143], a[128:131], a[12:15], v[140:143]\n"
    "v_mfma_f32_16x16x32_bf16 v[156:159], a[132:135], a[12:15], v[156:159]\n"
    "ds_read_b64_tr_b16 a[164:165], v15 offset:40960\n"
    "v_mfma_f32_16x16x32_bf16 v[152:155], a[132:135], a[8:11], v[152:155]\n"
    "s_add_u32 s25, s25, 131072\n"
    "buffer_load_dwordx4 a[120:123], v4, s[20:23], s38 offen offset:3072 sc1\n"
    "v_mfma_f32_16x16x32_bf16 v[148:151], a[132:135], a[4:7], v[148:151]\n"
    "ds_read_b64_tr_b16 a[166:167], v15 offset:45056\n"
    "v_mfma_f32_16x16x32_bf16 v[144:147], a[132:135], a[0:3], v[144:147]\n"
    "s_waitcnt lgkmcnt(12)\n"
    "v_mfma_f32_16x16x32_bf16 v[160:163], a[136:139], a[0:3], v[160:163]\n"
    "ds_read_b64_tr_b16 a[168:169], v16 offset:40960\n"
    "v_mfma_f32_16x16x32_bf16 v[164:167], a[136:139], a[4:7], v[164:167]\n"
    "buffer_load_dwordx4 a[124:127], v5, s[20:23], s38 offen offset:3072 sc1\n"
    "v_mfma_f32_16x16x32_bf16 v[168:171], a[136:139], a[8:11], v[168:171]\n"
    "ds_read_b64_tr_b16 a[170:171], v16 offset:45056\n"
    "v_mfma_f32_16x16x32_bf16 v[172:175], a[136:139], a[12:15], v[172:175]\n"
    "v_mfma_f32_16x16x32_bf16 v[188:191], a[140:143], a[12:15], v[188:191]\n"
    "ds_read_b64_tr_b16 a[172:173], v17 offset:40960\n"
    "v_mfma_f32_16x16x32_bf16 v[184:187], a[140:143], a[8:11], v[184:187]\n"
    "s_add_u32 s38, s38, 4096\n"
    "buffer_load_dwordx4 a[192:195], v2, s[20:23], s38 offen offset:0 sc1\n"
    "v_mfma_f32_16x16x32_bf16 v[180:183], a[140:143], a[4:7], v[180:183]\n"
    "ds_read_b64_tr_b16 a[174:175], v17 offset:45056\n"
    "v_mfma_f32_16x16x32_bf16 v[176:179], a[140:143], a[0:3], v[176:179]\n"
    "s_waitcnt lgkmcnt(12)\n"
    "v_mfma_f32_16x16x32_bf16 v[192:195], a[144:147], a[0:3], v[192:195]\n"
    "ds_read_b64_tr_b16 a[176:177], v18 offset:40960\n"
    "v_mfma_f32_16x16x32_bf16 v[196:199], a[144:147], a[4:7], v[196:199]\n"
    "buffer_load_dwordx4 a[196:199], v3, s[20:23], s38 offen offset:0 sc1\n"
    "v_mfma_f32_16x16x32_bf16 v[200:203], a[144:147], a[8:11], v[200:203]\n"
    "ds_read_b64_tr_b16 a[178:179], v18 offset:45056\n"
    "v_mfma_f32_16x16x32_bf16 v[204:207], a[144:147], a[12:15], v[204:207]\n"
    "v_mfma_f32_16x16x32_bf16 v[220:223], a[148:151], a[12:15], v[220:223]\n"
    "ds_read_b64_tr_b16 a[180:181], v19 offset:40960\n"
    "v_mfma_f32_16x16x32_bf16 v[216:219], a[148:151], a[8:11], v[216:219]\n"
    "buffer_load_dwordx4 a[200:203], v4, s[20:23], s38 offen offset:0 sc1\n"
    "v_mfma_f32_16x16x32_bf16 v[212:215], a[148:151], a[4:7], v[212:215]\n"
    "ds_read_b64_tr_b16 a[182:183], v19 offset:45056\n"
    "v_mfma_f32_16x16x32_bf16 v[208:211], a[148:151], a[0:3], v[208:211]\n"
    "s_waitcnt lgkmcnt(12)\n"
    "v_mfma_f32_16x16x32_bf16 v[224:227], a[152:155], a[0:3], v[224:227]\n"
    "ds_read_b64_tr_b16 a[184:185], v20 offset:40960\n"
    "v_mfma_f32_16x16x32_bf16 v[228:231], a[152:155], a[4:7], v[228:231]\n"
    "buffer_load_dwordx4 a[204:207], v5, s[20:23], s38 offen offset:0 sc1\n"
    "v_mfma_f32_16x16x32_bf16 v[232:235], a[152:155], a[8:11], v[232:235]\n"
    "ds_read_b64_tr_b16 a[186:187], v20 offset:45056\n"
    "v_mfma_f32_16x16x32_bf16 v[236:239], a[152:155], a[12:15], v[236:239]\n"
    "v_mfma_f32_16x16x32_bf16 v[252:255], a[156:159], a[12:15], v[252:255]\n"
    "ds_read_b64_tr_b16 a[188:189], v21 offset:40960\n"
    "v_mfma_f32_16x16x32_bf16 v[248:251], a[156:159], a[8:11], v[248:251]\n"
    "v_mfma_f32_16x16x32_bf16 v[244:247], a[156:159], a[4:7], v[244:247]\n"
    "ds_read_b64_tr_b16 a[190:191], v21 offset:45056\n"
    "v_mfma_f32_16x16x32_bf16 v[240:243], a[156:159], a[0:3], v[240:243]\n"
    "s_waitcnt vmcnt(45)\n"
    "s_waitcnt lgkmcnt(12)\n"
    "v_mfma_f32_16x16x32_bf16 v[128:131], a[160:163], a[16:19], v[128:131]\n"
    "ds_read_b64_tr_b16 a[128:129], v6 offset:0\n"
    "v_mfma_f32_16x16x32_bf16 v[132:135], a[160:163], a[20:23], v[132:135]\n"
    "buffer_load_dwordx4 a[208:211], v2, s[20:23], s38 offen offset:1024 sc1\n"
    "v_mfma_f32_16x16x32_bf16 v[136:139], a[160:163], a[24:27], v[136:139]\n"
    "ds_read_b64_tr_b16 a[130:131], v6 offset:4096\n"
    "v_mfma_f32_16x16x32_bf16 v[140:143], a[160:163], a[28:31], v[140:143]\n"
    "v_mfma_f32_16x16x32_bf16 v[156:159], a[164:167], a[28:31], v[156:159]\n"
    "ds_read_b64_tr_b16 a[132:133], v7 offset:0\n"
    "v_mfma_f32_16x16x32_bf16 v[152:155], a[164:167], a[24:27], v[152:155]\n"
    "buffer_load_dwordx4 a[212:215], v3, s[20:23], s38 offen offset:1024 sc1\n"
    "v_mfma_f32_16x16x32_bf16 v[148:151], a[164:167], a[20:23], v[148:151]\n"
    "ds_read_b64_tr_b16 a[134:135], v7 offset:4096\n"
    "v_mfma_f32_16x16x32_bf16 v[144:147], a[164:167], a[16:19], v[144:147]\n"
    "s_waitcnt lgkmcnt(12)\n"
    "v_mfma_f32_16x16x32_bf16 v[160:163], a[168:171], a[16:19], v[160:163]\n"
    "ds_read_b64_tr_b16 a[136:137], v8 offset:0\n"
    "v_mfma_f32_16x16x32_bf16 v[164:167], a[168:171], a[20:23], v[164:167]\n"
    "buffer_load_dwordx4 a[216:219], v4, s[20:23], s38 offen offset:1024 sc1\n"
    "v_mfma_f32_16x16x32_bf16 v[168:171], a[168:171], a[24:27], v[168:171]\n"
    "ds_read_b64_tr_b16 a[138:139], v8 offset:4096\n"
    "v_mfma_f32_16x16x32_bf16 v[172:175], a[168:171], a[28:31], v[172:175]\n"
    "v_mfma_f32_16x16x32_bf16 v[188:191], a[172:175], a[28:31], v[188:191]\n"
    "ds_read_b64_tr_b16 a[140:141], v9 offset:0\n"
    "v_mfma_f32_16x16x32_bf16 v[184:187], a[172:175], a[24:27], v[184:187]\n"
    "buffer_load_dwordx4 a[220:223], v5, s[20:23], s38 offen offset:1024 sc1\n"
    "v_mfma_f32_16x16x32_bf16 v[180:183], a[172:175], a[20:23], v[180:183]\n"
    "ds_read_b64_tr_b16 a[142:143], v9 offset:4096\n"
    "v_mfma_f32_16x16x32_bf16 v[176:179], a[172:175], a[16:19], v[176:179]\n"
    "s_waitcnt lgkmcnt(12)\n"
    "v_mfma_f32_16x16x32_bf16 v[192:195], a[176:179], a[16:19], v[192:195]\n"
    "ds_read_b64_tr_b16 a[144:145], v10 offset:0\n"
    "v_mfma_f32_16x16x32_bf16 v[196:199], a[176:179], a[20:23], v[196:199]\n"
    "buffer_load_dwordx4 a[224:227], v2, s[20:23], s38 offen offset:2048 sc1\n"
    "v_mfma_f32_16x16x32_bf16 v[200:203], a[176:179], a[24:27], v[200:203]\n"
    "ds_read_b64_tr_b16 a[146:147], v10 offset:4096\n"
    "v_mfma_f32_16x16x32_bf16 v[204:207], a[176:179], a[28:31], v[204:207]\n"
    "v_mfma_f32_16x16x32_bf16 v[220:223], a[180:183], a[28:31], v[220:223]\n"
    "ds_read_b64_tr_b16 a[148:149], v11 offset:0\n"
    "v_mfma_f32_16x16x32_bf16 v[216:219], a[180:183], a[24:27], v[216:219]\n"
    "buffer_load_dwordx4 a[228:231], v3, s[20:23], s38 offen offset:2048 sc1\n"
    "v_mfma_f32_16x16x32_bf16 v[212:215], a[180:183], a[20:23], v[212:215]\n"
    "ds_read_b64_tr_b16 a[150:151], v11 offset:4096\n"
    "v_mfma_f32_16x16x32_bf16 v[208:211], a[180:183], a[16:19], v[208:211]\n"
    "s_waitcnt lgkmcnt(12)\n"
    "v_mfma_f32_16x16x32_bf16 v[224:227], a[184:187], a[16:19], v[224:227]\n"
    "ds_read_b64_tr_b16 a[152:153], v12 offset:0\n"
    "v_mfma_f32_16x16x32_bf16 v[228:231], a[184:187], a[20:23], v[228:231]\n"
    "buffer_load_dwordx4 a[232:235], v4, s[20:23], s38 offen offset:2048 sc1\n"
    "v_mfma_f32_16x16x32_bf16 v[232:235], a[184:187], a[24:27], v[232:235]\n"
    "ds_read_b64_tr_b16 a[154:155], v12 offset:4096\n"
    "v_mfma_f32_16x16x32_bf16 v[236:239], a[184:187], a[28:31], v[236:239]\n"
    "v_mfma_f32_16x16x32_bf16 v[252:255], a[188:191], a[28:31], v[252:255]\n"
    "ds_read_b64_tr_b16 a[156:157], v13 offset:0\n"
    "v_mfma_f32_16x16x32_bf16 v[248:251], a[188:191], a[24:27], v[248:251]\n"
    "v_mfma_f32_16x16x32_bf16 v[244:247], a[188:191], a[20:23], v[244:247]\n"
    "ds_read_b64_tr_b16 a[158:159], v13 offset:4096\n"
    "v_mfma_f32_16x16x32_bf16 v[240:243], a[188:191], a[16:19], v[240:243]\n"
    "s_waitcnt vmcnt(39)\n"
    "s_waitcnt lgkmcnt(12)\n"
    "v_mfma_f32_16x16x32_bf16 v[128:131], a[128:131], a[32:35], v[128:131]\n"
    "ds_read_b64_tr_b16 a[160:161], v6 offset:8192\n"
    "v_mfma_f32_16x16x32_bf16 v[132:135], a[128:131], a[36:39], v[132:135]\n"
    "buffer_load_dwordx4 a[236:239], v5, s[20:23], s38 offen offset:2048 sc1\n"
    "v_mfma_f32_16x16x32_bf16 v[136:139], a[128:131], a[40:43], v[136:139]\n"
    "ds_read_b64_tr_b16 a[162:163], v6 offset:12288\n"
    "v_mfma_f32_16x16x32_bf16 v[140:143], a[128:131], a[44:47], v[140:143]\n"
    "v_mfma_f32_16x16x32_bf16 v[156:159], a[132:135], a[44:47], v[156:159]\n"
    "ds_read_b64_tr_b16 a[164:165], v7 offset:8192\n"
    "v_mfma_f32_16x16x32_bf16 v[152:155], a[132:135], a[40:43], v[152:155]\n"
    "buffer_load_dwordx4 a[240:243], v2, s[20:23], s38 offen offset:3072 sc1\n"
    "v_mfma_f32_16x16x32_bf16 v[148:151], a[132:135], a[36:39], v[148:151]\n"
    "ds_read_b64_tr_b16 a[166:167], v7 offset:12288\n"
    "v_mfma_f32_16x16x32_bf16 v[144:147], a[132:135], a[32:35], v[144:147]\n"
    "s_waitcnt lgkmcnt(12)\n"
    "v_mfma_f32_16x16x32_bf16 v[160:163], a[136:139], a[32:35], v[160:163]\n"
    "ds_read_b64_tr_b16 a[168:169], v8 offset:8192\n"
    "v_mfma_f32_16x16x32_bf16 v[164:167], a[136:139], a[36:39], v[164:167]\n"
    "buffer_load_dwordx4 a[244:247], v3, s[20:23], s38 offen offset:3072 sc1\n"
    "v_mfma_f32_16x16x32_bf16 v[168:171], a[136:139], a[40:43], v[168:171]\n"
    "ds_read_b64_tr_b16 a[170:171], v8 offset:12288\n"
    "v_mfma_f32_16x16x32_bf16 v[172:175], a[136:139], a[44:47], v[172:175]\n"
    "v_mfma_f32_16x16x32_bf16 v[188:191], a[140:143], a[44:47], v[188:191]\n"
    "ds_read_b64_tr_b16 a[172:173], v9 offset:8192\n"
    "v_mfma_f32_16x16x32_bf16 v[184:187], a[140:143], a[40:43], v[184:187]\n"
    "buffer_load_dwordx4 a[248:251], v4, s[20:23], s38 offen offset:3072 sc1\n"
    "v_mfma_f32_16x16x32_bf16 v[180:183], a[140:143], a[36:39], v[180:183]\n"
    "ds_read_b64_tr_b16 a[174:175], v9 offset:12288\n"
    "v_mfma_f32_16x16x32_bf16 v[176:179], a[140:143], a[32:35], v[176:179]\n"
    "s_waitcnt lgkmcnt(12)\n"
    "v_mfma_f32_16x16x32_bf16 v[192:195], a[144:147], a[32:35], v[192:195]\n"
    "ds_read_b64_tr_b16 a[176:177], v10 offset:8192\n"
    "v_mfma_f32_16x16x32_bf16 v[196:199], a[144:147], a[36:39], v[196:199]\n"
    "buffer_load_dwordx4 a[252:255], v5, s[20:23], s38 offen offset:3072 sc1\n"
    "v_mfma_f32_16x16x32_bf16 v[200:203], a[144:147], a[40:43], v[200:203]\n"
    "ds_read_b64_tr_b16 a[178:179], v10 offset:12288\n"
    "v_mfma_f32_16x16x32_bf16 v[204:207], a[144:147], a[44:47], v[204:207]\n"
    "v_mfma_f32_16x16x32_bf16 v[220:223], a[148:151], a[44:47], v[220:223]\n"
    "ds_read_b64_tr_b16 a[180:181], v11 offset:8192\n"
    "v_mfma_f32_16x16x32_bf16 v[216:219], a[148:151], a[40:43], v[216:219]\n"
    "s_add_u32 s38, s38, 4096\n"
    "buffer_load_dwordx4 a[0:3], v2, s[20:23], s38 offen offset:0 sc1\n"
    "v_mfma_f32_16x16x32_bf16 v[212:215], a[148:151], a[36:39], v[212:215]\n"
    "ds_read_b64_tr_b16 a[182:183], v11 offset:12288\n"
    "v_mfma_f32_16x16x32_bf16 v[208:211], a[148:151], a[32:35], v[208:211]\n"
    "s_waitcnt lgkmcnt(12)\n"
    "v_mfma_f32_16x16x32_bf16 v[224:227], a[152:155], a[32:35], v[224:227]\n"
    "ds_read_b64_tr_b16 a[184:185], v12 offset:8192\n"
    "v_mfma_f32_16x16x32_bf16 v[228:231], a[152:155], a[36:39], v[228:231]\n"
    "buffer_load_dwordx4 a[4:7], v3, s[20:23], s38 offen offset:0 sc1\n"
    "v_mfma_f32_16x16x32_bf16 v[232:235], a[152:155], a[40:43], v[232:235]\n"
    "ds_read_b64_tr_b16 a[186:187], v12 offset:12288\n"
    "v_mfma_f32_16x16x32_bf16 v[236:239], a[152:155], a[44:47], v[236:239]\n"
    "v_mfma_f32_16x16x32_bf16 v[252:255], a[156:159], a[44:47], v[252:255]\n"
    "ds_read_b64_tr_b16 a[188:189], v13 offset:8192\n"
    "v_mfma_f32_16x16x32_bf16 v[248:251], a[156:159], a[40:43], v[248:251]\n"
    "v_mfma_f32_16x16x32_bf16 v[244:247], a[156:159], a[36:39], v[244:247]\n"
    "ds_read_b64_tr_b16 a[190:191], v13 offset:12288\n"
    "v_mfma_f32_16x16x32_bf16 v[240:243], a[156:159], a[32:35], v[240:243]\n"
    "s_waitcnt vmcnt(24)\n"
    "s_barrier\n"
    "s_waitcnt lgkmcnt(12)\n"
    "v_mfma_f32_16x16x32_bf16 v[128:131], a[160:163], a[48:51], v[128:131]\n"
    "ds_read_b64_tr_b16 a[128:129], v6 offset:16384\n"
    "v_mfma_f32_16x16x32_bf16 v[132:135], a[160:163], a[52:55], v[132:135]\n"
    "s_add_u32 m0, s28, 81920\n"
    "s_nop 0\n"
    "buffer_load_dwordx4 v22, s[76:79], s25 offen offset:0 lds\n"
    "v_mfma_f32_16x16x32_bf16 v[136:139], a[160:163], a[56:59], v[136:139]\n"
    "ds_read_b64_tr_b16 a[130:131], v6 offset:20480\n"
    "v_mfma_f32_16x16x32_bf16 v[140:143], a[160:163], a[60:63], v[140:143]\n"
    "buffer_load_dwordx4 v23, s[76:79], s25 offen offset:1024 lds\n"
    "v_mfma_f32_16x16x32_bf16 v[156:159], a[164:167], a[60:63], v[156:159]\n"
    "ds_read_b64_tr_b16 a[132:133], v7 offset:16384\n"
    "v_mfma_f32_16x16x32_bf16 v[152:155], a[164:167], a[56:59], v[152:155]\n"
    "v_mfma_f32_16x16x32_bf16 v[148:151], a[164:167], a[52:55], v[148:151]\n"
    "ds_read_b64_tr_b16 a[134:135], v7 offset:20480\n"
    "v_mfma_f32_16x16x32_bf16 v[144:147], a[164:167], a[48:51], v[144:147]\n"
    "buffer_load_dwordx4 v24, s[76:79], s25 offen offset:2048 lds\n"
    "s_waitcnt lgkmcnt(12)\n"
    "v_mfma_f32_16x16x32_bf16 v[160:163], a[168:171], a[48:51], v[160:163]\n"
    "ds_read_b64_tr_b16 a[136:137], v8 offset:16384\n"
    "v_mfma_f32_16x16x32_bf16 v[164:167], a[168:171], a[52:55], v[164:167]\n"
    "v_mfma_f32_16x16x32_bf16 v[168:171], a[168:171], a[56:59], v[168:171]\n"
    "ds_read_b64_tr_b16 a[138:139], v8 offset:20480\n"
    "v_mfma_f32_16x16x32_bf16 v[172:175], a[168:171], a[60:63], v[172:175]\n"
    "buffer_load_dwordx4 v25, s[76:79], s25 offen offset:3072 lds\n"
    "v_mfma_f32_16x16x32_bf16 v[188:191], a[172:175], a[60:63], v[188:191]\n"
    "ds_read_b64_tr_b16 a[140:141], v9 offset:16384\n"
    "v_mfma_f32_16x16x32_bf16 v[184:187], a[172:175], a[56:59], v[184:187]\n"
    "v_mfma_f32_16x16x32_bf16 v[180:183], a[172:175], a[52:55], v[180:183]\n"
    "ds_read_b64_tr_b16 a[142:143], v9 offset:20480\n"
    "v_mfma_f32_16x16x32_bf16 v[176:179], a[172:175], a[48:51], v[176:179]\n"
    "s_add_u32 s25, s25, 131072\n"
    "s_add_u32 m0, s28, 98304\n"
    "s_nop 0\n"
    "buffer_load_dwordx4 v22, s[76:79], s25 offen offset:0 lds\n"
    "s_waitcnt lgkmcnt(12)\n"
    "v_mfma_f32_16x16x32_bf16 v[192:195], a[176:179], a[48:51], v[192:195]\n"
    "ds_read_b64_tr_b16 a[144:145], v10 offset:16384\n"
    "v_mfma_f32_16x16x32_bf16 v[196:199], a[176:179], a[52:55], v[196:199]\n"
    "v_mfma_f32_16x16x32_bf16 v[200:203], a[176:179], a[56:59], v[200:203]\n"
    "ds_read_b64_tr_b16 a[146:147], v10 offset:20480\n"
    "v_mfma_f32_16x16x32_bf16 v[204:207], a[176:179], a[60:63], v[204:207]\n"
    "buffer_load_dwordx4 v23, s[76:79], s25 offen offset:1024 lds\n"
    "v_mfma_f32_16x16x32_bf16 v[220:223], a[180:183], a[60:63], v[220:223]\n"
    "ds_read_b64_tr_b16 a[148:149], v11 offset:16384\n"
    "v_mfma_f32_16x16x32_bf16 v[216:219], a[180:183], a[56:59], v[216:219]\n"
    "v_mfma_f32_16x16x32_bf16 v[212:215], a[180:183], a[52:55], v[212:215]\n"
    "ds_read_b64_tr_b16 a[150:151], v11 offset:20480\n"
    "v_mfma_f32_16x16x32_bf16 v[208:211], a[180:183], a[48:51], v[208:211]\n"
    "buffer_load_dwordx4 v24, s[76:79], s25 offen offset:2048 lds\n"
    "s_waitcnt lgkmcnt(12)\n"
    "v_mfma_f32_16x16x32_bf16 v[224:227], a[184:187], a[48:51], v[224:227]\n"
    "ds_read_b64_tr_b16 a[152:153], v12 offset:16384\n"
    "v_mfma_f32_16x16x32_bf16 v[228:231], a[184:187], a[52:55], v[228:231]\n"
    "v_mfma_f32_16x16x32_bf16 v[232:235], a[184:187], a[56:59], v[232:235]\n"
    "ds_read_b64_tr_b16 a[154:155], v12 offset:20480\n"
    "v_mfma_f32_16x16x32_bf16 v[236:239], a[184:187], a[60:63], v[236:239]\n"
    "buffer_load_dwordx4 v25, s[76:79], s25 offen offset:3072 lds\n"
    "v_mfma_f32_16x16x32_bf16 v[252:255], a[188:191], a[60:63], v[252:255]\n"
    "ds_read_b64_tr_b16 a[156:157], v13 offset:16384\n"
    "v_mfma_f32_16x16x32_bf16 v[248:251], a[188:191], a[56:59], v[248:251]\n"
    "v_mfma_f32_16x16x32_bf16 v[244:247], a[188:191], a[52:55], v[244:247]\n"
    "ds_read_b64_tr_b16 a[158:159], v13 offset:20480\n"
    "v_mfma_f32_16x16x32_bf16 v[240:243], a[188:191], a[48:51], v[240:243]\n"
    "s_add_u32 s25, s25, 131072\n"
    "s_waitcnt lgkmcnt(12)\n"
    "v_mfma_f32_16x16x32_bf16 v[128:131], a[128:131], a[64:67], v[128:131]\n"
    "ds_read_b64_tr_b16 a[160:161], v6 offset:24576\n"
    "v_mfma_f32_16x16x32_bf16 v[132:135], a[128:131], a[68:71], v[132:135]\n"
    "buffer_load_dwordx4 a[8:11], v4, s[20:23], s38 offen offset:0 sc1\n"
    "v_mfma_f32_16x16x32_bf16 v[136:139], a[128:131], a[72:75], v[136:139]\n"
    "ds_read_b64_tr_b16 a[162:163], v6 offset:28672\n"
    "v_mfma_f32_16x16x32_bf16 v[140:143], a[128:131], a[76:79], v[140:143]\n"
    "buffer_load_dwordx4 v[40:43], v32, s[20:23], 0 offen offset:0 sc1\n"
    "v_mfma_f32_16x16x32_bf16 v[156:159], a[132:135], a[76:79], v[156:159]\n"
    "ds_read_b64_tr_b16 a[164:165], v7 offset:24576\n"
    "v_mfma_f32_16x16x32_bf16 v[152:155], a[132:135], a[72:75], v[152:155]\n"
    "buffer_load_dwordx4 a[12:15], v5, s[20:23], s38 offen offset:0 sc1\n"
    "v_mfma_f32_16x16x32_bf16 v[148:151], a[132:135], a[68:71], v[148:151]\n"
    "ds_read_b64_tr_b16 a[166:167], v7 offset:28672\n"
    "v_mfma_f32_16x16x32_bf16 v[144:147], a[132:135], a[64:67], v[144:147]\n"
    "s_waitcnt lgkmcnt(12)\n"
    "v_mfma_f32_16x16x32_bf16 v[160:163], a[136:139], a[64:67], v[160:163]\n"
    "ds_read_b64_tr_b16 a[168:169], v8 offset:24576\n"
    "v_mfma_f32_16x16x32_bf16 v[164:167], a[136:139], a[68:71], v[164:167]\n"
    "v_mfma_f32_16x16x32_bf16 v[168:171], a[136:139], a[72:75], v[168:171]\n"
    "ds_read_b64_tr_b16 a[170:171], v8 offset:28672\n"
    "v_mfma_f32_16x16x32_bf16 v[172:175], a[136:139], a[76:79], v[172:175]\n"
    "buffer_load_dwordx4 a[16:19], v2, s[20:23], s38 offen offset:1024 sc1\n"
    "v_mfma_f32_16x16x32_bf16 v[188:191], a[140:143], a[76:79], v[188:191]\n"
    "ds_read_b64_tr_b16 a[172:173], v9 offset:24576\n"
    "v_mfma_f32_16x16x32_bf16 v[184:187], a[140:143], a[72:75], v[184:187]\n"
    "v_mfma_f32_16x16x32_bf16 v[180:183], a[140:143], a[68:71], v[180:183]\n"
    "ds_read_b64_tr_b16 a[174:175], v9 offset:28672\n"
    "v_mfma_f32_16x16x32_bf16 v[176:179], a[140:143], a[64:67], v[176:179]\n"
    "buffer_load_dwordx4 a[20:23], v3, s[20:23], s38 offen offset:1024 sc1\n"
    "s_waitcnt lgkmcnt(12)\n"
    "v_mfma_f32_16x16x32_bf16 v[192:195], a[144:147], a[64:67], v[192:195]\n"
    "ds_read_b64_tr_b16 a[176:177], v10 offset:24576\n"
    "v_mfma_f32_16x16x32_bf16 v[196:199], a[144:147], a[68:71], v[196:199]\n"
    "buffer_load_dwordx4 v[44:47], v32, s[20:23], 0 offen offset:16 sc1\n"
    "v_mfma_f32_16x16x32_bf16 v[200:203], a[144:147], a[72:75], v[200:203]\n"
    "ds_read_b64_tr_b16 a[178:179], v10 offset:28672\n"
    "v_mfma_f32_16x16x32_bf16 v[204:207], a[144:147], a[76:79], v[204:207]\n"
    "v_mfma_f32_16x16x32_bf16 v[220:223], a[148:151], a[76:79], v[220:223]\n"
    "ds_read_b64_tr_b16 a[180:181], v11 offset:24576\n"
    "v_mfma_f32_16x16x32_bf16 v[216:219], a[148:151], a[72:75], v[216:219]\n"
    "buffer_load_dwordx4 a[24:27], v4, s[20:23], s38 offen offset:1024 sc1\n"
    "v_mfma_f32_16x16x32_bf16 v[212:215], a[148:151], a[68:71], v[212:215]\n"
    "ds_read_b64_tr_b16 a[182:183], v11 offset:28672\n"
    "v_mfma_f32_16x16x32_bf16 v[208:211], a[148:151], a[64:67], v[208:211]\n"
    "s_waitcnt lgkmcnt(12)\n"
    "v_mfma_f32_16x16x32_bf16 v[224:227], a[152:155], a[64:67], v[224:227]\n"
    "ds_read_b64_tr_b16 a[184:185], v12 offset:24576\n"
    "v_mfma_f32_16x16x32_bf16 v[228:231], a[152:155], a[68:71], v[228:231]\n"
    "buffer_load_dwordx4 a[28:31], v5, s[20:23], s38 offen offset:1024 sc1\n"
    "v_mfma_f32_16x16x32_bf16 v[232:235], a[152:155], a[72:75], v[232:235]\n"
    "ds_read_b64_tr_b16 a[186:187], v12 offset:28672\n"
    "v_mfma_f32_16x16x32_bf16 v[236:239], a[152:155], a[76:79], v[236:239]\n"
    "v_mfma_f32_16x16x32_bf16 v[252:255], a[156:159], a[76:79], v[252:255]\n"
    "ds_read_b64_tr_b16 a[188:189], v13 offset:24576\n"
    "v_mfma_f32_16x16x32_bf16 v[248:251], a[156:159], a[72:75], v[248:251]\n"
    "v_mfma_f32_16x16x32_bf16 v[244:247], a[156:159], a[68:71], v[244:247]\n"
    "ds_read_b64_tr_b16 a[190:191], v13 offset:28672\n"
    "v_mfma_f32_16x16x32_bf16 v[240:243], a[156:159], a[64:67], v[240:243]\n"
    "s_waitcnt lgkmcnt(12)\n"
    "v_mfma_f32_16x16x32_bf16 v[128:131], a[160:163], a[80:83], v[128:131]\n"
    "ds_read_b64_tr_b16 a[128:129], v8 offset:32768\n"
    "v_mfma_f32_16x16x32_bf16 v[132:135], a[160:163], a[84:87], v[132:135]\n"
    "buffer_load_dwordx4 a[32:35], v2, s[20:23], s38 offen offset:2048 sc1\n"
    "v_mfma_f32_16x16x32_bf16 v[136:139], a[160:163], a[88:91], v[136:139]\n"
    "ds_read_b64_tr_b16 a[130:131], v8 offset:36864\n"
    "v_mfma_f32_16x16x32_bf16 v[140:143], a[160:163], a[92:95], v[140:143]\n"
    "v_mfma_f32_16x16x32_bf16 v[156:159], a[164:167], a[92:95], v[156:159]\n"
    "ds_read_b64_tr_b16 a[132:133], v9 offset:32768\n"
    "v_mfma_f32_16x16x32_bf16 v[152:155], a[164:167], a[88:91], v[152:155]\n"
    "buffer_load_dwordx4 a[36:39], v3, s[20:23], s38 offen offset:2048 sc1\n"
    "v_mfma_f32_16x16x32_bf16 v[148:151], a[164:167], a[84:87], v[148:151]\n"
    "ds_read_b64_tr_b16 a[134:135], v9 offset:36864\n"
    "v_mfma_f32_16x16x32_bf16 v[144:147], a[164:167], a[80:83], v[144:147]\n"
    "s_waitcnt lgkmcnt(12)\n"
    "v_mfma_f32_16x16x32_bf16 v[160:163], a[168:171], a[80:83], v[160:163]\n"
    "ds_read_b64_tr_b16 a[136:137], v8 offset:40960\n"
    "v_mfma_f32_16x16x32_bf16 v[164:167], a[168:171], a[84:87], v[164:167]\n"
    "buffer_load_dwordx4 a[40:43], v4, s[20:23], s38 offen offset:2048 sc1\n"
    "v_mfma_f32_16x16x32_bf16 v[168:171], a[168:171], a[88:91], v[168:171]\n"
    "ds_read_b64_tr_b16 a[138:139], v8 offset:45056\n"
    "v_mfma_f32_16x16x32_bf16 v[172:175], a[168:171], a[92:95], v[172:175]\n"
    "v_mfma_f32_16x16x32_bf16 v[188:191], a[172:175], a[92:95], v[188:191]\n"
    "ds_read_b64_tr_b16 a[140:141], v9 offset:40960\n"
    "v_mfma_f32_16x16x32_bf16 v[184:187], a[172:175], a[88:91], v[184:187]\n"
    "buffer_load_dwordx4 a[44:47], v5, s[20:23], s38 offen offset:2048 sc1\n"
    "v_mfma_f32_16x16x32_bf16 v[180:183], a[172:175], a[84:87], v[180:183]\n"
    "ds_read_b64_tr_b16 a[142:143], v9 offset:45056\n"
    "v_mfma_f32_16x16x32_bf16 v[176:179], a[172:175], a[80:83], v[176:179]\n"
    "s_waitcnt lgkmcnt(12)\n"
    "v_mfma_f32_16x16x32_bf16 v[192:195], a[176:179], a[80:83], v[192:195]\n"
    "ds_read_b64_tr_b16 a[144:145], v8 offset:49152\n"
    "v_mfma_f32_16x16x32_bf16 v[196:199], a[176:179], a[84:87], v[196:199]\n"
    "buffer_load_dwordx4 a[48:51], v2, s[20:23], s38 offen offset:3072 sc1\n"
    "v_mfma_f32_16x16x32_bf16 v[200:203], a[176:179], a[88:91], v[200:203]\n"
    "ds_read_b64_tr_b16 a[146:147], v8 offset:53248\n"
    "v_mfma_f32_16x16x32_bf16 v[204:207], a[176:179], a[92:95], v[204:207]\n"
    "v_mfma_f32_16x16x32_bf16 v[220:223], a[180:183], a[92:95], v[220:223]\n"
    "ds_read_b64_tr_b16 a[148:149], v9 offset:49152\n"
    "v_mfma_f32_16x16x32_bf16 v[216:219], a[180:183], a[88:91], v[216:219]\n"
    "buffer_load_dwordx4 a[52:55], v3, s[20:23], s38 offen offset:3072 sc1\n"
    "v_mfma_f32_16x16x32_bf16 v[212:215], a[180:183], a[84:87], v[212:215]\n"
    "ds_read_b64_tr_b16 a[150:151], v9 offset:53248\n"
    "v_mfma_f32_16x16x32_bf16 v[208:211], a[180:183], a[80:83], v[208:211]\n"
    "s_waitcnt lgkmcnt(12)\n"
    "v_mfma_f32_16x16x32_bf16 v[224:227], a[184:187], a[80:83], v[224:227]\n"
    "ds_read_b64_tr_b16 a[152:153], v8 offset:57344\n"
    "v_mfma_f32_16x16x32_bf16 v[228:231], a[184:187], a[84:87], v[228:231]\n"
    "buffer_load_dwordx4 a[56:59], v4, s[20:23], s38 offen offset:3072 sc1\n"
    "v_mfma_f32_16x16x32_bf16 v[232:235], a[184:187], a[88:91], v[232:235]\n"
    "ds_read_b64_tr_b16 a[154:155], v8 offset:61440\n"
    "v_mfma_f32_16x16x32_bf16 v[236:239], a[184:187], a[92:95], v[236:239]\n"
    "v_mfma_f32_16x16x32_bf16 v[252:255], a[188:191], a[92:95], v[252:255]\n"
    "ds_read_b64_tr_b16 a[156:157], v9 offset:57344\n"
    "v_mfma_f32_16x16x32_bf16 v[248:251], a[188:191], a[88:91], v[248:251]\n"
    "v_mfma_f32_16x16x32_bf16 v[244:247], a[188:191], a[84:87], v[244:247]\n"
    "ds_read_b64_tr_b16 a[158:159], v9 offset:61440\n"
    "v_mfma_f32_16x16x32_bf16 v[240:243], a[188:191], a[80:83], v[240:243]\n"
    "s_waitcnt vmcnt(9)\n"
    "s_barrier\n"
    "s_mov_b32 s31, 0x4000\n"
    "ZCH_gated_mix__ssm_conv_h3__swiglu_ffn__prompt_kernel:\n"
    "v_cmp_ne_u64 vcc, s[70:71], v[40:41]\n"
    "v_cmp_ne_u64 s[66:67], s[70:71], v[42:43]\n"
    "v_cmp_ne_u64 s[68:69], s[70:71], v[44:45]\n"
    "s_or_b64 s[66:67], s[66:67], vcc\n"
    "v_cmp_ne_u64 vcc, s[70:71], v[46:47]\n"
    "s_or_b64 s[66:67], s[66:67], s[68:69]\n"
    "s_or_b64 vcc, s[66:67], vcc\n"
    "s_cbranch_vccz ZOK_gated_mix__ssm_conv_h3__swiglu_ffn__prompt_kernel\n"
    "buffer_load_dwordx4 v[40:43], v32, s[20:23], 0 offen offset:0 sc1\n"
    "buffer_load_dwordx4 v[44:47], v32, s[20:23], 0 offen offset:16 sc1\n"
    "s_waitcnt vmcnt(0)\n"
    "s_sub_u32 s31, s31, 1\n"
    "s_cmp_lg_u32 s31, 0\n"
    "s_cbranch_scc1 ZCH_gated_mix__ssm_conv_h3__swiglu_ffn__prompt_kernel\n"
    "ZOK_gated_mix__ssm_conv_h3__swiglu_ffn__prompt_kernel:\n"
    "s_or_b32 s30, s39, s34\n"
    "s_cmp_lg_u32 s30, 0\n"
    "s_cbranch_scc1 NOBUMP_gated_mix__ssm_conv_h3__swiglu_ffn__prompt_kernel\n"
    "s_mov_b32 s30, 40960000\n"
    "v_mov_b32 v48, s70\n"
    "v_mov_b32 v49, s71\n"
    "v_mov_b32 v50, 0\n"
    "s_mov_b64 exec, 1\n"
    "buffer_store_dwordx2 v[48:49], v50, s[20:23], s30 offen sc1\n"
    "s_mov_b64 exec, -1\n"
    "NOBUMP_gated_mix__ssm_conv_h3__swiglu_ffn__prompt_kernel:\n"
    "s_waitcnt lgkmcnt(12)\n"
    "v_mfma_f32_16x16x32_bf16 v[160:163], a[128:131], a[96:99], v[160:163]\n"
    "ds_read_b64_tr_b16 a[160:161], v16 offset:0\n"
    "s_mul_i32 s66, s34, 5120\n"
    "v_mfma_f32_16x16x32_bf16 v[164:167], a[128:131], a[100:103], v[164:167]\n"
    "buffer_load_dwordx4 a[60:63], v5, s[20:23], s38 offen offset:3072 sc1\n"
    "v_mfma_f32_16x16x32_bf16 v[168:171], a[128:131], a[104:107], v[168:171]\n"
    "ds_read_b64_tr_b16 a[162:163], v16 offset:4096\n"
    "v_mfma_f32_16x16x32_bf16 v[172:175], a[128:131], a[108:111], v[172:175]\n"
    "v_mfma_f32_16x16x32_bf16 v[176:179], a[132:135], a[96:99], v[176:179]\n"
    "ds_read_b64_tr_b16 a[164:165], v17 offset:0\n"
    "v_mfma_f32_16x16x32_bf16 v[180:183], a[132:135], a[100:103], v[180:183]\n"
    "s_add_u32 s38, s38, 4096\n"
    "s_add_u32 s66, s66, 114688\n"
    "v_mfma_f32_16x16x32_bf16 v[184:187], a[132:135], a[104:107], v[184:187]\n"
    "ds_read_b64_tr_b16 a[166:167], v17 offset:4096\n"
    "v_mfma_f32_16x16x32_bf16 v[188:191], a[132:135], a[108:111], v[188:191]\n"
    "s_waitcnt lgkmcnt(12)\n"
    "v_mfma_f32_16x16x32_bf16 v[160:163], a[136:139], a[112:115], v[160:163]\n"
    "ds_read_b64_tr_b16 a[168:169], v16 offset:8192\n"
    "v_mfma_f32_16x16x32_bf16 v[164:167], a[136:139], a[116:119], v[164:167]\n"
    "buffer_load_dwordx4 v[96:99], v30, s[20:23], s75 offen sc1\n"
    "v_mfma_f32_16x16x32_bf16 v[168:171], a[136:139], a[120:123], v[168:171]\n"
    "ds_read_b64_tr_b16 a[170:171], v16 offset:12288\n"
    "v_mfma_f32_16x16x32_bf16 v[172:175], a[136:139], a[124:127], v[172:175]\n"
    "v_lshrrev_b32 v38, 3, v1\n"
    "v_mfma_f32_16x16x32_bf16 v[176:179], a[140:143], a[112:115], v[176:179]\n"
    "ds_read_b64_tr_b16 a[172:173], v17 offset:8192\n"
    "v_mfma_f32_16x16x32_bf16 v[180:183], a[140:143], a[116:119], v[180:183]\n"
    "v_mfma_f32_16x16x32_bf16 v[184:187], a[140:143], a[120:123], v[184:187]\n"
    "ds_read_b64_tr_b16 a[174:175], v17 offset:12288\n"
    "s_add_u32 s75, s75, 33792\n"
    "v_mfma_f32_16x16x32_bf16 v[188:191], a[140:143], a[124:127], v[188:191]\n"
    "s_waitcnt lgkmcnt(12)\n"
    "v_mfma_f32_16x16x32_bf16 v[160:163], a[144:147], a[192:195], v[160:163]\n"
    "ds_read_b64_tr_b16 a[176:177], v16 offset:16384\n"
    "v_mul_u32_u24 v38, 160, v38\n"
    "v_mfma_f32_16x16x32_bf16 v[164:167], a[144:147], a[196:199], v[164:167]\n"
    "v_mfma_f32_16x16x32_bf16 v[168:171], a[144:147], a[200:203], v[168:171]\n"
    "ds_read_b64_tr_b16 a[178:179], v16 offset:20480\n"
    "buffer_load_dwordx4 v[100:103], v30, s[20:23], s75 offen sc1\n"
    "v_mfma_f32_16x16x32_bf16 v[172:175], a[144:147], a[204:207], v[172:175]\n"
    "v_mfma_f32_16x16x32_bf16 v[176:179], a[148:151], a[192:195], v[176:179]\n"
    "ds_read_b64_tr_b16 a[180:181], v17 offset:16384\n"
    "v_mfma_f32_16x16x32_bf16 v[180:183], a[148:151], a[196:199], v[180:183]\n"
    "v_mfma_f32_16x16x32_bf16 v[184:187], a[148:151], a[200:203], v[184:187]\n"
    "ds_read_b64_tr_b16 a[182:183], v17 offset:20480\n"
    "s_add_u32 s75, s75, 33792\n"
    "v_and_b32 v39, 7, v1\n"
    "v_mfma_f32_16x16x32_bf16 v[188:191], a[148:151], a[204:207], v[188:191]\n"
    "s_waitcnt lgkmcnt(12)\n"
    "v_mfma_f32_16x16x32_bf16 v[160:163], a[152:155], a[208:211], v[160:163]\n"
    "ds_read_b64_tr_b16 a[184:185], v16 offset:24576\n"
    "v_mfma_f32_16x16x32_bf16 v[164:167], a[152:155], a[212:215], v[164:167]\n"
    "v_mfma_f32_16x16x32_bf16 v[168:171], a[152:155], a[216:219], v[168:171]\n"
    "ds_read_b64_tr_b16 a[186:187], v16 offset:28672\n"
    "v_mfma_f32_16x16x32_bf16 v[172:175], a[152:155], a[220:223], v[172:175]\n"
    "buffer_load_dwordx4 v[104:107], v30, s[20:23], s75 offen sc1\n"
    "v_lshl_add_u32 v38, v39, 4, v38\n"
    "v_mfma_f32_16x16x32_bf16 v[176:179], a[156:159], a[208:211], v[176:179]\n"
    "ds_read_b64_tr_b16 a[188:189], v17 offset:24576\n"
    "v_mfma_f32_16x16x32_bf16 v[180:183], a[156:159], a[212:215], v[180:183]\n"
    "v_mfma_f32_16x16x32_bf16 v[184:187], a[156:159], a[216:219], v[184:187]\n"
    "ds_read_b64_tr_b16 a[190:191], v17 offset:28672\n"
    "v_mfma_f32_16x16x32_bf16 v[188:191], a[156:159], a[220:223], v[188:191]\n"
    "s_add_u32 s75, s75, 33792\n"
    "s_waitcnt lgkmcnt(12)\n"
    "v_mfma_f32_16x16x32_bf16 v[160:163], a[160:163], a[224:227], v[160:163]\n"
    "ds_read_b64_tr_b16 a[128:129], v16 offset:32768\n"
    "v_mfma_f32_16x16x32_bf16 v[164:167], a[160:163], a[228:231], v[164:167]\n"
    "v_add_u32 v38, s66, v38\n"
    "v_mfma_f32_16x16x32_bf16 v[168:171], a[160:163], a[232:235], v[168:171]\n"
    "ds_read_b64_tr_b16 a[130:131], v16 offset:36864\n"
    "v_mfma_f32_16x16x32_bf16 v[172:175], a[160:163], a[236:239], v[172:175]\n"
    "buffer_load_dwordx4 v[108:111], v30, s[20:23], s75 offen sc1\n"
    "v_mfma_f32_16x16x32_bf16 v[176:179], a[164:167], a[224:227], v[176:179]\n"
    "ds_read_b64_tr_b16 a[132:133], v17 offset:32768\n"
    "v_mfma_f32_16x16x32_bf16 v[180:183], a[164:167], a[228:231], v[180:183]\n"
    "v_mfma_f32_16x16x32_bf16 v[184:187], a[164:167], a[232:235], v[184:187]\n"
    "ds_read_b64_tr_b16 a[134:135], v17 offset:36864\n"
    "v_mfma_f32_16x16x32_bf16 v[188:191], a[164:167], a[236:239], v[188:191]\n"
    "s_lshl_b32 s30, s39, 2\n"
    "s_waitcnt lgkmcnt(12)\n"
    "v_mfma_f32_16x16x32_bf16 v[160:163], a[168:171], a[240:243], v[160:163]\n"
    "ds_read_b64_tr_b16 a[136:137], v16 offset:40960\n"
    "s_add_u32 s75, s75, 33792\n"
    "v_mfma_f32_16x16x32_bf16 v[164:167], a[168:171], a[244:247], v[164:167]\n"
    "v_mfma_f32_16x16x32_bf16 v[168:171], a[168:171], a[248:251], v[168:171]\n"
    "ds_read_b64_tr_b16 a[138:139], v16 offset:45056\n"
    "v_mfma_f32_16x16x32_bf16 v[172:175], a[168:171], a[252:255], v[172:175]\n"
    "v_mfma_f32_16x16x32_bf16 v[176:179], a[172:175], a[240:243], v[176:179]\n"
    "ds_read_b64_tr_b16 a[140:141], v17 offset:40960\n"
    "buffer_load_dword v56, v31, s[20:23], 0 offen offset:0 sc1\n"
    "s_add_u32 s30, s30, s34\n"
    "v_mfma_f32_16x16x32_bf16 v[180:183], a[172:175], a[244:247], v[180:183]\n"
    "v_mfma_f32_16x16x32_bf16 v[184:187], a[172:175], a[248:251], v[184:187]\n"
    "ds_read_b64_tr_b16 a[142:143], v17 offset:45056\n"
    "v_mfma_f32_16x16x32_bf16 v[188:191], a[172:175], a[252:255], v[188:191]\n"
    "s_waitcnt lgkmcnt(12)\n"
    "v_mfma_f32_16x16x32_bf16 v[160:163], a[176:179], a[0:3], v[160:163]\n"
    "ds_read_b64_tr_b16 a[144:145], v10 offset:32768\n"
    "buffer_load_dword v57, v31, s[20:23], 0 offen offset:64 sc1\n"
    "v_mfma_f32_16x16x32_bf16 v[164:167], a[176:179], a[4:7], v[164:167]\n"
    "v_mfma_f32_16x16x32_bf16 v[168:171], a[176:179], a[8:11], v[168:171]\n"
    "ds_read_b64_tr_b16 a[146:147], v10 offset:36864\n"
    "s_mul_i32 s68, s30, 13056\n"
    "v_mfma_f32_16x16x32_bf16 v[172:175], a[176:179], a[12:15], v[172:175]\n"
    "v_mfma_f32_16x16x32_bf16 v[176:179], a[180:183], a[0:3], v[176:179]\n"
    "ds_read_b64_tr_b16 a[148:149], v11 offset:32768\n"
    "v_mfma_f32_16x16x32_bf16 v[180:183], a[180:183], a[4:7], v[180:183]\n"
    "buffer_load_dword v58, v31, s[20:23], 0 offen offset:128 sc1\n"
    "v_mfma_f32_16x16x32_bf16 v[184:187], a[180:183], a[8:11], v[184:187]\n"
    "ds_read_b64_tr_b16 a[150:151], v11 offset:36864\n"
    "v_mfma_f32_16x16x32_bf16 v[188:191], a[180:183], a[12:15], v[188:191]\n"
    "s_add_u32 s67, s68, 4352\n"
    "s_waitcnt vmcnt(15) lgkmcnt(12)\n"
    "v_mfma_f32_16x16x32_bf16 v[160:163], a[184:187], a[16:19], v[160:163]\n"
    "ds_read_b64_tr_b16 a[152:153], v10 offset:40960\n"
    "v_mfma_f32_16x16x32_bf16 v[164:167], a[184:187], a[20:23], v[164:167]\n"
    "buffer_load_dword v59, v31, s[20:23], 0 offen offset:192 sc1\n"
    "v_mfma_f32_16x16x32_bf16 v[168:171], a[184:187], a[24:27], v[168:171]\n"
    "ds_read_b64_tr_b16 a[154:155], v10 offset:45056\n"
    "v_mfma_f32_16x16x32_bf16 v[172:175], a[184:187], a[28:31], v[172:175]\n"
    "v_mfma_f32_16x16x32_bf16 v[176:179], a[188:191], a[16:19], v[176:179]\n"
    "ds_read_b64_tr_b16 a[156:157], v11 offset:40960\n"
    "v_mfma_f32_16x16x32_bf16 v[180:183], a[188:191], a[20:23], v[180:183]\n"
    "buffer_load_dwordx4 v[40:43], v26, s[8:11], 0 offen\n"
    "s_add_u32 s69, s68, 8704\n"
    "v_mfma_f32_16x16x32_bf16 v[184:187], a[188:191], a[24:27], v[184:187]\n"
    "ds_read_b64_tr_b16 a[158:159], v11 offset:45056\n"
    "v_mfma_f32_16x16x32_bf16 v[188:191], a[188:191], a[28:31], v[188:191]\n"
    "s_waitcnt vmcnt(13) lgkmcnt(12)\n"
    "v_mfma_f32_16x16x32_bf16 v[160:163], a[128:131], a[32:35], v[160:163]\n"
    "ds_read_b64_tr_b16 a[160:161], v10 offset:49152\n"
    "v_mfma_f32_16x16x32_bf16 v[164:167], a[128:131], a[36:39], v[164:167]\n"
    "v_mfma_f32_16x16x32_bf16 v[168:171], a[128:131], a[40:43], v[168:171]\n"
    "ds_read_b64_tr_b16 a[162:163], v10 offset:53248\n"
    "buffer_load_dwordx4 v[44:47], v27, s[8:11], 0 offen\n"
    "v_lshlrev_b32 v32, 2, v1\n"
    "v_mfma_f32_16x16x32_bf16 v[172:175], a[128:131], a[44:47], v[172:175]\n"
    "v_mfma_f32_16x16x32_bf16 v[176:179], a[132:135], a[32:35], v[176:179]\n"
    "ds_read_b64_tr_b16 a[164:165], v11 offset:49152\n"
    "v_mfma_f32_16x16x32_bf16 v[180:183], a[132:135], a[36:39], v[180:183]\n"
    "v_mfma_f32_16x16x32_bf16 v[184:187], a[132:135], a[40:43], v[184:187]\n"
    "ds_read_b64_tr_b16 a[166:167], v11 offset:53248\n"
    "buffer_load_dwordx4 v[48:51], v28, s[8:11], 0 offen\n"
    "v_mfma_f32_16x16x32_bf16 v[188:191], a[132:135], a[44:47], v[188:191]\n"
    "s_waitcnt vmcnt(11) lgkmcnt(12)\n"
    "v_mfma_f32_16x16x32_bf16 v[160:163], a[136:139], a[48:51], v[160:163]\n"
    "ds_read_b64_tr_b16 a[168:169], v10 offset:57344\n"
    "v_add_u32 v32, 0x1000, v32\n"
    "v_mfma_f32_16x16x32_bf16 v[164:167], a[136:139], a[52:55], v[164:167]\n"
    "v_mfma_f32_16x16x32_bf16 v[168:171], a[136:139], a[56:59], v[168:171]\n"
    "ds_read_b64_tr_b16 a[170:171], v10 offset:61440\n"
    "buffer_load_dwordx4 v[52:55], v29, s[8:11], 0 offen\n"
    "v_mfma_f32_16x16x32_bf16 v[172:175], a[136:139], a[60:63], v[172:175]\n"
    "v_mfma_f32_16x16x32_bf16 v[176:179], a[140:143], a[48:51], v[176:179]\n"
    "ds_read_b64_tr_b16 a[172:173], v11 offset:57344\n"
    "v_mfma_f32_16x16x32_bf16 v[180:183], a[140:143], a[52:55], v[180:183]\n"
    "v_mfma_f32_16x16x32_bf16 v[184:187], a[140:143], a[56:59], v[184:187]\n"
    "ds_read_b64_tr_b16 a[174:175], v11 offset:61440\n"
    "v_mfma_f32_16x16x32_bf16 v[188:191], a[140:143], a[60:63], v[188:191]\n"
    "s_waitcnt lgkmcnt(12)\n"
    "v_mfma_f32_16x16x32_bf16 v[192:195], a[144:147], a[96:99], v[192:195]\n"
    "ds_read_b64_tr_b16 a[176:177], v18 offset:0\n"
    "v_mfma_f32_16x16x32_bf16 v[196:199], a[144:147], a[100:103], v[196:199]\n"
    "v_mfma_f32_16x16x32_bf16 v[200:203], a[144:147], a[104:107], v[200:203]\n"
    "ds_read_b64_tr_b16 a[178:179], v18 offset:4096\n"
    "s_xor_b32 s31, s30, 8\n"
    "v_mfma_f32_16x16x32_bf16 v[204:207], a[144:147], a[108:111], v[204:207]\n"
    "s_mul_i32 s31, s31, 13056\n"
    "v_mfma_f32_16x16x32_bf16 v[208:211], a[148:151], a[96:99], v[208:211]\n"
    "ds_read_b64_tr_b16 a[180:181], v19 offset:0\n"
    "s_add_u32 s31, s31, 0\n"
    "v_mfma_f32_16x16x32_bf16 v[212:215], a[148:151], a[100:103], v[212:215]\n"
    "v_mfma_f32_16x16x32_bf16 v[216:219], a[148:151], a[104:107], v[216:219]\n"
    "ds_read_b64_tr_b16 a[182:183], v19 offset:4096\n"
    "v_cvt_pk_bf16_f32 v160, v160, v161\n"
    "v_mfma_f32_16x16x32_bf16 v[220:223], a[148:151], a[108:111], v[220:223]\n"
    "v_cvt_pk_bf16_f32 v161, v162, v163\n"
    "s_waitcnt lgkmcnt(12)\n"
    "v_mfma_f32_16x16x32_bf16 v[192:195], a[152:155], a[112:115], v[192:195]\n"
    "ds_read_b64_tr_b16 a[184:185], v18 offset:8192\n"
    "v_cvt_pk_bf16_f32 v162, v176, v177\n"
    "v_mfma_f32_16x16x32_bf16 v[196:199], a[152:155], a[116:119], v[196:199]\n"
    "v_mfma_f32_16x16x32_bf16 v[200:203], a[152:155], a[120:123], v[200:203]\n"
    "ds_read_b64_tr_b16 a[186:187], v18 offset:12288\n"
    "v_cvt_pk_bf16_f32 v163, v178, v179\n"
    "v_mfma_f32_16x16x32_bf16 v[204:207], a[152:155], a[124:127], v[204:207]\n"
    "buffer_store_dwordx4 v[160:163], v2, s[20:23], s31 offen offset:0 sc1\n"
    "v_mfma_f32_16x16x32_bf16 v[208:211], a[156:159], a[112:115], v[208:211]\n"
    "ds_read_b64_tr_b16 a[188:189], v19 offset:8192\n"
    "v_mfma_f32_16x16x32_bf16 v[212:215], a[156:159], a[116:119], v[212:215]\n"
    "v_add3_u32 v36, v160, v161, s70\n"
    "v_mfma_f32_16x16x32_bf16 v[216:219], a[156:159], a[120:123], v[216:219]\n"
    "ds_read_b64_tr_b16 a[190:191], v19 offset:12288\n"
    "v_add3_u32 v36, v162, v163, v36\n"
    "v_mfma_f32_16x16x32_bf16 v[220:223], a[156:159], a[124:127], v[220:223]\n"
    "v_cvt_pk_bf16_f32 v164, v164, v165\n"
    "s_waitcnt lgkmcnt(12)\n"
    "v_mfma_f32_16x16x32_bf16 v[192:195], a[160:163], a[192:195], v[192:195]\n"
    "ds_read_b64_tr_b16 a[128:129], v18 offset:16384\n"
    "v_mfma_f32_16x16x32_bf16 v[196:199], a[160:163], a[196:199], v[196:199]\n"
    "v_cvt_pk_bf16_f32 v165, v166, v167\n"
    "v_mfma_f32_16x16x32_bf16 v[200:203], a[160:163], a[200:203], v[200:203]\n"
    "ds_read_b64_tr_b16 a[130:131], v18 offset:20480\n"
    "v_cvt_pk_bf16_f32 v166, v180, v181\n"
    "v_mfma_f32_16x16x32_bf16 v[204:207], a[160:163], a[204:207], v[204:207]\n"
    "v_cvt_pk_bf16_f32 v167, v182, v183\n"
    "v_mfma_f32_16x16x32_bf16 v[208:211], a[164:167], a[192:195], v[208:211]\n"
    "ds_read_b64_tr_b16 a[132:133], v19 offset:16384\n"
    "v_mfma_f32_16x16x32_bf16 v[212:215], a[164:167], a[196:199], v[212:215]\n"
    "buffer_store_dwordx4 v[164:167], v2, s[20:23], s31 offen offset:1024 sc1\n"
    "v_mfma_f32_16x16x32_bf16 v[216:219], a[164:167], a[200:203], v[216:219]\n"
    "ds_read_b64_tr_b16 a[134:135], v19 offset:20480\n"
    "v_add3_u32 v36, v164, v165, v36\n"
    "v_mfma_f32_16x16x32_bf16 v[220:223], a[164:167], a[204:207], v[220:223]\n"
    "s_waitcnt lgkmcnt(12)\n"
    "v_mfma_f32_16x16x32_bf16 v[192:195], a[168:171], a[208:211], v[192:195]\n"
    "ds_read_b64_tr_b16 a[136:137], v18 offset:24576\n"
    "v_add3_u32 v36, v166, v167, v36\n"
    "v_mfma_f32_16x16x32_bf16 v[196:199], a[168:171], a[212:215], v[196:199]\n"
    "v_cvt_pk_bf16_f32 v168, v168, v169\n"
    "v_mfma_f32_16x16x32_bf16 v[200:203], a[168:171], a[216:219], v[200:203]\n"
    "ds_read_b64_tr_b16 a[138:139], v18 offset:28672\n"
    "v_cvt_pk_bf16_f32 v169, v170, v171\n"
    "v_mfma_f32_16x16x32_bf16 v[204:207], a[168:171], a[220:223], v[204:207]\n"
    "v_mfma_f32_16x16x32_bf16 v[208:211], a[172:175], a[208:211], v[208:211]\n"
    "ds_read_b64_tr_b16 a[140:141], v19 offset:24576\n"
    "v_cvt_pk_bf16_f32 v170, v184, v185\n"
    "v_mfma_f32_16x16x32_bf16 v[212:215], a[172:175], a[212:215], v[212:215]\n"
    "v_cvt_pk_bf16_f32 v171, v186, v187\n"
    "v_mfma_f32_16x16x32_bf16 v[216:219], a[172:175], a[216:219], v[216:219]\n"
    "ds_read_b64_tr_b16 a[142:143], v19 offset:28672\n"
    "buffer_store_dwordx4 v[168:171], v2, s[20:23], s31 offen offset:2048 sc1\n"
    "v_mfma_f32_16x16x32_bf16 v[220:223], a[172:175], a[220:223], v[220:223]\n"
    "s_waitcnt lgkmcnt(12)\n"
    "v_mfma_f32_16x16x32_bf16 v[192:195], a[176:179], a[224:227], v[192:195]\n"
    "ds_read_b64_tr_b16 a[144:145], v18 offset:32768\n"
    "v_add3_u32 v36, v168, v169, v36\n"
    "v_mfma_f32_16x16x32_bf16 v[196:199], a[176:179], a[228:231], v[196:199]\n"
    "v_add3_u32 v36, v170, v171, v36\n"
    "v_mfma_f32_16x16x32_bf16 v[200:203], a[176:179], a[232:235], v[200:203]\n"
    "ds_read_b64_tr_b16 a[146:147], v18 offset:36864\n"
    "v_mfma_f32_16x16x32_bf16 v[204:207], a[176:179], a[236:239], v[204:207]\n"
    "v_cvt_pk_bf16_f32 v172, v172, v173\n"
    "v_mfma_f32_16x16x32_bf16 v[208:211], a[180:183], a[224:227], v[208:211]\n"
    "ds_read_b64_tr_b16 a[148:149], v19 offset:32768\n"
    "v_cvt_pk_bf16_f32 v173, v174, v175\n"
    "v_mfma_f32_16x16x32_bf16 v[212:215], a[180:183], a[228:231], v[212:215]\n"
    "v_cvt_pk_bf16_f32 v174, v188, v189\n"
    "v_mfma_f32_16x16x32_bf16 v[216:219], a[180:183], a[232:235], v[216:219]\n"
    "ds_read_b64_tr_b16 a[150:151], v19 offset:36864\n"
    "v_mfma_f32_16x16x32_bf16 v[220:223], a[180:183], a[236:239], v[220:223]\n"
    "v_cvt_pk_bf16_f32 v175, v190, v191\n"
    "s_waitcnt lgkmcnt(12)\n"
    "v_mfma_f32_16x16x32_bf16 v[192:195], a[184:187], a[240:243], v[192:195]\n"
    "ds_read_b64_tr_b16 a[152:153], v18 offset:40960\n"
    "buffer_store_dwordx4 v[172:175], v2, s[20:23], s31 offen offset:3072 sc1\n"
    "v_mfma_f32_16x16x32_bf16 v[196:199], a[184:187], a[244:247], v[196:199]\n"
    "v_add3_u32 v36, v172, v173, v36\n"
    "v_mfma_f32_16x16x32_bf16 v[200:203], a[184:187], a[248:251], v[200:203]\n"
    "ds_read_b64_tr_b16 a[154:155], v18 offset:45056\n"
    "v_mfma_f32_16x16x32_bf16 v[204:207], a[184:187], a[252:255], v[204:207]\n"
    "v_add3_u32 v36, v174, v175, v36\n"
    "v_mfma_f32_16x16x32_bf16 v[208:211], a[188:191], a[240:243], v[208:211]\n"
    "ds_read_b64_tr_b16 a[156:157], v19 offset:40960\n"
    "buffer_store_dword v36, v32, s[20:23], s31 offen sc1\n"
    "v_mfma_f32_16x16x32_bf16 v[212:215], a[188:191], a[244:247], v[212:215]\n"
    "v_mfma_f32_16x16x32_bf16 v[216:219], a[188:191], a[248:251], v[216:219]\n"
    "ds_read_b64_tr_b16 a[158:159], v19 offset:45056\n"
    "v_mfma_f32_16x16x32_bf16 v[220:223], a[188:191], a[252:255], v[220:223]\n"
    "s_waitcnt lgkmcnt(12)\n"
    "v_mfma_f32_16x16x32_bf16 v[192:195], a[128:131], a[0:3], v[192:195]\n"
    "ds_read_b64_tr_b16 a[160:161], v12 offset:32768\n"
    "v_mfma_f32_16x16x32_bf16 v[196:199], a[128:131], a[4:7], v[196:199]\n"
    "v_mfma_f32_16x16x32_bf16 v[200:203], a[128:131], a[8:11], v[200:203]\n"
    "ds_read_b64_tr_b16 a[162:163], v12 offset:36864\n"
    "v_mfma_f32_16x16x32_bf16 v[204:207], a[128:131], a[12:15], v[204:207]\n"
    "v_mfma_f32_16x16x32_bf16 v[208:211], a[132:135], a[0:3], v[208:211]\n"
    "ds_read_b64_tr_b16 a[164:165], v13 offset:32768\n"
    "v_mfma_f32_16x16x32_bf16 v[212:215], a[132:135], a[4:7], v[212:215]\n"
    "v_mfma_f32_16x16x32_bf16 v[216:219], a[132:135], a[8:11], v[216:219]\n"
    "ds_read_b64_tr_b16 a[166:167], v13 offset:36864\n"
    "v_mfma_f32_16x16x32_bf16 v[220:223], a[132:135], a[12:15], v[220:223]\n"
    "s_waitcnt lgkmcnt(12)\n"
    "v_mfma_f32_16x16x32_bf16 v[192:195], a[136:139], a[16:19], v[192:195]\n"
    "ds_read_b64_tr_b16 a[168:169], v12 offset:40960\n"
    "v_mfma_f32_16x16x32_bf16 v[196:199], a[136:139], a[20:23], v[196:199]\n"
    "v_mfma_f32_16x16x32_bf16 v[200:203], a[136:139], a[24:27], v[200:203]\n"
    "ds_read_b64_tr_b16 a[170:171], v12 offset:45056\n"
    "v_mfma_f32_16x16x32_bf16 v[204:207], a[136:139], a[28:31], v[204:207]\n"
    "v_mfma_f32_16x16x32_bf16 v[208:211], a[140:143], a[16:19], v[208:211]\n"
    "ds_read_b64_tr_b16 a[172:173], v13 offset:40960\n"
    "v_mfma_f32_16x16x32_bf16 v[212:215], a[140:143], a[20:23], v[212:215]\n"
    "v_mfma_f32_16x16x32_bf16 v[216:219], a[140:143], a[24:27], v[216:219]\n"
    "ds_read_b64_tr_b16 a[174:175], v13 offset:45056\n"
    "v_mfma_f32_16x16x32_bf16 v[220:223], a[140:143], a[28:31], v[220:223]\n"
    "s_waitcnt lgkmcnt(12)\n"
    "v_mfma_f32_16x16x32_bf16 v[192:195], a[144:147], a[32:35], v[192:195]\n"
    "ds_read_b64_tr_b16 a[176:177], v12 offset:49152\n"
    "v_mfma_f32_16x16x32_bf16 v[196:199], a[144:147], a[36:39], v[196:199]\n"
    "v_mfma_f32_16x16x32_bf16 v[200:203], a[144:147], a[40:43], v[200:203]\n"
    "ds_read_b64_tr_b16 a[178:179], v12 offset:53248\n"
    "v_mfma_f32_16x16x32_bf16 v[204:207], a[144:147], a[44:47], v[204:207]\n"
    "v_mfma_f32_16x16x32_bf16 v[208:211], a[148:151], a[32:35], v[208:211]\n"
    "ds_read_b64_tr_b16 a[180:181], v13 offset:49152\n"
    "v_mfma_f32_16x16x32_bf16 v[212:215], a[148:151], a[36:39], v[212:215]\n"
    "v_mfma_f32_16x16x32_bf16 v[216:219], a[148:151], a[40:43], v[216:219]\n"
    "ds_read_b64_tr_b16 a[182:183], v13 offset:53248\n"
    "v_mfma_f32_16x16x32_bf16 v[220:223], a[148:151], a[44:47], v[220:223]\n"
    "s_waitcnt lgkmcnt(12)\n"
    "v_mfma_f32_16x16x32_bf16 v[192:195], a[152:155], a[48:51], v[192:195]\n"
    "ds_read_b64_tr_b16 a[184:185], v12 offset:57344\n"
    "v_mfma_f32_16x16x32_bf16 v[196:199], a[152:155], a[52:55], v[196:199]\n"
    "v_mfma_f32_16x16x32_bf16 v[200:203], a[152:155], a[56:59], v[200:203]\n"
    "ds_read_b64_tr_b16 a[186:187], v12 offset:61440\n"
    "v_mfma_f32_16x16x32_bf16 v[204:207], a[152:155], a[60:63], v[204:207]\n"
    "v_mfma_f32_16x16x32_bf16 v[208:211], a[156:159], a[48:51], v[208:211]\n"
    "ds_read_b64_tr_b16 a[188:189], v13 offset:57344\n"
    "v_mfma_f32_16x16x32_bf16 v[212:215], a[156:159], a[52:55], v[212:215]\n"
    "v_mfma_f32_16x16x32_bf16 v[216:219], a[156:159], a[56:59], v[216:219]\n"
    "ds_read_b64_tr_b16 a[190:191], v13 offset:61440\n"
    "v_mfma_f32_16x16x32_bf16 v[220:223], a[156:159], a[60:63], v[220:223]\n"
    "s_waitcnt lgkmcnt(12)\n"
    "v_mfma_f32_16x16x32_bf16 v[224:227], a[160:163], a[96:99], v[224:227]\n"
    "ds_read_b64_tr_b16 a[128:129], v20 offset:0\n"
    "v_mfma_f32_16x16x32_bf16 v[228:231], a[160:163], a[100:103], v[228:231]\n"
    "s_waitcnt vmcnt(13)\n"
    "v_mfma_f32_16x16x32_bf16 v[232:235], a[160:163], a[104:107], v[232:235]\n"
    "ds_read_b64_tr_b16 a[130:131], v20 offset:4096\n"
    "s_xor_b32 s31, s30, 16\n"
    "v_mfma_f32_16x16x32_bf16 v[236:239], a[160:163], a[108:111], v[236:239]\n"
    "s_mul_i32 s31, s31, 13056\n"
    "v_mfma_f32_16x16x32_bf16 v[240:243], a[164:167], a[96:99], v[240:243]\n"
    "ds_read_b64_tr_b16 a[132:133], v21 offset:0\n"
    "s_add_u32 s31, s31, 4352\n"
    "v_mfma_f32_16x16x32_bf16 v[244:247], a[164:167], a[100:103], v[244:247]\n"
    "ds_write_b128 v38, v[96:99] offset:0\n"
    "v_mfma_f32_16x16x32_bf16 v[248:251], a[164:167], a[104:107], v[248:251]\n"
    "ds_read_b64_tr_b16 a[134:135], v21 offset:4096\n"
    "v_cvt_pk_bf16_f32 v192, v192, v193\n"
    "v_mfma_f32_16x16x32_bf16 v[252:255], a[164:167], a[108:111], v[252:255]\n"
    "v_cvt_pk_bf16_f32 v193, v194, v195\n"
    "s_waitcnt lgkmcnt(13)\n"
    "v_mfma_f32_16x16x32_bf16 v[224:227], a[168:171], a[112:115], v[224:227]\n"
    "ds_read_b64_tr_b16 a[136:137], v20 offset:8192\n"
    "v_cvt_pk_bf16_f32 v194, v208, v209\n"
    "v_mfma_f32_16x16x32_bf16 v[228:231], a[168:171], a[116:119], v[228:231]\n"
    "ds_write_b128 v38, v[100:103] offset:1280\n"
    "v_mfma_f32_16x16x32_bf16 v[232:235], a[168:171], a[120:123], v[232:235]\n"
    "ds_read_b64_tr_b16 a[138:139], v20 offset:12288\n"
    "v_cvt_pk_bf16_f32 v195, v210, v211\n"
    "v_mfma_f32_16x16x32_bf16 v[236:239], a[168:171], a[124:127], v[236:239]\n"
    "buffer_store_dwordx4 v[192:195], v2, s[20:23], s31 offen offset:0 sc1\n"
    "v_mfma_f32_16x16x32_bf16 v[240:243], a[172:175], a[112:115], v[240:243]\n"
    "ds_read_b64_tr_b16 a[140:141], v21 offset:8192\n"
    "v_mfma_f32_16x16x32_bf16 v[244:247], a[172:175], a[116:119], v[244:247]\n"
    "v_add3_u32 v36, v192, v193, s70\n"
    "v_mfma_f32_16x16x32_bf16 v[248:251], a[172:175], a[120:123], v[248:251]\n"
    "ds_read_b64_tr_b16 a[142:143], v21 offset:12288\n"
    "v_add3_u32 v36, v194, v195, v36\n"
    "ds_write_b128 v38, v[104:107] offset:2560\n"
    "v_mfma_f32_16x16x32_bf16 v[252:255], a[172:175], a[124:127], v[252:255]\n"
    "v_cvt_pk_bf16_f32 v196, v196, v197\n"
    "s_waitcnt lgkmcnt(15)\n"
    "v_mfma_f32_16x16x32_bf16 v[224:227], a[176:179], a[192:195], v[224:227]\n"
    "ds_read_b64_tr_b16 a[144:145], v20 offset:16384\n"
    "v_mfma_f32_16x16x32_bf16 v[228:231], a[176:179], a[196:199], v[228:231]\n"
    "v_cvt_pk_bf16_f32 v197, v198, v199\n"
    "v_mfma_f32_16x16x32_bf16 v[232:235], a[176:179], a[200:203], v[232:235]\n"
    "ds_read_b64_tr_b16 a[146:147], v20 offset:20480\n"
    "v_cvt_pk_bf16_f32 v198, v212, v213\n"
    "ds_write_b128 v38, v[108:111] offset:3840\n"
    "v_mfma_f32_16x16x32_bf16 v[236:239], a[176:179], a[204:207], v[236:239]\n"
    "v_cvt_pk_bf16_f32 v199, v214, v215\n"
    "v_mfma_f32_16x16x32_bf16 v[240:243], a[180:183], a[192:195], v[240:243]\n"
    "ds_read_b64_tr_b16 a[148:149], v21 offset:16384\n"
    "v_mfma_f32_16x16x32_bf16 v[244:247], a[180:183], a[196:199], v[244:247]\n"
    "buffer_store_dwordx4 v[196:199], v2, s[20:23], s31 offen offset:1024 sc1\n"
    "v_mfma_f32_16x16x32_bf16 v[248:251], a[180:183], a[200:203], v[248:251]\n"
    "ds_read_b64_tr_b16 a[150:151], v21 offset:20480\n"
    "v_add3_u32 v36, v196, v197, v36\n"
    "v_lshrrev_b32 v38, 2, v1\n"
    "v_mfma_f32_16x16x32_bf16 v[252:255], a[180:183], a[204:207], v[252:255]\n"
    "s_waitcnt lgkmcnt(15)\n"
    "v_mfma_f32_16x16x32_bf16 v[224:227], a[184:187], a[208:211], v[224:227]\n"
    "ds_read_b64_tr_b16 a[152:153], v20 offset:24576\n"
    "v_add3_u32 v36, v198, v199, v36\n"
    "v_mfma_f32_16x16x32_bf16 v[228:231], a[184:187], a[212:215], v[228:231]\n"
    "v_cvt_pk_bf16_f32 v200, v200, v201\n"
    "v_mfma_f32_16x16x32_bf16 v[232:235], a[184:187], a[216:219], v[232:235]\n"
    "ds_read_b64_tr_b16 a[154:155], v20 offset:28672\n"
    "v_cvt_pk_bf16_f32 v201, v202, v203\n"
    "v_mfma_f32_16x16x32_bf16 v[236:239], a[184:187], a[220:223], v[236:239]\n"
    "v_mul_u32_u24 v38, 160, v38\n"
    "v_mfma_f32_16x16x32_bf16 v[240:243], a[188:191], a[208:211], v[240:243]\n"
    "ds_read_b64_tr_b16 a[156:157], v21 offset:24576\n"
    "v_cvt_pk_bf16_f32 v202, v216, v217\n"
    "v_mfma_f32_16x16x32_bf16 v[244:247], a[188:191], a[212:215], v[244:247]\n"
    "v_cvt_pk_bf16_f32 v203, v218, v219\n"
    "v_mfma_f32_16x16x32_bf16 v[248:251], a[188:191], a[216:219], v[248:251]\n"
    "ds_read_b64_tr_b16 a[158:159], v21 offset:28672\n"
    "buffer_store_dwordx4 v[200:203], v2, s[20:23], s31 offen offset:2048 sc1\n"
    "v_mfma_f32_16x16x32_bf16 v[252:255], a[188:191], a[220:223], v[252:255]\n"
    "v_and_b32 v39, 3, v1\n"
    "s_waitcnt lgkmcnt(15)\n"
    "v_mfma_f32_16x16x32_bf16 v[224:227], a[128:131], a[224:227], v[224:227]\n"
    "ds_read_b64_tr_b16 a[160:161], v20 offset:32768\n"
    "v_add3_u32 v36, v200, v201, v36\n"
    "v_mfma_f32_16x16x32_bf16 v[228:231], a[128:131], a[228:231], v[228:231]\n"
    "v_add3_u32 v36, v202, v203, v36\n"
    "v_mfma_f32_16x16x32_bf16 v[232:235], a[128:131], a[232:235], v[232:235]\n"
    "ds_read_b64_tr_b16 a[162:163], v20 offset:36864\n"
    "v_mfma_f32_16x16x32_bf16 v[236:239], a[128:131], a[236:239], v[236:239]\n"
    "v_cvt_pk_bf16_f32 v204, v204, v205\n"
    "v_lshl_add_u32 v38, v39, 3, v38\n"
    "v_mfma_f32_16x16x32_bf16 v[240:243], a[132:135], a[224:227], v[240:243]\n"
    "ds_read_b64_tr_b16 a[164:165], v21 offset:32768\n"
    "v_cvt_pk_bf16_f32 v205, v206, v207\n"
    "v_mfma_f32_16x16x32_bf16 v[244:247], a[132:135], a[228:231], v[244:247]\n"
    "v_cvt_pk_bf16_f32 v206, v220, v221\n"
    "v_mfma_f32_16x16x32_bf16 v[248:251], a[132:135], a[232:235], v[248:251]\n"
    "ds_read_b64_tr_b16 a[166:167], v21 offset:36864\n"
    "v_mfma_f32_16x16x32_bf16 v[252:255], a[132:135], a[236:239], v[252:255]\n"
    "v_cvt_pk_bf16_f32 v207, v222, v223\n"
    "s_waitcnt lgkmcnt(14)\n"
    "v_mfma_f32_16x16x32_bf16 v[224:227], a[136:139], a[240:243], v[224:227]\n"
    "ds_read_b64_tr_b16 a[168:169], v20 offset:40960\n"
    "buffer_store_dwordx4 v[204:207], v2, s[20:23], s31 offen offset:3072 sc1\n"
    "v_add_u32 v38, s66, v38\n"
    "v_mfma_f32_16x16x32_bf16 v[228:231], a[136:139], a[244:247], v[228:231]\n"
    "v_add3_u32 v36, v204, v205, v36\n"
    "v_mfma_f32_16x16x32_bf16 v[232:235], a[136:139], a[248:251], v[232:235]\n"
    "ds_read_b64_tr_b16 a[170:171], v20 offset:45056\n"
    "v_mfma_f32_16x16x32_bf16 v[236:239], a[136:139], a[252:255], v[236:239]\n"
    "v_add3_u32 v36, v206, v207, v36\n"
    "v_mfma_f32_16x16x32_bf16 v[240:243], a[140:143], a[240:243], v[240:243]\n"
    "ds_read_b64_tr_b16 a[172:173], v21 offset:40960\n"
    "buffer_store_dword v36, v32, s[20:23], s31 offen sc1\n"
    "ds_read_b64_tr_b16 v[112:113], v38 offset:0\n"
    "v_mfma_f32_16x16x32_bf16 v[244:247], a[140:143], a[244:247], v[244:247]\n"
    "v_mfma_f32_16x16x32_bf16 v[248:251], a[140:143], a[248:251], v[248:251]\n"
    "ds_read_b64_tr_b16 a[174:175], v21 offset:45056\n"
    "v_mfma_f32_16x16x32_bf16 v[252:255], a[140:143], a[252:255], v[252:255]\n"
    "s_waitcnt lgkmcnt(13)\n"
    "v_mfma_f32_16x16x32_bf16 v[224:227], a[144:147], a[0:3], v[224:227]\n"
    "ds_read_b64_tr_b16 a[176:177], v6 offset:32768\n"
    "ds_read_b64_tr_b16 v[114:115], v38 offset:2560\n"
    "v_mfma_f32_16x16x32_bf16 v[228:231], a[144:147], a[4:7], v[228:231]\n"
    "v_mfma_f32_16x16x32_bf16 v[232:235], a[144:147], a[8:11], v[232:235]\n"
    "ds_read_b64_tr_b16 a[178:179], v6 offset:36864\n"
    "v_mfma_f32_16x16x32_bf16 v[236:239], a[144:147], a[12:15], v[236:239]\n"
    "v_mfma_f32_16x16x32_bf16 v[240:243], a[148:151], a[0:3], v[240:243]\n"
    "ds_read_b64_tr_b16 a[180:181], v7 offset:32768\n"
    "v_mfma_f32_16x16x32_bf16 v[244:247], a[148:151], a[4:7], v[244:247]\n"
    "ds_read_b64_tr_b16 v[116:117], v38 offset:32\n"
    "v_mfma_f32_16x16x32_bf16 v[248:251], a[148:151], a[8:11], v[248:251]\n"
    "ds_read_b64_tr_b16 a[182:183], v7 offset:36864\n"
    "v_mfma_f32_16x16x32_bf16 v[252:255], a[148:151], a[12:15], v[252:255]\n"
    "s_waitcnt lgkmcnt(15)\n"
    "v_mfma_f32_16x16x32_bf16 v[224:227], a[152:155], a[16:19], v[224:227]\n"
    "ds_read_b64_tr_b16 a[184:185], v6 offset:40960\n"
    "v_mfma_f32_16x16x32_bf16 v[228:231], a[152:155], a[20:23], v[228:231]\n"
    "ds_read_b64_tr_b16 v[118:119], v38 offset:2592\n"
    "v_mfma_f32_16x16x32_bf16 v[232:235], a[152:155], a[24:27], v[232:235]\n"
    "ds_read_b64_tr_b16 a[186:187], v6 offset:45056\n"
    "v_mfma_f32_16x16x32_bf16 v[236:239], a[152:155], a[28:31], v[236:239]\n"
    "v_mfma_f32_16x16x32_bf16 v[240:243], a[156:159], a[16:19], v[240:243]\n"
    "ds_read_b64_tr_b16 a[188:189], v7 offset:40960\n"
    "v_mfma_f32_16x16x32_bf16 v[244:247], a[156:159], a[20:23], v[244:247]\n"
    "ds_read_b64_tr_b16 v[120:121], v38 offset:64\n"
    "v_mfma_f32_16x16x32_bf16 v[248:251], a[156:159], a[24:27], v[248:251]\n"
    "ds_read_b64_tr_b16 a[190:191], v7 offset:45056\n"
    "v_mfma_f32_16x16x32_bf16 v[252:255], a[156:159], a[28:31], v[252:255]\n"
    "s_waitcnt lgkmcnt(15)\n"
    "v_mfma_f32_16x16x32_bf16 v[224:227], a[160:163], a[32:35], v[224:227]\n"
    "ds_read_b64_tr_b16 a[128:129], v6 offset:49152\n"
    "v_mfma_f32_16x16x32_bf16 v[228:231], a[160:163], a[36:39], v[228:231]\n"
    "v_mfma_f32_16x16x32_bf16 v[232:235], a[160:163], a[40:43], v[232:235]\n"
    "ds_read_b64_tr_b16 a[130:131], v6 offset:53248\n"
    "ds_read_b64_tr_b16 v[122:123], v38 offset:2624\n"
    "v_mfma_f32_16x16x32_bf16 v[236:239], a[160:163], a[44:47], v[236:239]\n"
    "v_mfma_f32_16x16x32_bf16 v[240:243], a[164:167], a[32:35], v[240:243]\n"
    "ds_read_b64_tr_b16 a[132:133], v7 offset:49152\n"
    "v_mfma_f32_16x16x32_bf16 v[244:247], a[164:167], a[36:39], v[244:247]\n"
    "v_mfma_f32_16x16x32_bf16 v[248:251], a[164:167], a[40:43], v[248:251]\n"
    "ds_read_b64_tr_b16 a[134:135], v7 offset:53248\n"
    "ds_read_b64_tr_b16 v[124:125], v38 offset:96\n"
    "v_mfma_f32_16x16x32_bf16 v[252:255], a[164:167], a[44:47], v[252:255]\n"
    "s_waitcnt lgkmcnt(15)\n"
    "v_mfma_f32_16x16x32_bf16 v[224:227], a[168:171], a[48:51], v[224:227]\n"
    "ds_read_b64_tr_b16 a[136:137], v6 offset:57344\n"
    "v_mfma_f32_16x16x32_bf16 v[228:231], a[168:171], a[52:55], v[228:231]\n"
    "v_mfma_f32_16x16x32_bf16 v[232:235], a[168:171], a[56:59], v[232:235]\n"
    "ds_read_b64_tr_b16 a[138:139], v6 offset:61440\n"
    "ds_read_b64_tr_b16 v[126:127], v38 offset:2656\n"
    "v_mfma_f32_16x16x32_bf16 v[236:239], a[168:171], a[60:63], v[236:239]\n"
    "v_mfma_f32_16x16x32_bf16 v[240:243], a[172:175], a[48:51], v[240:243]\n"
    "ds_read_b64_tr_b16 a[140:141], v7 offset:57344\n"
    "v_mfma_f32_16x16x32_bf16 v[244:247], a[172:175], a[52:55], v[244:247]\n"
    "v_mfma_f32_16x16x32_bf16 v[248:251], a[172:175], a[56:59], v[248:251]\n"
    "ds_read_b64_tr_b16 a[142:143], v7 offset:61440\n"
    "v_mfma_f32_16x16x32_bf16 v[252:255], a[172:175], a[60:63], v[252:255]\n"
    "s_waitcnt lgkmcnt(15)\n"
    "v_mfma_f32_16x16x32_bf16 v[128:131], a[176:179], a[96:99], v[128:131]\n"
    "ds_read_b64_tr_b16 a[144:145], v14 offset:0\n"
    "v_mfma_f32_16x16x32_bf16 v[132:135], a[176:179], a[100:103], v[132:135]\n"
    "v_mfma_f32_16x16x32_bf16 v[136:139], a[176:179], a[104:107], v[136:139]\n"
    "ds_read_b64_tr_b16 a[146:147], v14 offset:4096\n"
    "s_xor_b32 s31, s30, 24\n"
    "v_mfma_f32_16x16x32_bf16 v[140:143], a[176:179], a[108:111], v[140:143]\n"
    "s_mul_i32 s31, s31, 13056\n"
    "v_mfma_f32_16x16x32_bf16 v[144:147], a[180:183], a[96:99], v[144:147]\n"
    "ds_read_b64_tr_b16 a[148:149], v15 offset:0\n"
    "s_add_u32 s31, s31, 8704\n"
    "v_mfma_f32_16x16x32_bf16 v[148:151], a[180:183], a[100:103], v[148:151]\n"
    "v_mfma_f32_16x16x32_bf16 v[152:155], a[180:183], a[104:107], v[152:155]\n"
    "ds_read_b64_tr_b16 a[150:151], v15 offset:4096\n"
    "v_cvt_pk_bf16_f32 v224, v224, v225\n"
    "v_mfma_f32_16x16x32_bf16 v[156:159], a[180:183], a[108:111], v[156:159]\n"
    "v_cvt_pk_bf16_f32 v225, v226, v227\n"
    "s_waitcnt lgkmcnt(15)\n"
    "v_mfma_f32_16x16x32_bf16 v[128:131], a[184:187], a[112:115], v[128:131]\n"
    "ds_read_b64_tr_b16 a[152:153], v14 offset:8192\n"
    "v_cvt_pk_bf16_f32 v226, v240, v241\n"
    "v_mfma_f32_16x16x32_bf16 v[132:135], a[184:187], a[116:119], v[132:135]\n"
    "v_mfma_f32_16x16x32_bf16 v[136:139], a[184:187], a[120:123], v[136:139]\n"
    "ds_read_b64_tr_b16 a[154:155], v14 offset:12288\n"
    "v_cvt_pk_bf16_f32 v227, v242, v243\n"
    "v_mfma_f32_16x16x32_bf16 v[140:143], a[184:187], a[124:127], v[140:143]\n"
    "buffer_store_dwordx4 v[224:227], v2, s[20:23], s31 offen offset:0 sc1\n"
    "v_mfma_f32_16x16x32_bf16 v[144:147], a[188:191], a[112:115], v[144:147]\n"
    "ds_read_b64_tr_b16 a[156:157], v15 offset:8192\n"
    "v_mfma_f32_16x16x32_bf16 v[148:151], a[188:191], a[116:119], v[148:151]\n"
    "v_add3_u32 v36, v224, v225, s70\n"
    "v_mfma_f32_16x16x32_bf16 v[152:155], a[188:191], a[120:123], v[152:155]\n"
    "ds_read_b64_tr_b16 a[158:159], v15 offset:12288\n"
    "v_add3_u32 v36, v226, v227, v36\n"
    "v_mfma_f32_16x16x32_bf16 v[156:159], a[188:191], a[124:127], v[156:159]\n"
    "v_cvt_pk_bf16_f32 v228, v228, v229\n"
    "s_waitcnt lgkmcnt(14)\n"
    "v_mfma_f32_16x16x32_bf16 v[128:131], a[128:131], a[192:195], v[128:131]\n"
    "ds_read_b64_tr_b16 a[160:161], v14 offset:16384\n"
    "v_mfma_f32_16x16x32_bf16 v[132:135], a[128:131], a[196:199], v[132:135]\n"
    "v_cvt_pk_bf16_f32 v229, v230, v231\n"
    "v_mfma_f32_16x16x32_bf16 v[136:139], a[128:131], a[200:203], v[136:139]\n"
    "ds_read_b64_tr_b16 a[162:163], v14 offset:20480\n"
    "v_cvt_pk_bf16_f32 v230, v244, v245\n"
    "v_mfma_f32_16x16x32_bf16 v[140:143], a[128:131], a[204:207], v[140:143]\n"
    "v_cvt_pk_bf16_f32 v231, v246, v247\n"
    "v_mfma_f32_16x16x32_bf16 v[144:147], a[132:135], a[192:195], v[144:147]\n"
    "ds_read_b64_tr_b16 a[164:165], v15 offset:16384\n"
    "v_mfma_f32_16x16x32_bf16 v[148:151], a[132:135], a[196:199], v[148:151]\n"
    "buffer_store_dwordx4 v[228:231], v2, s[20:23], s31 offen offset:1024 sc1\n"
    "v_mfma_f32_16x16x32_bf16 v[152:155], a[132:135], a[200:203], v[152:155]\n"
    "ds_read_b64_tr_b16 a[166:167], v15 offset:20480\n"
    "v_add3_u32 v36, v228, v229, v36\n"
    "v_mfma_f32_16x16x32_bf16 v[156:159], a[132:135], a[204:207], v[156:159]\n"
    "s_waitcnt lgkmcnt(12)\n"
    "v_mfma_f32_16x16x32_bf16 v[128:131], a[136:139], a[208:211], v[128:131]\n"
    "ds_read_b64_tr_b16 a[168:169], v14 offset:24576\n"
    "v_add3_u32 v36, v230, v231, v36\n"
    "v_mfma_f32_16x16x32_bf16 v[132:135], a[136:139], a[212:215], v[132:135]\n"
    "v_cvt_pk_bf16_f32 v232, v232, v233\n"
    "v_mfma_f32_16x16x32_bf16 v[136:139], a[136:139], a[216:219], v[136:139]\n"
    "ds_read_b64_tr_b16 a[170:171], v14 offset:28672\n"
    "v_cvt_pk_bf16_f32 v233, v234, v235\n"
    "v_mfma_f32_16x16x32_bf16 v[140:143], a[136:139], a[220:223], v[140:143]\n"
    "v_mfma_f32_16x16x32_bf16 v[144:147], a[140:143], a[208:211], v[144:147]\n"
    "ds_read_b64_tr_b16 a[172:173], v15 offset:24576\n"
    "v_cvt_pk_bf16_f32 v234, v248, v249\n"
    "v_mfma_f32_16x16x32_bf16 v[148:151], a[140:143], a[212:215], v[148:151]\n"
    "v_cvt_pk_bf16_f32 v235, v250, v251\n"
    "v_mfma_f32_16x16x32_bf16 v[152:155], a[140:143], a[216:219], v[152:155]\n"
    "ds_read_b64_tr_b16 a[174:175], v15 offset:28672\n"
    "buffer_store_dwordx4 v[232:235], v2, s[20:23], s31 offen offset:2048 sc1\n"
    "v_mfma_f32_16x16x32_bf16 v[156:159], a[140:143], a[220:223], v[156:159]\n"
    "s_waitcnt lgkmcnt(12)\n"
    "v_mfma_f32_16x16x32_bf16 v[128:131], a[144:147], a[224:227], v[128:131]\n"
    "ds_read_b64_tr_b16 a[176:177], v14 offset:32768\n"
    "v_add3_u32 v36, v232, v233, v36\n"
    "v_mfma_f32_16x16x32_bf16 v[132:135], a[144:147], a[228:231], v[132:135]\n"
    "v_add3_u32 v36, v234, v235, v36\n"
    "v_mfma_f32_16x16x32_bf16 v[136:139], a[144:147], a[232:235], v[136:139]\n"
    "ds_read_b64_tr_b16 a[178:179], v14 offset:36864\n"
    "v_mfma_f32_16x16x32_bf16 v[140:143], a[144:147], a[236:239], v[140:143]\n"
    "v_cvt_pk_bf16_f32 v236, v236, v237\n"
    "v_mfma_f32_16x16x32_bf16 v[144:147], a[148:151], a[224:227], v[144:147]\n"
    "ds_read_b64_tr_b16 a[180:181], v15 offset:32768\n"
    "v_cvt_pk_bf16_f32 v237, v238, v239\n"
    "v_mfma_f32_16x16x32_bf16 v[148:151], a[148:151], a[228:231], v[148:151]\n"
    "v_cvt_pk_bf16_f32 v238, v252, v253\n"
    "v_mfma_f32_16x16x32_bf16 v[152:155], a[148:151], a[232:235], v[152:155]\n"
    "ds_read_b64_tr_b16 a[182:183], v15 offset:36864\n"
    "v_mfma_f32_16x16x32_bf16 v[156:159], a[148:151], a[236:239], v[156:159]\n"
    "v_cvt_pk_bf16_f32 v239, v254, v255\n"
    "s_waitcnt lgkmcnt(12)\n"
    "v_mfma_f32_16x16x32_bf16 v[128:131], a[152:155], a[240:243], v[128:131]\n"
    "ds_read_b64_tr_b16 a[184:185], v14 offset:40960\n"
    "buffer_store_dwordx4 v[236:239], v2, s[20:23], s31 offen offset:3072 sc1\n"
    "v_mfma_f32_16x16x32_bf16 v[132:135], a[152:155], a[244:247], v[132:135]\n"
    "v_add3_u32 v36, v236, v237, v36\n"
    "v_mfma_f32_16x16x32_bf16 v[136:139], a[152:155], a[248:251], v[136:139]\n"
    "ds_read_b64_tr_b16 a[186:187], v14 offset:45056\n"
    "v_mfma_f32_16x16x32_bf16 v[140:143], a[152:155], a[252:255], v[140:143]\n"
    "v_add3_u32 v36, v238, v239, v36\n"
    "v_mfma_f32_16x16x32_bf16 v[144:147], a[156:159], a[240:243], v[144:147]\n"
    "ds_read_b64_tr_b16 a[188:189], v15 offset:40960\n"
    "buffer_store_dword v36, v32, s[20:23], s31 offen sc1\n"
    "v_mfma_f32_16x16x32_bf16 v[148:151], a[156:159], a[244:247], v[148:151]\n"
    "v_mfma_f32_16x16x32_bf16 v[152:155], a[156:159], a[248:251], v[152:155]\n"
    "ds_read_b64_tr_b16 a[190:191], v15 offset:45056\n"
    "v_mfma_f32_16x16x32_bf16 v[156:159], a[156:159], a[252:255], v[156:159]\n"
    "s_waitcnt lgkmcnt(12)\n"
    "v_mfma_f32_16x16x32_bf16 v[128:131], a[160:163], a[0:3], v[128:131]\n"
    "v_mfma_f32_16x16x32_bf16 v[132:135], a[160:163], a[4:7], v[132:135]\n"
    "v_mfma_f32_16x16x32_bf16 v[136:139], a[160:163], a[8:11], v[136:139]\n"
    "v_mfma_f32_16x16x32_bf16 v[140:143], a[160:163], a[12:15], v[140:143]\n"
    "v_mfma_f32_16x16x32_bf16 v[144:147], a[164:167], a[0:3], v[144:147]\n"
    "v_mfma_f32_16x16x32_bf16 v[148:151], a[164:167], a[4:7], v[148:151]\n"
    "v_mfma_f32_16x16x32_bf16 v[152:155], a[164:167], a[8:11], v[152:155]\n"
    "v_mfma_f32_16x16x32_bf16 v[156:159], a[164:167], a[12:15], v[156:159]\n"
    "s_waitcnt lgkmcnt(8)\n"
    "v_mfma_f32_16x16x32_bf16 v[128:131], a[168:171], a[16:19], v[128:131]\n"
    "v_mfma_f32_16x16x32_bf16 v[132:135], a[168:171], a[20:23], v[132:135]\n"
    "v_mfma_f32_16x16x32_bf16 v[136:139], a[168:171], a[24:27], v[136:139]\n"
    "v_mfma_f32_16x16x32_bf16 v[140:143], a[168:171], a[28:31], v[140:143]\n"
    "v_mfma_f32_16x16x32_bf16 v[144:147], a[172:175], a[16:19], v[144:147]\n"
    "v_mfma_f32_16x16x32_bf16 v[148:151], a[172:175], a[20:23], v[148:151]\n"
    "v_mfma_f32_16x16x32_bf16 v[152:155], a[172:175], a[24:27], v[152:155]\n"
    "v_mfma_f32_16x16x32_bf16 v[156:159], a[172:175], a[28:31], v[156:159]\n"
    "s_waitcnt lgkmcnt(4)\n"
    "v_mfma_f32_16x16x32_bf16 v[128:131], a[176:179], a[32:35], v[128:131]\n"
    "v_mfma_f32_16x16x32_bf16 v[132:135], a[176:179], a[36:39], v[132:135]\n"
    "v_mfma_f32_16x16x32_bf16 v[136:139], a[176:179], a[40:43], v[136:139]\n"
    "v_mfma_f32_16x16x32_bf16 v[140:143], a[176:179], a[44:47], v[140:143]\n"
    "v_mfma_f32_16x16x32_bf16 v[144:147], a[180:183], a[32:35], v[144:147]\n"
    "v_mfma_f32_16x16x32_bf16 v[148:151], a[180:183], a[36:39], v[148:151]\n"
    "v_mfma_f32_16x16x32_bf16 v[152:155], a[180:183], a[40:43], v[152:155]\n"
    "v_mfma_f32_16x16x32_bf16 v[156:159], a[180:183], a[44:47], v[156:159]\n"
    "s_waitcnt lgkmcnt(0)\n"
    "v_mfma_f32_16x16x32_bf16 v[128:131], a[184:187], a[48:51], v[128:131]\n"
    "v_mfma_f32_16x16x32_bf16 v[132:135], a[184:187], a[52:55], v[132:135]\n"
    "v_mfma_f32_16x16x32_bf16 v[136:139], a[184:187], a[56:59], v[136:139]\n"
    "v_mfma_f32_16x16x32_bf16 v[140:143], a[184:187], a[60:63], v[140:143]\n"
    "v_mfma_f32_16x16x32_bf16 v[144:147], a[188:191], a[48:51], v[144:147]\n"
    "v_mfma_f32_16x16x32_bf16 v[148:151], a[188:191], a[52:55], v[148:151]\n"
    "v_mfma_f32_16x16x32_bf16 v[152:155], a[188:191], a[56:59], v[152:155]\n"
    "v_mfma_f32_16x16x32_bf16 v[156:159], a[188:191], a[60:63], v[156:159]\n"
    "buffer_load_dwordx4 v[176:179], v2, s[20:23], s68 offen offset:0 sc1\n"
    "buffer_load_dwordx4 v[180:183], v2, s[20:23], s68 offen offset:1024 sc1\n"
    "buffer_load_dwordx4 v[184:187], v2, s[20:23], s68 offen offset:2048 sc1\n"
    "buffer_load_dwordx4 v[188:191], v2, s[20:23], s68 offen offset:3072 sc1\n"
    "buffer_load_dword v60, v32, s[20:23], s68 offen sc1\n"
    "buffer_load_dwordx4 v[208:211], v2, s[20:23], s67 offen offset:0 sc1\n"
    "buffer_load_dwordx4 v[212:215], v2, s[20:23], s67 offen offset:1024 sc1\n"
    "buffer_load_dwordx4 v[216:219], v2, s[20:23], s67 offen offset:2048 sc1\n"
    "buffer_load_dwordx4 v[220:223], v2, s[20:23], s67 offen offset:3072 sc1\n"
    "buffer_load_dword v61, v32, s[20:23], s67 offen sc1\n"
    "buffer_load_dwordx4 v[240:243], v2, s[20:23], s69 offen offset:0 sc1\n"
    "buffer_load_dwordx4 v[244:247], v2, s[20:23], s69 offen offset:1024 sc1\n"
    "buffer_load_dwordx4 v[248:251], v2, s[20:23], s69 offen offset:2048 sc1\n"
    "buffer_load_dwordx4 v[252:255], v2, s[20:23], s69 offen offset:3072 sc1\n"
    "buffer_load_dword v62, v32, s[20:23], s69 offen sc1\n"
    "s_mov_b32 s29, 32768\n"
    "s_mov_b32 s66, 0xffff0000\n"
    "s_waitcnt vmcnt(30)\n"
    "s_waitcnt lgkmcnt(0)\n"
    "v_permlane16_swap_b32 v40, v42\n"
    "v_permlane16_swap_b32 v41, v43\n"
    "v_permlane16_swap_b32 v44, v46\n"
    "v_permlane16_swap_b32 v45, v47\n"
    "v_permlane16_swap_b32 v48, v50\n"
    "v_permlane16_swap_b32 v49, v51\n"
    "v_permlane16_swap_b32 v52, v54\n"
    "v_permlane16_swap_b32 v53, v55\n"
    "v_lshlrev_b32 v64, 16, v40\n"
    "v_and_b32 v65, s66, v40\n"
    "v_lshlrev_b32 v36, 16, v112\n"
    "v_and_b32 v37, s66, v112\n"
    "v_pk_mul_f32 v[64:65], v[64:65], v[56:57] op_sel:[0,0] op_sel_hi:[1,0]\n"
    "v_pk_mul_f32 v[64:65], v[64:65], v[36:37]\n"
    "v_lshlrev_b32 v66, 16, v41\n"
    "v_and_b32 v67, s66, v41\n"
    "v_lshlrev_b32 v36, 16, v113\n"
    "v_and_b32 v37, s66, v113\n"
    "v_pk_mul_f32 v[66:67], v[66:67], v[56:57] op_sel:[0,0] op_sel_hi:[1,0]\n"
    "v_pk_mul_f32 v[66:67], v[66:67], v[36:37]\n"
    "v_lshlrev_b32 v68, 16, v42\n"
    "v_and_b32 v69, s66, v42\n"
    "v_lshlrev_b32 v36, 16, v114\n"
    "v_and_b32 v37, s66, v114\n"
    "v_pk_mul_f32 v[68:69], v[68:69], v[56:57] op_sel:[0,0] op_sel_hi:[1,0]\n"
    "v_pk_mul_f32 v[68:69], v[68:69], v[36:37]\n"
    "v_lshlrev_b32 v70, 16, v43\n"
    "v_and_b32 v71, s66, v43\n"
    "v_lshlrev_b32 v36, 16, v115\n"
    "v_and_b32 v37, s66, v115\n"
    "v_pk_mul_f32 v[70:71], v[70:71], v[56:57] op_sel:[0,0] op_sel_hi:[1,0]\n"
    "v_pk_mul_f32 v[70:71], v[70:71], v[36:37]\n"
    "v_lshlrev_b32 v72, 16, v44\n"
    "v_and_b32 v73, s66, v44\n"
    "v_lshlrev_b32 v36, 16, v116\n"
    "v_and_b32 v37, s66, v116\n"
    "v_pk_mul_f32 v[72:73], v[72:73], v[56:57] op_sel:[0,1] op_sel_hi:[1,1]\n"
    "v_pk_mul_f32 v[72:73], v[72:73], v[36:37]\n"
    "v_lshlrev_b32 v74, 16, v45\n"
    "v_and_b32 v75, s66, v45\n"
    "v_lshlrev_b32 v36, 16, v117\n"
    "v_and_b32 v37, s66, v117\n"
    "v_pk_mul_f32 v[74:75], v[74:75], v[56:57] op_sel:[0,1] op_sel_hi:[1,1]\n"
    "v_pk_mul_f32 v[74:75], v[74:75], v[36:37]\n"
    "v_lshlrev_b32 v76, 16, v46\n"
    "v_and_b32 v77, s66, v46\n"
    "v_lshlrev_b32 v36, 16, v118\n"
    "v_and_b32 v37, s66, v118\n"
    "v_pk_mul_f32 v[76:77], v[76:77], v[56:57] op_sel:[0,1] op_sel_hi:[1,1]\n"
    "v_pk_mul_f32 v[76:77], v[76:77], v[36:37]\n"
    "v_lshlrev_b32 v78, 16, v47\n"
    "v_and_b32 v79, s66, v47\n"
    "v_lshlrev_b32 v36, 16, v119\n"
    "v_and_b32 v37, s66, v119\n"
    "v_pk_mul_f32 v[78:79], v[78:79], v[56:57] op_sel:[0,1] op_sel_hi:[1,1]\n"
    "v_pk_mul_f32 v[78:79], v[78:79], v[36:37]\n"
    "v_lshlrev_b32 v80, 16, v48\n"
    "v_and_b32 v81, s66, v48\n"
    "v_lshlrev_b32 v36, 16, v120\n"
    "v_and_b32 v37, s66, v120\n"
    "v_pk_mul_f32 v[80:81], v[80:81], v[58:59] op_sel:[0,0] op_sel_hi:[1,0]\n"
    "v_pk_mul_f32 v[80:81], v[80:81], v[36:37]\n"
    "v_lshlrev_b32 v82, 16, v49\n"
    "v_and_b32 v83, s66, v49\n"
    "v_lshlrev_b32 v36, 16, v121\n"
    "v_and_b32 v37, s66, v121\n"
    "v_pk_mul_f32 v[82:83], v[82:83], v[58:59] op_sel:[0,0] op_sel_hi:[1,0]\n"
    "v_pk_mul_f32 v[82:83], v[82:83], v[36:37]\n"
    "v_lshlrev_b32 v84, 16, v50\n"
    "v_and_b32 v85, s66, v50\n"
    "v_lshlrev_b32 v36, 16, v122\n"
    "v_and_b32 v37, s66, v122\n"
    "v_pk_mul_f32 v[84:85], v[84:85], v[58:59] op_sel:[0,0] op_sel_hi:[1,0]\n"
    "v_pk_mul_f32 v[84:85], v[84:85], v[36:37]\n"
    "v_lshlrev_b32 v86, 16, v51\n"
    "v_and_b32 v87, s66, v51\n"
    "v_lshlrev_b32 v36, 16, v123\n"
    "v_and_b32 v37, s66, v123\n"
    "v_pk_mul_f32 v[86:87], v[86:87], v[58:59] op_sel:[0,0] op_sel_hi:[1,0]\n"
    "v_pk_mul_f32 v[86:87], v[86:87], v[36:37]\n"
    "v_lshlrev_b32 v88, 16, v52\n"
    "v_and_b32 v89, s66, v52\n"
    "v_lshlrev_b32 v36, 16, v124\n"
    "v_and_b32 v37, s66, v124\n"
    "v_pk_mul_f32 v[88:89], v[88:89], v[58:59] op_sel:[0,1] op_sel_hi:[1,1]\n"
    "v_pk_mul_f32 v[88:89], v[88:89], v[36:37]\n"
    "v_lshlrev_b32 v90, 16, v53\n"
    "v_and_b32 v91, s66, v53\n"
    "v_lshlrev_b32 v36, 16, v125\n"
    "v_and_b32 v37, s66, v125\n"
    "v_pk_mul_f32 v[90:91], v[90:91], v[58:59] op_sel:[0,1] op_sel_hi:[1,1]\n"
    "v_pk_mul_f32 v[90:91], v[90:91], v[36:37]\n"
    "v_lshlrev_b32 v92, 16, v54\n"
    "v_and_b32 v93, s66, v54\n"
    "v_lshlrev_b32 v36, 16, v126\n"
    "v_and_b32 v37, s66, v126\n"
    "v_pk_mul_f32 v[92:93], v[92:93], v[58:59] op_sel:[0,1] op_sel_hi:[1,1]\n"
    "v_pk_mul_f32 v[92:93], v[92:93], v[36:37]\n"
    "v_lshlrev_b32 v94, 16, v55\n"
    "v_and_b32 v95, s66, v55\n"
    "v_lshlrev_b32 v36, 16, v127\n"
    "v_and_b32 v37, s66, v127\n"
    "v_pk_mul_f32 v[94:95], v[94:95], v[58:59] op_sel:[0,1] op_sel_hi:[1,1]\n"
    "v_pk_mul_f32 v[94:95], v[94:95], v[36:37]\n"
    "v_sub_f32 v56, 1.0, v56\n"
    "v_sub_f32 v57, 1.0, v57\n"
    "v_sub_f32 v58, 1.0, v58\n"
    "v_sub_f32 v59, 1.0, v59\n"
    "s_mov_b32 s66, 0x3f80\n"
    "s_mov_b32 s31, 0x3f800000\n"
    "s_waitcnt vmcnt(10)\n"
    "RCK1_gated_mix__ssm_conv_h3__swiglu_ffn__prompt_kernel:\n"
    "v_add3_u32 v36, v176, v177, s70\n"
    "v_add3_u32 v37, v178, v179, v180\n"
    "v_add3_u32 v36, v181, v182, v36\n"
    "v_add3_u32 v37, v183, v184, v37\n"
    "v_add3_u32 v36, v185, v186, v36\n"
    "v_add3_u32 v37, v187, v188, v37\n"
    "v_add3_u32 v36, v189, v190, v36\n"
    "v_add3_u32 v36, v191, v37, v36\n"
    "v_cmp_ne_u32 vcc, v36, v60\n"
    "s_cbranch_vccnz RSL1_gated_mix__ssm_conv_h3__swiglu_ffn__prompt_kernel\n"
    "ROK1_gated_mix__ssm_conv_h3__swiglu_ffn__prompt_kernel:\n"
    "v_dot2c_f32_bf16 v128, s66, v176\n"
    "v_dot2c_f32_bf16 v129, s31, v176\n"
    "v_dot2c_f32_bf16 v130, s66, v177\n"
    "v_dot2c_f32_bf16 v131, s31, v177\n"
    "v_dot2c_f32_bf16 v144, s66, v178\n"
    "v_dot2c_f32_bf16 v145, s31, v178\n"
    "v_dot2c_f32_bf16 v146, s66, v179\n"
    "v_dot2c_f32_bf16 v147, s31, v179\n"
    "v_dot2c_f32_bf16 v132, s66, v180\n"
    "v_dot2c_f32_bf16 v133, s31, v180\n"
    "v_dot2c_f32_bf16 v134, s66, v181\n"
    "v_dot2c_f32_bf16 v135, s31, v181\n"
    "v_dot2c_f32_bf16 v148, s66, v182\n"
    "v_dot2c_f32_bf16 v149, s31, v182\n"
    "v_dot2c_f32_bf16 v150, s66, v183\n"
    "v_dot2c_f32_bf16 v151, s31, v183\n"
    "v_dot2c_f32_bf16 v136, s66, v184\n"
    "v_dot2c_f32_bf16 v137, s31, v184\n"
    "v_dot2c_f32_bf16 v138, s66, v185\n"
    "v_dot2c_f32_bf16 v139, s31, v185\n"
    "v_dot2c_f32_bf16 v152, s66, v186\n"
    "v_dot2c_f32_bf16 v153, s31, v186\n"
    "v_dot2c_f32_bf16 v154, s66, v187\n"
    "v_dot2c_f32_bf16 v155, s31, v187\n"
    "v_dot2c_f32_bf16 v140, s66, v188\n"
    "v_dot2c_f32_bf16 v141, s31, v188\n"
    "v_dot2c_f32_bf16 v142, s66, v189\n"
    "v_dot2c_f32_bf16 v143, s31, v189\n"
    "v_dot2c_f32_bf16 v156, s66, v190\n"
    "v_dot2c_f32_bf16 v157, s31, v190\n"
    "v_dot2c_f32_bf16 v158, s66, v191\n"
    "v_dot2c_f32_bf16 v159, s31, v191\n"
    "s_waitcnt vmcnt(5)\n"
    "RCK2_gated_mix__ssm_conv_h3__swiglu_ffn__prompt_kernel:\n"
    "v_add3_u32 v36, v208, v209, s70\n"
    "v_add3_u32 v37, v210, v211, v212\n"
    "v_add3_u32 v36, v213, v214, v36\n"
    "v_add3_u32 v37, v215, v216, v37\n"
    "v_add3_u32 v36, v217, v218, v36\n"
    "v_add3_u32 v37, v219, v220, v37\n"
    "v_add3_u32 v36, v221, v222, v36\n"
    "v_add3_u32 v36, v223, v37, v36\n"
    "v_cmp_ne_u32 vcc, v36, v61\n"
    "s_cbranch_vccnz RSL2_gated_mix__ssm_conv_h3__swiglu_ffn__prompt_kernel\n"
    "ROK2_gated_mix__ssm_conv_h3__swiglu_ffn__prompt_kernel:\n"
    "v_dot2c_f32_bf16 v128, s66, v208\n"
    "v_dot2c_f32_bf16 v129, s31, v208\n"
    "v_dot2c_f32_bf16 v130, s66, v209\n"
    "v_dot2c_f32_bf16 v131, s31, v209\n"
    "v_dot2c_f32_bf16 v144, s66, v210\n"
    "v_dot2c_f32_bf16 v145, s31, v210\n"
    "v_dot2c_f32_bf16 v146, s66, v211\n"
    "v_dot2c_f32_bf16 v147, s31, v211\n"
    "v_dot2c_f32_bf16 v132, s66, v212\n"
    "v_dot2c_f32_bf16 v133, s31, v212\n"
    "v_dot2c_f32_bf16 v134, s66, v213\n"
    "v_dot2c_f32_bf16 v135, s31, v213\n"
    "v_dot2c_f32_bf16 v148, s66, v214\n"
    "v_dot2c_f32_bf16 v149, s31, v214\n"
    "v_dot2c_f32_bf16 v150, s66, v215\n"
    "v_dot2c_f32_bf16 v151, s31, v215\n"
    "v_dot2c_f32_bf16 v136, s66, v216\n"
    "v_dot2c_f32_bf16 v137, s31, v216\n"
    "v_dot2c_f32_bf16 v138, s66, v217\n"
    "v_dot2c_f32_bf16 v139, s31, v217\n"
    "v_dot2c_f32_bf16 v152, s66, v218\n"
    "v_dot2c_f32_bf16 v153, s31, v218\n"
    "v_dot2c_f32_bf16 v154, s66, v219\n"
    "v_dot2c_f32_bf16 v155, s31, v219\n"
    "v_dot2c_f32_bf16 v140, s66, v220\n"
    "v_dot2c_f32_bf16 v141, s31, v220\n"
    "v_dot2c_f32_bf16 v142, s66, v221\n"
    "v_dot2c_f32_bf16 v143, s31, v221\n"
    "v_dot2c_f32_bf16 v156, s66, v222\n"
    "v_dot2c_f32_bf16 v157, s31, v222\n"
    "v_dot2c_f32_bf16 v158, s66, v223\n"
    "v_dot2c_f32_bf16 v159, s31, v223\n"
    "s_waitcnt vmcnt(0)\n"
    "RCK3_gated_mix__ssm_conv_h3__swiglu_ffn__prompt_kernel:\n"
    "v_add3_u32 v36, v240, v241, s70\n"
    "v_add3_u32 v37, v242, v243, v244\n"
    "v_add3_u32 v36, v245, v246, v36\n"
    "v_add3_u32 v37, v247, v248, v37\n"
    "v_add3_u32 v36, v249, v250, v36\n"
    "v_add3_u32 v37, v251, v252, v37\n"
    "v_add3_u32 v36, v253, v254, v36\n"
    "v_add3_u32 v36, v255, v37, v36\n"
    "v_cmp_ne_u32 vcc, v36, v62\n"
    "s_cbranch_vccnz RSL3_gated_mix__ssm_conv_h3__swiglu_ffn__prompt_kernel\n"
    "ROK3_gated_mix__ssm_conv_h3__swiglu_ffn__prompt_kernel:\n"
    "v_dot2c_f32_bf16 v128, s66, v240\n"
    "v_dot2c_f32_bf16 v129, s31, v240\n"
    "v_dot2c_f32_bf16 v130, s66, v241\n"
    "v_dot2c_f32_bf16 v131, s31, v241\n"
    "v_dot2c_f32_bf16 v144, s66, v242\n"
    "v_dot2c_f32_bf16 v145, s31, v242\n"
    "v_dot2c_f32_bf16 v146, s66, v243\n"
    "v_dot2c_f32_bf16 v147, s31, v243\n"
    "s_nop 0\n"
    "v_pk_fma_f32 v[128:129], v[128:129], v[56:57], v[64:65] op_sel:[0,0,0] op_sel_hi:[1,0,1]\n"
    "v_pk_fma_f32 v[130:131], v[130:131], v[56:57], v[66:67] op_sel:[0,0,0] op_sel_hi:[1,0,1]\n"
    "s_nop 0\n"
    "v_pk_fma_f32 v[144:145], v[144:145], v[56:57], v[68:69] op_sel:[0,0,0] op_sel_hi:[1,0,1]\n"
    "v_pk_fma_f32 v[146:147], v[146:147], v[56:57], v[70:71] op_sel:[0,0,0] op_sel_hi:[1,0,1]\n"
    "v_cvt_pk_bf16_f32 v40, v128, v129\n"
    "v_cvt_pk_bf16_f32 v41, v130, v131\n"
    "v_cvt_pk_bf16_f32 v42, v144, v145\n"
    "v_cvt_pk_bf16_f32 v43, v146, v147\n"
    "s_nop 1\n"
    "v_permlane16_swap_b32 v40, v42\n"
    "v_permlane16_swap_b32 v41, v43\n"
    "buffer_store_dwordx4 v[40:43], v26, s[84:87], 0 offen\n"
    "v_dot2c_f32_bf16 v132, s66, v244\n"
    "v_dot2c_f32_bf16 v133, s31, v244\n"
    "v_dot2c_f32_bf16 v134, s66, v245\n"
    "v_dot2c_f32_bf16 v135, s31, v245\n"
    "v_dot2c_f32_bf16 v148, s66, v246\n"
    "v_dot2c_f32_bf16 v149, s31, v246\n"
    "v_dot2c_f32_bf16 v150, s66, v247\n"
    "v_dot2c_f32_bf16 v151, s31, v247\n"
    "s_nop 0\n"
    "v_pk_fma_f32 v[132:133], v[132:133], v[56:57], v[72:73] op_sel:[0,1,0] op_sel_hi:[1,1,1]\n"
    "v_pk_fma_f32 v[134:135], v[134:135], v[56:57], v[74:75] op_sel:[0,1,0] op_sel_hi:[1,1,1]\n"
    "s_nop 0\n"
    "v_pk_fma_f32 v[148:149], v[148:149], v[56:57], v[76:77] op_sel:[0,1,0] op_sel_hi:[1,1,1]\n"
    "v_pk_fma_f32 v[150:151], v[150:151], v[56:57], v[78:79] op_sel:[0,1,0] op_sel_hi:[1,1,1]\n"
    "v_cvt_pk_bf16_f32 v44, v132, v133\n"
    "v_cvt_pk_bf16_f32 v45, v134, v135\n"
    "v_cvt_pk_bf16_f32 v46, v148, v149\n"
    "v_cvt_pk_bf16_f32 v47, v150, v151\n"
    "s_nop 1\n"
    "v_permlane16_swap_b32 v44, v46\n"
    "v_permlane16_swap_b32 v45, v47\n"
    "buffer_store_dwordx4 v[44:47], v27, s[84:87], 0 offen\n"
    "v_dot2c_f32_bf16 v136, s66, v248\n"
    "v_dot2c_f32_bf16 v137, s31, v248\n"
    "v_dot2c_f32_bf16 v138, s66, v249\n"
    "v_dot2c_f32_bf16 v139, s31, v249\n"
    "v_dot2c_f32_bf16 v152, s66, v250\n"
    "v_dot2c_f32_bf16 v153, s31, v250\n"
    "v_dot2c_f32_bf16 v154, s66, v251\n"
    "v_dot2c_f32_bf16 v155, s31, v251\n"
    "s_nop 0\n"
    "v_pk_fma_f32 v[136:137], v[136:137], v[58:59], v[80:81] op_sel:[0,0,0] op_sel_hi:[1,0,1]\n"
    "v_pk_fma_f32 v[138:139], v[138:139], v[58:59], v[82:83] op_sel:[0,0,0] op_sel_hi:[1,0,1]\n"
    "s_nop 0\n"
    "v_pk_fma_f32 v[152:153], v[152:153], v[58:59], v[84:85] op_sel:[0,0,0] op_sel_hi:[1,0,1]\n"
    "v_pk_fma_f32 v[154:155], v[154:155], v[58:59], v[86:87] op_sel:[0,0,0] op_sel_hi:[1,0,1]\n"
    "v_cvt_pk_bf16_f32 v48, v136, v137\n"
    "v_cvt_pk_bf16_f32 v49, v138, v139\n"
    "v_cvt_pk_bf16_f32 v50, v152, v153\n"
    "v_cvt_pk_bf16_f32 v51, v154, v155\n"
    "s_nop 1\n"
    "v_permlane16_swap_b32 v48, v50\n"
    "v_permlane16_swap_b32 v49, v51\n"
    "buffer_store_dwordx4 v[48:51], v28, s[84:87], 0 offen\n"
    "v_dot2c_f32_bf16 v140, s66, v252\n"
    "v_dot2c_f32_bf16 v141, s31, v252\n"
    "v_dot2c_f32_bf16 v142, s66, v253\n"
    "v_dot2c_f32_bf16 v143, s31, v253\n"
    "v_dot2c_f32_bf16 v156, s66, v254\n"
    "v_dot2c_f32_bf16 v157, s31, v254\n"
    "v_dot2c_f32_bf16 v158, s66, v255\n"
    "v_dot2c_f32_bf16 v159, s31, v255\n"
    "s_nop 0\n"
    "v_pk_fma_f32 v[140:141], v[140:141], v[58:59], v[88:89] op_sel:[0,1,0] op_sel_hi:[1,1,1]\n"
    "v_pk_fma_f32 v[142:143], v[142:143], v[58:59], v[90:91] op_sel:[0,1,0] op_sel_hi:[1,1,1]\n"
    "s_nop 0\n"
    "v_pk_fma_f32 v[156:157], v[156:157], v[58:59], v[92:93] op_sel:[0,1,0] op_sel_hi:[1,1,1]\n"
    "v_pk_fma_f32 v[158:159], v[158:159], v[58:59], v[94:95] op_sel:[0,1,0] op_sel_hi:[1,1,1]\n"
    "v_cvt_pk_bf16_f32 v52, v140, v141\n"
    "v_cvt_pk_bf16_f32 v53, v142, v143\n"
    "v_cvt_pk_bf16_f32 v54, v156, v157\n"
    "v_cvt_pk_bf16_f32 v55, v158, v159\n"
    "s_nop 1\n"
    "v_permlane16_swap_b32 v52, v54\n"
    "v_permlane16_swap_b32 v53, v55\n"
    "buffer_store_dwordx4 v[52:55], v29, s[84:87], 0 offen\n"
    "s_waitcnt vmcnt(0)\n"
    "s_endpgm\n"
    "PHSA_gated_mix__ssm_conv_h3__swiglu_ffn__prompt_kernel:\n"
    "s_mov_b32 s29, 0x4000\n"
    "PHA_gated_mix__ssm_conv_h3__swiglu_ffn__prompt_kernel:\n"
    "buffer_load_dwordx2 v[84:85], v80, s[20:23], 0 offen sc1\n"
    "s_waitcnt vmcnt(0)\n"
    "v_cmp_ne_u64 vcc, s[70:71], v[84:85]\n"
    "s_cbranch_vccz PHAd_gated_mix__ssm_conv_h3__swiglu_ffn__prompt_kernel\n"
    "s_sub_u32 s29, s29, 1\n"
    "s_cmp_lg_u32 s29, 0\n"
    "s_cbranch_scc1 PHA_gated_mix__ssm_conv_h3__swiglu_ffn__prompt_kernel\n"
    "PHAd_gated_mix__ssm_conv_h3__swiglu_ffn__prompt_kernel:\n"
    "s_branch PHRA_gated_mix__ssm_conv_h3__swiglu_ffn__prompt_kernel\n"
    "RSL1_gated_mix__ssm_conv_h3__swiglu_ffn__prompt_kernel:\n"
    "s_sub_u32 s29, s29, 1\n"
    "s_cmp_lt_i32 s29, 1\n"
    "s_cbranch_scc1 ROK1_gated_mix__ssm_conv_h3__swiglu_ffn__prompt_kernel\n"
    "buffer_load_dwordx4 v[176:179], v2, s[20:23], s68 offen offset:0 sc1\n"
    "buffer_load_dwordx4 v[180:183], v2, s[20:23], s68 offen offset:1024 sc1\n"
    "buffer_load_dwordx4 v[184:187], v2, s[20:23], s68 offen offset:2048 sc1\n"
    "buffer_load_dwordx4 v[188:191], v2, s[20:23], s68 offen offset:3072 sc1\n"
    "buffer_load_dword v60, v32, s[20:23], s68 offen sc1\n"
    "s_waitcnt vmcnt(0)\n"
    "s_branch RCK1_gated_mix__ssm_conv_h3__swiglu_ffn__prompt_kernel\n"
    "RSL2_gated_mix__ssm_conv_h3__swiglu_ffn__prompt_kernel:\n"
    "s_sub_u32 s29, s29, 1\n"
    "s_cmp_lt_i32 s29, 1\n"
    "s_cbranch_scc1 ROK2_gated_mix__ssm_conv_h3__swiglu_ffn__prompt_kernel\n"
    "buffer_load_dwordx4 v[208:211], v2, s[20:23], s67 offen offset:0 sc1\n"
    "buffer_load_dwordx4 v[212:215], v2, s[20:23], s67 offen offset:1024 sc1\n"
    "buffer_load_dwordx4 v[216:219], v2, s[20:23], s67 offen offset:2048 sc1\n"
    "buffer_load_dwordx4 v[220:223], v2, s[20:23], s67 offen offset:3072 sc1\n"
    "buffer_load_dword v61, v32, s[20:23], s67 offen sc1\n"
    "s_waitcnt vmcnt(0)\n"
    "s_branch RCK2_gated_mix__ssm_conv_h3__swiglu_ffn__prompt_kernel\n"
    "RSL3_gated_mix__ssm_conv_h3__swiglu_ffn__prompt_kernel:\n"
    "s_sub_u32 s29, s29, 1\n"
    "s_cmp_lt_i32 s29, 1\n"
    "s_cbranch_scc1 ROK3_gated_mix__ssm_conv_h3__swiglu_ffn__prompt_kernel\n"
    "buffer_load_dwordx4 v[240:243], v2, s[20:23], s69 offen offset:0 sc1\n"
    "buffer_load_dwordx4 v[244:247], v2, s[20:23], s69 offen offset:1024 sc1\n"
    "buffer_load_dwordx4 v[248:251], v2, s[20:23], s69 offen offset:2048 sc1\n"
    "buffer_load_dwordx4 v[252:255], v2, s[20:23], s69 offen offset:3072 sc1\n"
    "buffer_load_dword v62, v32, s[20:23], s69 offen sc1\n"
    "s_waitcnt vmcnt(0)\n"
    "s_branch RCK3_gated_mix__ssm_conv_h3__swiglu_ffn__prompt_kernel\n"
    "SPARE_gated_mix__ssm_conv_h3__swiglu_ffn__prompt_kernel:\n"
    "s_load_dwordx8 s[12:19], %0, 128\n"
    "s_load_dwordx2 s[98:99], %0, 160\n"
    "s_load_dwordx4 s[20:23], %0, 48\n"
    "s_load_dwordx4 s[8:11], %0, 0\n"
    "s_load_dwordx16 s[76:91], %0, 64\n"
    "v_lshrrev_b32 v16, 6, %2\n"
    "v_and_b32 v1, 63, %2\n"
    "v_readfirstlane_b32 s34, v16\n"
    "s_mov_b32 s39, %1\n"
    "s_getpc_b64 s[92:93]\n"
    "PCB_gated_mix__ssm_conv_h3__swiglu_ffn__prompt_kernel:\n"
    "s_sub_u32 s92, s92, PCB_gated_mix__ssm_conv_h3__swiglu_ffn__prompt_kernel-KSTART_gated_mix__ssm_conv_h3__swiglu_ffn__prompt_kernel\n"
    "s_subb_u32 s93, s93, 0\n"
    "s_mov_b32 s94, KEND_gated_mix__ssm_conv_h3__swiglu_ffn__prompt_kernel-KSTART_gated_mix__ssm_conv_h3__swiglu_ffn__prompt_kernel\n"
    "s_mov_b32 s95, 0x20000\n"
    "s_sub_u32 s68, s39, 224\n"
    "v_lshrrev_b32 v40, 3, v1\n"
    "v_lshlrev_b32 v40, 11, v40\n"
    "v_and_b32 v41, 1, v1\n"
    "v_lshl_add_u32 v40, v41, 4, v40\n"
    "v_bfe_u32 v41, v1, 1, 2\n"
    "v_bfe_u32 v42, v1, 4, 1\n"
    "v_xor_b32 v41, v41, v42\n"
    "v_lshl_add_u32 v2, v41, 5, v40\n"
    "v_xor_b32 v41, 2, v41\n"
    "v_lshl_add_u32 v3, v41, 5, v40\n"
    "v_add_u32 v4, 30720, v2\n"
    "v_add_u32 v5, 46080, v3\n"
    "v_add_u32 v3, 15360, v3\n"
    "s_lshl_b32 s72, s34, 1\n"
    "s_lshl_b32 s25, s68, 17\n"
    "s_lshl_b32 s30, s72, 7\n"
    "s_add_u32 s25, s25, s30\n"
    "s_mul_i32 s27, s34, 34816\n"
    "v_and_b32 v10, 15, v1\n"
    "v_mul_u32_u24 v11, 4224, v10\n"
    "v_lshlrev_b32 v10, 3, v10\n"
    "s_lshl_b32 s30, s72, 9\n"
    "v_add_u32 v10, s30, v10\n"
    "v_lshrrev_b32 v41, 4, v1\n"
    "v_lshl_add_u32 v11, v41, 4, v11\n"
    "s_waitcnt lgkmcnt(0)\n"
    "global_load_dwordx2 v[12:13], v10, s[98:99] offset:0\n"
    "global_load_dwordx2 v[14:15], v10, s[98:99] offset:128\n"
    "global_load_dwordx2 v[16:17], v10, s[98:99] offset:256\n"
    "global_load_dwordx2 v[18:19], v10, s[98:99] offset:384\n"
    "s_sub_u32 s96, s25, 32768\n"
    "s_cmp_eq_u32 s68, 0\n"
    "s_cselect_b32 s96, 2146435072, s96\n"
    "s_add_u32 m0, s27, 7168\n"
    "s_add_u32 s97, s25, 65536\n"
    "buffer_load_dwordx4 v3, s[12:15], s96 offen offset:1024 lds\n"
    "s_add_u32 m0, s27, 9216\n"
    "s_nop 0\n"
    "buffer_load_dwordx4 v2, s[12:15], s25 offen offset:0 lds\n"
    "buffer_load_dwordx4 v3, s[12:15], s25 offen offset:1024 lds\n"
    "buffer_load_dwordx4 v4, s[12:15], s25 offen offset:2048 lds\n"
    "buffer_load_dwordx4 v5, s[12:15], s25 offen offset:3072 lds\n"
    "s_add_u32 m0, s27, 0\n"
    "s_nop 0\n"
    "buffer_load_dwordx4 v2, s[16:19], s25 offen offset:0 lds\n"
    "buffer_load_dwordx4 v3, s[16:19], s25 offen offset:1024 lds\n"
    "buffer_load_dwordx4 v4, s[16:19], s25 offen offset:2048 lds\n"
    "buffer_load_dwordx4 v5, s[16:19], s25 offen offset:3072 lds\n"
    "s_add_u32 m0, s27, 13312\n"
    "s_nop 0\n"
    "buffer_load_dwordx4 v2, s[12:15], s97 offen offset:0 lds\n"
    "buffer_load_dwordx4 v3, s[12:15], s97 offen offset:1024 lds\n"
    "buffer_load_dwordx4 v4, s[12:15], s97 offen offset:2048 lds\n"
    "buffer_load_dwordx4 v5, s[12:15], s97 offen offset:3072 lds\n"
    "s_add_u32 m0, s27, 4096\n"
    "s_nop 0\n"
    "buffer_load_dwordx4 v2, s[16:19], s97 offen offset:0 lds\n"
    "buffer_load_dwordx4 v3, s[16:19], s97 offen offset:1024 lds\n"
    "buffer_load_dwordx4 v4, s[16:19], s97 offen offset:2048 lds\n"
    "buffer_load_dwordx4 v5, s[16:19], s97 offen offset:3072 lds\n"
    "s_add_u32 s66, s20, 40960000\n"
    "s_addc_u32 s67, s21, 0\n"
    "s_load_dwordx2 s[70:71], s[66:67], 0\n"
    "v_lshrrev_b32 v40, 4, v1\n"
    "v_lshlrev_b32 v40, 3, v40\n"
    "v_bfe_u32 v41, v1, 2, 2\n"
    "v_add_lshl_u32 v40, v40, v41, 7\n"
    "v_and_b32 v42, 3, v1\n"
    "v_lshl_add_u32 v40, v42, 3, v40\n"
    "v_lshrrev_b32 v41, 1, v41\n"
    "v_bfe_u32 v42, v1, 4, 1\n"
    "v_lshl_or_b32 v41, v42, 1, v41\n"
    "v_add_u32 v40, s27, v40\n"
    "v_xor_b32 v42, 0, v41\n"
    "v_lshl_add_u32 v6, v42, 5, v40\n"
    "v_xor_b32 v42, 1, v41\n"
    "v_lshl_add_u32 v7, v42, 5, v40\n"
    "v_xor_b32 v42, 2, v41\n"
    "v_lshl_add_u32 v8, v42, 5, v40\n"
    "v_xor_b32 v42, 3, v41\n"
    "v_lshl_add_u32 v9, v42, 5, v40\n"
    "s_mul_i32 s24, s72, 270336\n"
    "s_lshl_b32 s30, s68, 7\n"
    "s_add_u32 s24, s24, s30\n"
    "s_add_u32 s24, s24, 18874368\n"
    "s_mul_i32 s30, s34, 2304\n"
    "s_add_u32 s30, s30, 139264\n"
    "s_mov_b32 s38, 2146435072\n"
    "v_and_b32 v84, 15, v1\n"
    "v_mul_u32_u24 v84, 144, v84\n"
    "v_lshrrev_b32 v85, 4, v1\n"
    "v_lshl_add_u32 v84, v85, 4, v84\n"
    "v_add_u32 v84, s30, v84\n"
    "v_lshrrev_b32 v86, 3, v1\n"
    "v_mul_u32_u24 v85, 144, v86\n"
    "v_and_b32 v87, 7, v1\n"
    "v_lshl_add_u32 v85, v87, 4, v85\n"
    "v_add_u32 v85, s30, v85\n"
    "v_mul_u32_u24 v86, 4224, v86\n"
    "v_lshl_add_u32 v86, v87, 4, v86\n"
    "v_add_u32 v87, 33792, v86\n"
    "s_mul_i32 s30, s34, 21248\n"
    "s_add_u32 s30, s30, 66560\n"
    "s_mul_i32 s67, s34, 16640\n"
    "s_add_u32 s69, s30, 16640\n"
    "v_lshlrev_b32 v160, 4, v1\n"
    "v_cmp_eq_u32 vcc, 0, v1\n"
    "v_subrev_u32 v161, 8, v160\n"
    "v_mov_b32 v72, 0x80000000\n"
    "v_cndmask_b32 v161, v161, v72, vcc\n"
    "v_and_b32 v72, 7, v1\n"
    "v_bfe_u32 v73, v1, 3, 2\n"
    "v_mul_u32_u24 v73, 4160, v73\n"
    "v_lshl_add_u32 v72, v72, 2, v73\n"
    "v_add_u32 v162, s30, v72\n"
    "v_add_u32 v163, s67, v72\n"
    "v_min_u32 v72, 59, v1\n"
    "v_lshlrev_b32 v72, 3, v72\n"
    "v_add_u32 v164, s69, v72\n"
    "v_add_u32 v165, s69, v160\n"
    "v_add_u32 v177, s30, v160\n"
    "s_mov_b32 s67, 0\n"
    "v_bfe_u32 v72, v1, 2, 2\n"
    "v_mul_u32_u24 v64, 4160, v72\n"
    "v_and_b32 v72, 3, v1\n"
    "v_lshl_add_u32 v64, v72, 3, v64\n"
    "v_lshrrev_b32 v72, 4, v1\n"
    "v_lshlrev_b32 v72, 4, v72\n"
    "v_sub_u32 v64, v64, v72\n"
    "v_add_u32 v64, s67, v64\n"
    "v_add_u32 v64, 56, v64\n"
    "v_and_b32 v65, 15, v1\n"
    "v_lshl_add_u32 v65, v65, 5, v72\n"
    "v_add_u32 v65, s69, v65\n"
    "v_mov_b32 v166, v64\n"
    "v_mov_b32 v167, v65\n"
    "v_lshrrev_b32 v72, 4, v1\n"
    "v_bfe_u32 v73, v1, 3, 1\n"
    "v_add_u32 v73, 2, v73\n"
    "v_cmp_eq_u32 vcc, v72, v73\n"
    "v_bfe_u32 v72, v1, 1, 2\n"
    "v_mov_b32 v73, 9\n"
    "v_cndmask_b32 v72, v73, v72, vcc\n"
    "v_and_b32 v73, 1, v1\n"
    "v_lshlrev_b32 v73, 4, v73\n"
    "v_mov_b32 v74, 0xffff\n"
    "v_lshlrev_b32 v73, v73, v74\n"
    "v_cmp_eq_u32 vcc, 0, v72\n"
    "s_nop 1\n"
    "v_cndmask_b32 v168, 0, v73, vcc\n"
    "v_cmp_eq_u32 vcc, 1, v72\n"
    "s_nop 1\n"
    "v_cndmask_b32 v169, 0, v73, vcc\n"
    "v_cmp_eq_u32 vcc, 2, v72\n"
    "s_nop 1\n"
    "v_cndmask_b32 v170, 0, v73, vcc\n"
    "v_cmp_eq_u32 vcc, 3, v72\n"
    "s_nop 1\n"
    "v_cndmask_b32 v171, 0, v73, vcc\n"
    "v_and_b32 v172, 15, v1\n"
    "v_lshlrev_b32 v172, 5, v172\n"
    "v_bfe_u32 v72, v1, 4, 1\n"
    "v_lshl_add_u32 v172, v72, 9, v172\n"
    "v_lshrrev_b32 v72, 5, v1\n"
    "v_lshl_add_u32 v172, v72, 4, v172\n"
    "v_and_b32 v176, 31, v1\n"
    "v_lshlrev_b32 v176, 7, v176\n"
    "v_add_u32 v173, 40964096, v176\n"
    "v_add_u32 v175, 41005056, v176\n"
    "v_mov_b32 v174, v176\n"
    "s_sub_u32 s72, s39, 224\n"
    "s_lshl_b32 s72, s72, 2\n"
    "s_add_u32 s72, s72, s34\n"
    "s_mul_i32 s72, s72, 4\n"
    "s_lshl_b32 s73, s72, 12\n"
    "s_lshl_b32 s30, s72, 1\n"
    "s_load_dwordx2 s[52:53], s[88:89], s30\n"
    "s_lshl_b32 s30, s39, 2\n"
    "s_add_u32 s30, s30, 1024\n"
    "s_load_dword s54, s[88:89], s30\n"
    "s_lshr_b32 s30, s34, 1\n"
    "s_lshl_b32 s74, s39, 1\n"
    "s_add_u32 s74, s74, s30\n"
    "s_add_u32 s74, s74, 512\n"
    "s_lshl_b32 s74, s74, 12\n"
    "s_mov_b32 s26, 4\n"
    "s_mov_b32 s66, 0xffff0000\n"
    "s_mov_b32 s31, 17408\n"
    "s_waitcnt vmcnt(0) lgkmcnt(0)\n"
    "s_add_u32 s70, s70, 1\n"
    "PPL_gated_mix__ssm_conv_h3__swiglu_ffn__prompt_kernel:\n"
    "ds_read_b64_tr_b16 v[40:41], v8 offset:8704\n"
    "ds_read_b64_tr_b16 v[42:43], v6 offset:9216\n"
    "ds_read_b64_tr_b16 v[44:45], v6 offset:9728\n"
    "ds_read_b64_tr_b16 v[46:47], v6 offset:0\n"
    "ds_read_b64_tr_b16 v[48:49], v6 offset:512\n"
    "v_alignbit_b32 v20, v12, v12, 16\n"
    "v_alignbit_b32 v21, v13, v13, 16\n"
    "v_and_b32 v22, 0xffff, v12\n"
    "v_bfi_b32 v23, s66, v12, v13\n"
    "v_and_b32 v24, s66, v13\n"
    "v_alignbit_b32 v25, v14, v14, 16\n"
    "v_alignbit_b32 v26, v15, v15, 16\n"
    "v_and_b32 v27, 0xffff, v14\n"
    "v_bfi_b32 v28, s66, v14, v15\n"
    "v_and_b32 v29, s66, v15\n"
    "v_alignbit_b32 v30, v16, v16, 16\n"
    "v_alignbit_b32 v31, v17, v17, 16\n"
    "v_and_b32 v32, 0xffff, v16\n"
    "v_bfi_b32 v33, s66, v16, v17\n"
    "v_and_b32 v34, s66, v17\n"
    "v_alignbit_b32 v35, v18, v18, 16\n"
    "v_alignbit_b32 v36, v19, v19, 16\n"
    "v_and_b32 v37, 0xffff, v18\n"
    "v_bfi_b32 v38, s66, v18, v19\n"
    "v_and_b32 v39, s66, v19\n"
    "s_add_u32 s27, s27, s31\n"
    "s_cmp_eq_u32 s26, 3\n"
    "s_cselect_b32 s30, 7, 1\n"
    "s_cselect_b32 s75, 1622016, 0\n"
    "s_lshl_b32 s67, s30, 9\n"
    "v_add_u32 v10, s67, v10\n"
    "s_lshl_b32 s30, s30, 7\n"
    "s_add_u32 s25, s25, s30\n"
    "ds_read_b64_tr_b16 v[50:51], v8 offset:12800\n"
    "ds_read_b64_tr_b16 v[52:53], v6 offset:13312\n"
    "ds_read_b64_tr_b16 v[54:55], v6 offset:13824\n"
    "ds_read_b64_tr_b16 v[56:57], v6 offset:4096\n"
    "ds_read_b64_tr_b16 v[58:59], v6 offset:4608\n"
    "s_waitcnt lgkmcnt(5)\n"
    "v_dot2_f32_bf16 v60, v42, v22, 0\n"
    "v_dot2_f32_bf16 v61, v42, v20, 0\n"
    "v_dot2_f32_bf16 v62, v43, v22, 0\n"
    "buffer_store_dwordx4 v[208:211], v86, s[20:23], s38 offen sc1\n"
    "v_dot2_f32_bf16 v63, v43, v20, 0\n"
    "v_dot2_f32_bf16 v64, v44, v22, 0\n"
    "v_dot2_f32_bf16 v65, v44, v20, 0\n"
    "v_dot2_f32_bf16 v66, v45, v22, 0\n"
    "v_dot2_f32_bf16 v67, v45, v20, 0\n"
    "buffer_store_dwordx4 v[212:215], v87, s[20:23], s38 offen sc1\n"
    "v_dot2c_f32_bf16 v60, v41, v23\n"
    "v_dot2c_f32_bf16 v61, v41, v21\n"
    "v_dot2c_f32_bf16 v62, v42, v23\n"
    "v_dot2c_f32_bf16 v63, v42, v21\n"
    "v_dot2c_f32_bf16 v64, v43, v23\n"
    "s_add_u32 s38, s38, 67584\n"
    "v_dot2c_f32_bf16 v65, v43, v21\n"
    "v_dot2c_f32_bf16 v66, v44, v23\n"
    "v_dot2c_f32_bf16 v67, v44, v21\n"
    "v_dot2c_f32_bf16 v60, v40, v24\n"
    "v_dot2c_f32_bf16 v62, v41, v24\n"
    "buffer_store_dwordx4 v[216:219], v86, s[20:23], s38 offen sc1\n"
    "v_dot2c_f32_bf16 v64, v42, v24\n"
    "v_dot2c_f32_bf16 v66, v43, v24\n"
    "v_lshlrev_b32 v68, 16, v46\n"
    "v_and_b32 v69, s66, v46\n"
    "v_lshlrev_b32 v70, 16, v47\n"
    "buffer_store_dwordx4 v[220:223], v87, s[20:23], s38 offen sc1\n"
    "v_and_b32 v71, s66, v47\n"
    "v_lshlrev_b32 v72, 16, v48\n"
    "v_and_b32 v73, s66, v48\n"
    "v_lshlrev_b32 v74, 16, v49\n"
    "v_and_b32 v75, s66, v49\n"
    "s_add_u32 s38, s38, 67584\n"
    "v_pk_mul_f32 v[60:61], v[60:61], v[68:69]\n"
    "v_pk_mul_f32 v[62:63], v[62:63], v[70:71]\n"
    "v_pk_mul_f32 v[64:65], v[64:65], v[72:73]\n"
    "v_pk_mul_f32 v[66:67], v[66:67], v[74:75]\n"
    "v_cvt_pk_bf16_f32 v76, v60, v61\n"
    "v_cvt_pk_bf16_f32 v77, v62, v63\n"
    "v_cvt_pk_bf16_f32 v78, v64, v65\n"
    "v_cvt_pk_bf16_f32 v79, v66, v67\n"
    "ds_write_b128 v84, v[76:79] offset:0\n"
    "ds_read_b64_tr_b16 v[40:41], v9 offset:8704\n"
    "ds_read_b64_tr_b16 v[42:43], v7 offset:9216\n"
    "ds_read_b64_tr_b16 v[44:45], v7 offset:9728\n"
    "ds_read_b64_tr_b16 v[46:47], v7 offset:0\n"
    "ds_read_b64_tr_b16 v[48:49], v7 offset:512\n"
    "s_waitcnt lgkmcnt(5)\n"
    "v_dot2_f32_bf16 v60, v52, v22, 0\n"
    "v_dot2_f32_bf16 v61, v52, v20, 0\n"
    "v_dot2_f32_bf16 v62, v53, v22, 0\n"
    "buffer_store_dwordx4 v[224:227], v86, s[20:23], s38 offen sc1\n"
    "v_dot2_f32_bf16 v63, v53, v20, 0\n"
    "v_dot2_f32_bf16 v64, v54, v22, 0\n"
    "v_dot2_f32_bf16 v65, v54, v20, 0\n"
    "v_dot2_f32_bf16 v66, v55, v22, 0\n"
    "v_dot2_f32_bf16 v67, v55, v20, 0\n"
    "buffer_store_dwordx4 v[228:231], v87, s[20:23], s38 offen sc1\n"
    "v_dot2c_f32_bf16 v60, v51, v23\n"
    "v_dot2c_f32_bf16 v61, v51, v21\n"
    "v_dot2c_f32_bf16 v62, v52, v23\n"
    "v_dot2c_f32_bf16 v63, v52, v21\n"
    "v_dot2c_f32_bf16 v64, v53, v23\n"
    "s_add_u32 s38, s38, 67584\n"
    "v_dot2c_f32_bf16 v65, v53, v21\n"
    "v_dot2c_f32_bf16 v66, v54, v23\n"
    "v_dot2c_f32_bf16 v67, v54, v21\n"
    "v_dot2c_f32_bf16 v60, v50, v24\n"
    "v_dot2c_f32_bf16 v62, v51, v24\n"
    "buffer_store_dwordx4 v[232:235], v86, s[20:23], s38 offen sc1\n"
    "v_dot2c_f32_bf16 v64, v52, v24\n"
    "v_dot2c_f32_bf16 v66, v53, v24\n"
    "v_lshlrev_b32 v68, 16, v56\n"
    "v_and_b32 v69, s66, v56\n"
    "v_lshlrev_b32 v70, 16, v57\n"
    "buffer_store_dwordx4 v[236:239], v87, s[20:23], s38 offen sc1\n"
    "v_and_b32 v71, s66, v57\n"
    "v_lshlrev_b32 v72, 16, v58\n"
    "v_and_b32 v73, s66, v58\n"
    "v_lshlrev_b32 v74, 16, v59\n"
    "v_and_b32 v75, s66, v59\n"
    "s_add_u32 s38, s38, 67584\n"
    "v_pk_mul_f32 v[60:61], v[60:61], v[68:69]\n"
    "v_pk_mul_f32 v[62:63], v[62:63], v[70:71]\n"
    "v_pk_mul_f32 v[64:65], v[64:65], v[72:73]\n"
    "v_pk_mul_f32 v[66:67], v[66:67], v[74:75]\n"
    "v_cvt_pk_bf16_f32 v80, v60, v61\n"
    "v_cvt_pk_bf16_f32 v81, v62, v63\n"
    "v_cvt_pk_bf16_f32 v82, v64, v65\n"
    "v_cvt_pk_bf16_f32 v83, v66, v67\n"
    "ds_write_b128 v84, v[80:83] offset:64\n"
    "ds_read_b128 v[208:211], v85\n"
    "ds_read_b128 v[212:215], v85 offset:1152\n"
    "ds_read_b64_tr_b16 v[50:51], v9 offset:12800\n"
    "ds_read_b64_tr_b16 v[52:53], v7 offset:13312\n"
    "ds_read_b64_tr_b16 v[54:55], v7 offset:13824\n"
    "ds_read_b64_tr_b16 v[56:57], v7 offset:4096\n"
    "ds_read_b64_tr_b16 v[58:59], v7 offset:4608\n"
    "s_waitcnt lgkmcnt(5)\n"
    "v_dot2_f32_bf16 v60, v42, v27, 0\n"
    "v_dot2_f32_bf16 v61, v42, v25, 0\n"
    "v_dot2_f32_bf16 v62, v43, v27, 0\n"
    "global_load_dwordx2 v[12:13], v10, s[98:99] offset:0\n"
    "v_dot2_f32_bf16 v63, v43, v25, 0\n"
    "v_dot2_f32_bf16 v64, v44, v27, 0\n"
    "v_dot2_f32_bf16 v65, v44, v25, 0\n"
    "v_dot2_f32_bf16 v66, v45, v27, 0\n"
    "v_dot2_f32_bf16 v67, v45, v25, 0\n"
    "global_load_dwordx2 v[14:15], v10, s[98:99] offset:128\n"
    "v_dot2c_f32_bf16 v60, v41, v28\n"
    "v_dot2c_f32_bf16 v61, v41, v26\n"
    "v_dot2c_f32_bf16 v62, v42, v28\n"
    "v_dot2c_f32_bf16 v63, v42, v26\n"
    "v_dot2c_f32_bf16 v64, v43, v28\n"
    "global_load_dwordx2 v[16:17], v10, s[98:99] offset:256\n"
    "v_dot2c_f32_bf16 v65, v43, v26\n"
    "v_dot2c_f32_bf16 v66, v44, v28\n"
    "v_dot2c_f32_bf16 v67, v44, v26\n"
    "v_dot2c_f32_bf16 v60, v40, v29\n"
    "v_dot2c_f32_bf16 v62, v41, v29\n"
    "global_load_dwordx2 v[18:19], v10, s[98:99] offset:384\n"
    "v_dot2c_f32_bf16 v64, v42, v29\n"
    "v_dot2c_f32_bf16 v66, v43, v29\n"
    "v_lshlrev_b32 v68, 16, v46\n"
    "v_and_b32 v69, s66, v46\n"
    "v_lshlrev_b32 v70, 16, v47\n"
    "s_sub_u32 s96, s25, 32768\n"
    "s_cmp_eq_u32 s68, 0\n"
    "s_cselect_b32 s96, 2146435072, s96\n"
    "v_and_b32 v71, s66, v47\n"
    "v_lshlrev_b32 v72, 16, v48\n"
    "v_and_b32 v73, s66, v48\n"
    "v_lshlrev_b32 v74, 16, v49\n"
    "v_and_b32 v75, s66, v49\n"
    "s_add_u32 m0, s27, 7168\n"
    "v_pk_mul_f32 v[60:61], v[60:61], v[68:69]\n"
    "v_pk_mul_f32 v[62:63], v[62:63], v[70:71]\n"
    "v_pk_mul_f32 v[64:65], v[64:65], v[72:73]\n"
    "v_pk_mul_f32 v[66:67], v[66:67], v[74:75]\n"
    "v_cvt_pk_bf16_f32 v76, v60, v61\n"
    "s_add_u32 s97, s25, 65536\n"
    "v_cvt_pk_bf16_f32 v77, v62, v63\n"
    "v_cvt_pk_bf16_f32 v78, v64, v65\n"
    "v_cvt_pk_bf16_f32 v79, v66, v67\n"
    "ds_write_b128 v84, v[76:79] offset:0\n"
    "ds_read_b64_tr_b16 v[40:41], v6 offset:8704\n"
    "ds_read_b64_tr_b16 v[42:43], v8 offset:9216\n"
    "ds_read_b64_tr_b16 v[44:45], v8 offset:9728\n"
    "ds_read_b64_tr_b16 v[46:47], v8 offset:0\n"
    "ds_read_b64_tr_b16 v[48:49], v8 offset:512\n"
    "s_waitcnt lgkmcnt(5)\n"
    "v_dot2_f32_bf16 v60, v52, v27, 0\n"
    "v_dot2_f32_bf16 v61, v52, v25, 0\n"
    "v_dot2_f32_bf16 v62, v53, v27, 0\n"
    "buffer_load_dwordx4 v3, s[12:15], s96 offen offset:1024 lds\n"
    "v_dot2_f32_bf16 v63, v53, v25, 0\n"
    "v_dot2_f32_bf16 v64, v54, v27, 0\n"
    "v_dot2_f32_bf16 v65, v54, v25, 0\n"
    "v_dot2_f32_bf16 v66, v55, v27, 0\n"
    "v_dot2_f32_bf16 v67, v55, v25, 0\n"
    "s_add_u32 m0, s27, 9216\n"
    "s_nop 0\n"
    "v_dot2c_f32_bf16 v60, v51, v28\n"
    "v_dot2c_f32_bf16 v61, v51, v26\n"
    "v_dot2c_f32_bf16 v62, v52, v28\n"
    "v_dot2c_f32_bf16 v63, v52, v26\n"
    "v_dot2c_f32_bf16 v64, v53, v28\n"
    "buffer_load_dwordx4 v2, s[12:15], s25 offen offset:0 lds\n"
    "v_dot2c_f32_bf16 v65, v53, v26\n"
    "v_dot2c_f32_bf16 v66, v54, v28\n"
    "v_dot2c_f32_bf16 v67, v54, v26\n"
    "v_dot2c_f32_bf16 v60, v50, v29\n"
    "v_dot2c_f32_bf16 v62, v51, v29\n"
    "buffer_load_dwordx4 v3, s[12:15], s25 offen offset:1024 lds\n"
    "v_dot2c_f32_bf16 v64, v52, v29\n"
    "v_dot2c_f32_bf16 v66, v53, v29\n"
    "v_lshlrev_b32 v68, 16, v56\n"
    "v_and_b32 v69, s66, v56\n"
    "v_lshlrev_b32 v70, 16, v57\n"
    "buffer_load_dwordx4 v4, s[12:15], s25 offen offset:2048 lds\n"
    "v_and_b32 v71, s66, v57\n"
    "v_lshlrev_b32 v72, 16, v58\n"
    "v_and_b32 v73, s66, v58\n"
    "v_lshlrev_b32 v74, 16, v59\n"
    "v_and_b32 v75, s66, v59\n"
    "buffer_load_dwordx4 v5, s[12:15], s25 offen offset:3072 lds\n"
    "v_pk_mul_f32 v[60:61], v[60:61], v[68:69]\n"
    "v_pk_mul_f32 v[62:63], v[62:63], v[70:71]\n"
    "v_pk_mul_f32 v[64:65], v[64:65], v[72:73]\n"
    "v_pk_mul_f32 v[66:67], v[66:67], v[74:75]\n"
    "v_cvt_pk_bf16_f32 v80, v60, v61\n"
    "s_add_u32 m0, s27, 0\n"
    "s_nop 0\n"
    "v_cvt_pk_bf16_f32 v81, v62, v63\n"
    "v_cvt_pk_bf16_f32 v82, v64, v65\n"
    "v_cvt_pk_bf16_f32 v83, v66, v67\n"
    "ds_write_b128 v84, v[80:83] offset:64\n"
    "ds_read_b128 v[216:219], v85\n"
    "ds_read_b128 v[220:223], v85 offset:1152\n"
    "ds_read_b64_tr_b16 v[50:51], v6 offset:12800\n"
    "ds_read_b64_tr_b16 v[52:53], v8 offset:13312\n"
    "ds_read_b64_tr_b16 v[54:55], v8 offset:13824\n"
    "ds_read_b64_tr_b16 v[56:57], v8 offset:4096\n"
    "ds_read_b64_tr_b16 v[58:59], v8 offset:4608\n"
    "s_waitcnt lgkmcnt(5)\n"
    "v_dot2_f32_bf16 v60, v42, v32, 0\n"
    "v_dot2_f32_bf16 v61, v42, v30, 0\n"
    "v_dot2_f32_bf16 v62, v43, v32, 0\n"
    "buffer_load_dwordx4 v2, s[16:19], s25 offen offset:0 lds\n"
    "v_dot2_f32_bf16 v63, v43, v30, 0\n"
    "v_dot2_f32_bf16 v64, v44, v32, 0\n"
    "v_dot2_f32_bf16 v65, v44, v30, 0\n"
    "v_dot2_f32_bf16 v66, v45, v32, 0\n"
    "v_dot2_f32_bf16 v67, v45, v30, 0\n"
    "buffer_load_dwordx4 v3, s[16:19], s25 offen offset:1024 lds\n"
    "v_dot2c_f32_bf16 v60, v41, v33\n"
    "v_dot2c_f32_bf16 v61, v41, v31\n"
    "v_dot2c_f32_bf16 v62, v42, v33\n"
    "v_dot2c_f32_bf16 v63, v42, v31\n"
    "v_dot2c_f32_bf16 v64, v43, v33\n"
    "buffer_load_dwordx4 v4, s[16:19], s25 offen offset:2048 lds\n"
    "v_dot2c_f32_bf16 v65, v43, v31\n"
    "v_dot2c_f32_bf16 v66, v44, v33\n"
    "v_dot2c_f32_bf16 v67, v44, v31\n"
    "v_dot2c_f32_bf16 v60, v40, v34\n"
    "v_dot2c_f32_bf16 v62, v41, v34\n"
    "buffer_load_dwordx4 v5, s[16:19], s25 offen offset:3072 lds\n"
    "v_dot2c_f32_bf16 v64, v42, v34\n"
    "v_dot2c_f32_bf16 v66, v43, v34\n"
    "v_lshlrev_b32 v68, 16, v46\n"
    "v_and_b32 v69, s66, v46\n"
    "v_lshlrev_b32 v70, 16, v47\n"
    "s_add_u32 m0, s27, 13312\n"
    "s_nop 0\n"
    "v_and_b32 v71, s66, v47\n"
    "v_lshlrev_b32 v72, 16, v48\n"
    "v_and_b32 v73, s66, v48\n"
    "v_lshlrev_b32 v74, 16, v49\n"
    "v_and_b32 v75, s66, v49\n"
    "buffer_load_dwordx4 v2, s[12:15], s97 offen offset:0 lds\n"
    "v_pk_mul_f32 v[60:61], v[60:61], v[68:69]\n"
    "v_pk_mul_f32 v[62:63], v[62:63], v[70:71]\n"
    "v_pk_mul_f32 v[64:65], v[64:65], v[72:73]\n"
    "v_pk_mul_f32 v[66:67], v[66:67], v[74:75]\n"
    "v_cvt_pk_bf16_f32 v76, v60, v61\n"
    "buffer_load_dwordx4 v3, s[12:15], s97 offen offset:1024 lds\n"
    "v_cvt_pk_bf16_f32 v77, v62, v63\n"
    "v_cvt_pk_bf16_f32 v78, v64, v65\n"
    "v_cvt_pk_bf16_f32 v79, v66, v67\n"
    "ds_write_b128 v84, v[76:79] offset:0\n"
    "ds_read_b64_tr_b16 v[40:41], v7 offset:8704\n"
    "ds_read_b64_tr_b16 v[42:43], v9 offset:9216\n"
    "ds_read_b64_tr_b16 v[44:45], v9 offset:9728\n"
    "ds_read_b64_tr_b16 v[46:47], v9 offset:0\n"
    "ds_read_b64_tr_b16 v[48:49], v9 offset:512\n"
    "s_waitcnt lgkmcnt(5)\n"
    "v_dot2_f32_bf16 v60, v52, v32, 0\n"
    "v_dot2_f32_bf16 v61, v52, v30, 0\n"
    "v_dot2_f32_bf16 v62, v53, v32, 0\n"
    "buffer_load_dwordx4 v4, s[12:15], s97 offen offset:2048 lds\n"
    "v_dot2_f32_bf16 v63, v53, v30, 0\n"
    "v_dot2_f32_bf16 v64, v54, v32, 0\n"
    "v_dot2_f32_bf16 v65, v54, v30, 0\n"
    "v_dot2_f32_bf16 v66, v55, v32, 0\n"
    "v_dot2_f32_bf16 v67, v55, v30, 0\n"
    "buffer_load_dwordx4 v5, s[12:15], s97 offen offset:3072 lds\n"
    "v_dot2c_f32_bf16 v60, v51, v33\n"
    "v_dot2c_f32_bf16 v61, v51, v31\n"
    "v_dot2c_f32_bf16 v62, v52, v33\n"
    "v_dot2c_f32_bf16 v63, v52, v31\n"
    "v_dot2c_f32_bf16 v64, v53, v33\n"
    "s_add_u32 m0, s27, 4096\n"
    "s_nop 0\n"
    "v_dot2c_f32_bf16 v65, v53, v31\n"
    "v_dot2c_f32_bf16 v66, v54, v33\n"
    "v_dot2c_f32_bf16 v67, v54, v31\n"
    "v_dot2c_f32_bf16 v60, v50, v34\n"
    "v_dot2c_f32_bf16 v62, v51, v34\n"
    "buffer_load_dwordx4 v2, s[16:19], s97 offen offset:0 lds\n"
    "v_dot2c_f32_bf16 v64, v52, v34\n"
    "v_dot2c_f32_bf16 v66, v53, v34\n"
    "v_lshlrev_b32 v68, 16, v56\n"
    "v_and_b32 v69, s66, v56\n"
    "v_lshlrev_b32 v70, 16, v57\n"
    "buffer_load_dwordx4 v3, s[16:19], s97 offen offset:1024 lds\n"
    "v_and_b32 v71, s66, v57\n"
    "v_lshlrev_b32 v72, 16, v58\n"
    "v_and_b32 v73, s66, v58\n"
    "v_lshlrev_b32 v74, 16, v59\n"
    "v_and_b32 v75, s66, v59\n"
    "buffer_load_dwordx4 v4, s[16:19], s97 offen offset:2048 lds\n"
    "v_pk_mul_f32 v[60:61], v[60:61], v[68:69]\n"
    "v_pk_mul_f32 v[62:63], v[62:63], v[70:71]\n"
    "v_pk_mul_f32 v[64:65], v[64:65], v[72:73]\n"
    "v_pk_mul_f32 v[66:67], v[66:67], v[74:75]\n"
    "v_cvt_pk_bf16_f32 v80, v60, v61\n"
    "buffer_load_dwordx4 v5, s[16:19], s97 offen offset:3072 lds\n"
    "v_cvt_pk_bf16_f32 v81, v62, v63\n"
    "v_cvt_pk_bf16_f32 v82, v64, v65\n"
    "v_cvt_pk_bf16_f32 v83, v66, v67\n"
    "ds_write_b128 v84, v[80:83] offset:64\n"
    "ds_read_b128 v[224:227], v85\n"
    "ds_read_b128 v[228:231], v85 offset:1152\n"
    "ds_read_b64_tr_b16 v[50:51], v7 offset:12800\n"
    "ds_read_b64_tr_b16 v[52:53], v9 offset:13312\n"
    "ds_read_b64_tr_b16 v[54:55], v9 offset:13824\n"
    "ds_read_b64_tr_b16 v[56:57], v9 offset:4096\n"
    "ds_read_b64_tr_b16 v[58:59], v9 offset:4608\n"
    "s_waitcnt lgkmcnt(5)\n"
    "v_dot2_f32_bf16 v60, v42, v37, 0\n"
    "v_dot2_f32_bf16 v61, v42, v35, 0\n"
    "v_dot2_f32_bf16 v62, v43, v37, 0\n"
    "v_dot2_f32_bf16 v63, v43, v35, 0\n"
    "v_dot2_f32_bf16 v64, v44, v37, 0\n"
    "v_dot2_f32_bf16 v65, v44, v35, 0\n"
    "v_dot2_f32_bf16 v66, v45, v37, 0\n"
    "v_dot2_f32_bf16 v67, v45, v35, 0\n"
    "v_dot2c_f32_bf16 v60, v41, v38\n"
    "v_dot2c_f32_bf16 v61, v41, v36\n"
    "v_dot2c_f32_bf16 v62, v42, v38\n"
    "v_dot2c_f32_bf16 v63, v42, v36\n"
    "v_dot2c_f32_bf16 v64, v43, v38\n"
    "v_dot2c_f32_bf16 v65, v43, v36\n"
    "v_dot2c_f32_bf16 v66, v44, v38\n"
    "v_dot2c_f32_bf16 v67, v44, v36\n"
    "v_dot2c_f32_bf16 v60, v40, v39\n"
    "v_dot2c_f32_bf16 v62, v41, v39\n"
    "v_dot2c_f32_bf16 v64, v42, v39\n"
    "v_dot2c_f32_bf16 v66, v43, v39\n"
    "v_lshlrev_b32 v68, 16, v46\n"
    "v_and_b32 v69, s66, v46\n"
    "v_lshlrev_b32 v70, 16, v47\n"
    "v_and_b32 v71, s66, v47\n"
    "v_lshlrev_b32 v72, 16, v48\n"
    "v_and_b32 v73, s66, v48\n"
    "v_lshlrev_b32 v74, 16, v49\n"
    "v_and_b32 v75, s66, v49\n"
    "v_pk_mul_f32 v[60:61], v[60:61], v[68:69]\n"
    "v_pk_mul_f32 v[62:63], v[62:63], v[70:71]\n"
    "v_pk_mul_f32 v[64:65], v[64:65], v[72:73]\n"
    "v_pk_mul_f32 v[66:67], v[66:67], v[74:75]\n"
    "v_cvt_pk_bf16_f32 v76, v60, v61\n"
    "v_cvt_pk_bf16_f32 v77, v62, v63\n"
    "v_cvt_pk_bf16_f32 v78, v64, v65\n"
    "v_cvt_pk_bf16_f32 v79, v66, v67\n"
    "ds_write_b128 v84, v[76:79] offset:0\n"
    "s_waitcnt lgkmcnt(0)\n"
    "v_dot2_f32_bf16 v60, v52, v37, 0\n"
    "v_dot2_f32_bf16 v61, v52, v35, 0\n"
    "v_dot2_f32_bf16 v62, v53, v37, 0\n"
    "v_dot2_f32_bf16 v63, v53, v35, 0\n"
    "v_dot2_f32_bf16 v64, v54, v37, 0\n"
    "v_dot2_f32_bf16 v65, v54, v35, 0\n"
    "v_dot2_f32_bf16 v66, v55, v37, 0\n"
    "v_dot2_f32_bf16 v67, v55, v35, 0\n"
    "v_dot2c_f32_bf16 v60, v51, v38\n"
    "v_dot2c_f32_bf16 v61, v51, v36\n"
    "v_dot2c_f32_bf16 v62, v52, v38\n"
    "v_dot2c_f32_bf16 v63, v52, v36\n"
    "v_dot2c_f32_bf16 v64, v53, v38\n"
    "v_dot2c_f32_bf16 v65, v53, v36\n"
    "v_dot2c_f32_bf16 v66, v54, v38\n"
    "v_dot2c_f32_bf16 v67, v54, v36\n"
    "v_dot2c_f32_bf16 v60, v50, v39\n"
    "v_dot2c_f32_bf16 v62, v51, v39\n"
    "v_dot2c_f32_bf16 v64, v52, v39\n"
    "v_dot2c_f32_bf16 v66, v53, v39\n"
    "v_lshlrev_b32 v68, 16, v56\n"
    "v_and_b32 v69, s66, v56\n"
    "v_lshlrev_b32 v70, 16, v57\n"
    "v_and_b32 v71, s66, v57\n"
    "v_lshlrev_b32 v72, 16, v58\n"
    "v_and_b32 v73, s66, v58\n"
    "v_lshlrev_b32 v74, 16, v59\n"
    "v_and_b32 v75, s66, v59\n"
    "v_pk_mul_f32 v[60:61], v[60:61], v[68:69]\n"
    "v_pk_mul_f32 v[62:63], v[62:63], v[70:71]\n"
    "v_pk_mul_f32 v[64:65], v[64:65], v[72:73]\n"
    "v_pk_mul_f32 v[66:67], v[66:67], v[74:75]\n"
    "v_cvt_pk_bf16_f32 v80, v60, v61\n"
    "v_cvt_pk_bf16_f32 v81, v62, v63\n"
    "v_cvt_pk_bf16_f32 v82, v64, v65\n"
    "v_cvt_pk_bf16_f32 v83, v66, v67\n"
    "ds_write_b128 v84, v[80:83] offset:64\n"
    "ds_read_b128 v[232:235], v85\n"
    "ds_read_b128 v[236:239], v85 offset:1152\n"
    "s_mov_b32 s38, s24\n"
    "v_add_u32 v6, s31, v6\n"
    "v_add_u32 v7, s31, v7\n"
    "v_add_u32 v8, s31, v8\n"
    "v_add_u32 v9, s31, v9\n"
    "s_sub_u32 s31, 0, s31\n"
    "s_add_u32 s24, s24, 270336\n"
    "s_add_u32 s24, s24, s75\n"
    "s_waitcnt vmcnt(0)\n"
    "s_sub_u32 s26, s26, 1\n"
    "s_cmp_lg_u32 s26, 1\n"
    "s_cbranch_scc1 PPL_gated_mix__ssm_conv_h3__swiglu_ffn__prompt_kernel\n"
    "s_barrier\n"
    "s_cmp_lg_u32 s34, 0\n"
    "s_cbranch_scc1 NOFA_gated_mix__ssm_conv_h3__swiglu_ffn__prompt_kernel\n"
    "s_lshl_b32 s30, s68, 7\n"
    "s_add_u32 s30, s30, 41005056\n"
    "v_mov_b32 v96, s70\n"
    "v_mov_b32 v97, s71\n"
    "v_mov_b32 v98, 0\n"
    "s_mov_b64 exec, 1\n"
    "buffer_store_dwordx2 v[96:97], v98, s[20:23], s30 offen sc1\n"
    "s_mov_b64 exec, -1\n"
    "NOFA_gated_mix__ssm_conv_h3__swiglu_ffn__prompt_kernel:\n"
    "ds_read_b64_tr_b16 v[40:41], v8 offset:8704\n"
    "ds_read_b64_tr_b16 v[42:43], v6 offset:9216\n"
    "ds_read_b64_tr_b16 v[44:45], v6 offset:9728\n"
    "ds_read_b64_tr_b16 v[46:47], v6 offset:0\n"
    "ds_read_b64_tr_b16 v[48:49], v6 offset:512\n"
    "v_alignbit_b32 v20, v12, v12, 16\n"
    "v_alignbit_b32 v21, v13, v13, 16\n"
    "v_and_b32 v22, 0xffff, v12\n"
    "v_bfi_b32 v23, s66, v12, v13\n"
    "v_and_b32 v24, s66, v13\n"
    "v_alignbit_b32 v25, v14, v14, 16\n"
    "v_alignbit_b32 v26, v15, v15, 16\n"
    "v_and_b32 v27, 0xffff, v14\n"
    "v_bfi_b32 v28, s66, v14, v15\n"
    "v_and_b32 v29, s66, v15\n"
    "v_alignbit_b32 v30, v16, v16, 16\n"
    "v_alignbit_b32 v31, v17, v17, 16\n"
    "v_and_b32 v32, 0xffff, v16\n"
    "v_bfi_b32 v33, s66, v16, v17\n"
    "v_and_b32 v34, s66, v17\n"
    "v_alignbit_b32 v35, v18, v18, 16\n"
    "v_alignbit_b32 v36, v19, v19, 16\n"
    "v_and_b32 v37, 0xffff, v18\n"
    "v_bfi_b32 v38, s66, v18, v19\n"
    "v_and_b32 v39, s66, v19\n"
    "ds_read_b64_tr_b16 v[50:51], v8 offset:12800\n"
    "ds_read_b64_tr_b16 v[52:53], v6 offset:13312\n"
    "ds_read_b64_tr_b16 v[54:55], v6 offset:13824\n"
    "ds_read_b64_tr_b16 v[56:57], v6 offset:4096\n"
    "ds_read_b64_tr_b16 v[58:59], v6 offset:4608\n"
    "s_waitcnt lgkmcnt(5)\n"
    "v_dot2_f32_bf16 v60, v42, v22, 0\n"
    "v_dot2_f32_bf16 v61, v42, v20, 0\n"
    "v_dot2_f32_bf16 v62, v43, v22, 0\n"
    "buffer_store_dwordx4 v[208:211], v86, s[20:23], s38 offen sc1\n"
    "v_dot2_f32_bf16 v63, v43, v20, 0\n"
    "v_dot2_f32_bf16 v64, v44, v22, 0\n"
    "v_dot2_f32_bf16 v65, v44, v20, 0\n"
    "v_dot2_f32_bf16 v66, v45, v22, 0\n"
    "v_dot2_f32_bf16 v67, v45, v20, 0\n"
    "buffer_store_dwordx4 v[212:215], v87, s[20:23], s38 offen sc1\n"
    "v_dot2c_f32_bf16 v60, v41, v23\n"
    "v_dot2c_f32_bf16 v61, v41, v21\n"
    "v_dot2c_f32_bf16 v62, v42, v23\n"
    "v_dot2c_f32_bf16 v63, v42, v21\n"
    "v_dot2c_f32_bf16 v64, v43, v23\n"
    "s_add_u32 s38, s38, 67584\n"
    "v_dot2c_f32_bf16 v65, v43, v21\n"
    "v_dot2c_f32_bf16 v66, v44, v23\n"
    "v_dot2c_f32_bf16 v67, v44, v21\n"
    "v_dot2c_f32_bf16 v60, v40, v24\n"
    "v_dot2c_f32_bf16 v62, v41, v24\n"
    "buffer_store_dwordx4 v[216:219], v86, s[20:23], s38 offen sc1\n"
    "v_dot2c_f32_bf16 v64, v42, v24\n"
    "v_dot2c_f32_bf16 v66, v43, v24\n"
    "v_lshlrev_b32 v68, 16, v46\n"
    "v_and_b32 v69, s66, v46\n"
    "v_lshlrev_b32 v70, 16, v47\n"
    "buffer_store_dwordx4 v[220:223], v87, s[20:23], s38 offen sc1\n"
    "v_and_b32 v71, s66, v47\n"
    "v_lshlrev_b32 v72, 16, v48\n"
    "v_and_b32 v73, s66, v48\n"
    "v_lshlrev_b32 v74, 16, v49\n"
    "v_and_b32 v75, s66, v49\n"
    "s_add_u32 s38, s38, 67584\n"
    "v_pk_mul_f32 v[60:61], v[60:61], v[68:69]\n"
    "v_pk_mul_f32 v[62:63], v[62:63], v[70:71]\n"
    "v_pk_mul_f32 v[64:65], v[64:65], v[72:73]\n"
    "v_pk_mul_f32 v[66:67], v[66:67], v[74:75]\n"
    "v_cvt_pk_bf16_f32 v76, v60, v61\n"
    "v_cvt_pk_bf16_f32 v77, v62, v63\n"
    "v_cvt_pk_bf16_f32 v78, v64, v65\n"
    "v_cvt_pk_bf16_f32 v79, v66, v67\n"
    "ds_write_b128 v84, v[76:79] offset:0\n"
    "ds_read_b64_tr_b16 v[40:41], v9 offset:8704\n"
    "ds_read_b64_tr_b16 v[42:43], v7 offset:9216\n"
    "ds_read_b64_tr_b16 v[44:45], v7 offset:9728\n"
    "ds_read_b64_tr_b16 v[46:47], v7 offset:0\n"
    "ds_read_b64_tr_b16 v[48:49], v7 offset:512\n"
    "s_waitcnt lgkmcnt(5)\n"
    "v_dot2_f32_bf16 v60, v52, v22, 0\n"
    "v_dot2_f32_bf16 v61, v52, v20, 0\n"
    "v_dot2_f32_bf16 v62, v53, v22, 0\n"
    "buffer_store_dwordx4 v[224:227], v86, s[20:23], s38 offen sc1\n"
    "v_dot2_f32_bf16 v63, v53, v20, 0\n"
    "v_dot2_f32_bf16 v64, v54, v22, 0\n"
    "v_dot2_f32_bf16 v65, v54, v20, 0\n"
    "v_dot2_f32_bf16 v66, v55, v22, 0\n"
    "v_dot2_f32_bf16 v67, v55, v20, 0\n"
    "buffer_store_dwordx4 v[228:231], v87, s[20:23], s38 offen sc1\n"
    "v_dot2c_f32_bf16 v60, v51, v23\n"
    "v_dot2c_f32_bf16 v61, v51, v21\n"
    "v_dot2c_f32_bf16 v62, v52, v23\n"
    "v_dot2c_f32_bf16 v63, v52, v21\n"
    "v_dot2c_f32_bf16 v64, v53, v23\n"
    "s_add_u32 s38, s38, 67584\n"
    "v_dot2c_f32_bf16 v65, v53, v21\n"
    "v_dot2c_f32_bf16 v66, v54, v23\n"
    "v_dot2c_f32_bf16 v67, v54, v21\n"
    "v_dot2c_f32_bf16 v60, v50, v24\n"
    "v_dot2c_f32_bf16 v62, v51, v24\n"
    "buffer_store_dwordx4 v[232:235], v86, s[20:23], s38 offen sc1\n"
    "v_dot2c_f32_bf16 v64, v52, v24\n"
    "v_dot2c_f32_bf16 v66, v53, v24\n"
    "v_lshlrev_b32 v68, 16, v56\n"
    "v_and_b32 v69, s66, v56\n"
    "v_lshlrev_b32 v70, 16, v57\n"
    "buffer_store_dwordx4 v[236:239], v87, s[20:23], s38 offen sc1\n"
    "v_and_b32 v71, s66, v57\n"
    "v_lshlrev_b32 v72, 16, v58\n"
    "v_and_b32 v73, s66, v58\n"
    "v_lshlrev_b32 v74, 16, v59\n"
    "v_and_b32 v75, s66, v59\n"
    "s_add_u32 s38, s38, 67584\n"
    "v_pk_mul_f32 v[60:61], v[60:61], v[68:69]\n"
    "v_pk_mul_f32 v[62:63], v[62:63], v[70:71]\n"
    "v_pk_mul_f32 v[64:65], v[64:65], v[72:73]\n"
    "v_pk_mul_f32 v[66:67], v[66:67], v[74:75]\n"
    "v_cvt_pk_bf16_f32 v80, v60, v61\n"
    "v_cvt_pk_bf16_f32 v81, v62, v63\n"
    "v_cvt_pk_bf16_f32 v82, v64, v65\n"
    "v_cvt_pk_bf16_f32 v83, v66, v67\n"
    "ds_write_b128 v84, v[80:83] offset:64\n"
    "ds_read_b128 v[208:211], v85\n"
    "ds_read_b128 v[212:215], v85 offset:1152\n"
    "ds_read_b64_tr_b16 v[50:51], v9 offset:12800\n"
    "ds_read_b64_tr_b16 v[52:53], v7 offset:13312\n"
    "ds_read_b64_tr_b16 v[54:55], v7 offset:13824\n"
    "ds_read_b64_tr_b16 v[56:57], v7 offset:4096\n"
    "ds_read_b64_tr_b16 v[58:59], v7 offset:4608\n"
    "s_waitcnt lgkmcnt(5)\n"
    "buffer_load_dwordx4 v[126:129], v160, s[80:83], s73 offen offset:0\n"
    "buffer_load_dwordx2 v[124:125], v161, s[80:83], s73 offen offset:0\n"
    "buffer_load_dwordx4 v[132:135], v160, s[80:83], s73 offen offset:1024\n"
    "buffer_load_dwordx2 v[130:131], v160, s[80:83], s73 offen offset:1016\n"
    "buffer_load_dwordx4 v[138:141], v160, s[80:83], s73 offen offset:2048\n"
    "buffer_load_dwordx2 v[136:137], v160, s[80:83], s73 offen offset:2040\n"
    "buffer_load_dwordx4 v[144:147], v160, s[80:83], s73 offen offset:3072\n"
    "buffer_load_dwordx2 v[142:143], v160, s[80:83], s73 offen offset:3064\n"
    "s_lshl_b32 s30, s39, 2\n"
    "s_add_u32 s30, s30, s34\n"
    "s_lshl_b32 s30, s30, 12\n"
    "buffer_load_dwordx4 v[104:107], v160, s[8:11], s30 offen offset:0\n"
    "buffer_load_dwordx4 v[108:111], v160, s[8:11], s30 offen offset:1024\n"
    "buffer_load_dwordx4 v[112:115], v160, s[8:11], s30 offen offset:2048\n"
    "buffer_load_dwordx4 v[116:119], v160, s[8:11], s30 offen offset:3072\n"
    "global_load_dwordx4 v[148:151], v160, s[90:91] offset:0\n"
    "global_load_dwordx4 v[152:155], v160, s[90:91] offset:1024\n"
    "s_lshl_b32 s30, s34, 12\n"
    "s_add_u32 s30, s30, SPC_gated_mix__ssm_conv_h3__swiglu_ffn__prompt_kernel-KSTART_gated_mix__ssm_conv_h3__swiglu_ffn__prompt_kernel\n"
    "buffer_load_dword a50, v176, s[92:95], s30 offen\n"
    "buffer_load_dwordx2 v[100:101], v175, s[20:23], 0 offen sc1\n"
    "v_dot2_f32_bf16 v60, v42, v27, 0\n"
    "v_dot2_f32_bf16 v61, v42, v25, 0\n"
    "v_dot2_f32_bf16 v62, v43, v27, 0\n"
    "v_dot2_f32_bf16 v63, v43, v25, 0\n"
    "v_dot2_f32_bf16 v64, v44, v27, 0\n"
    "v_dot2_f32_bf16 v65, v44, v25, 0\n"
    "v_dot2_f32_bf16 v66, v45, v27, 0\n"
    "v_dot2_f32_bf16 v67, v45, v25, 0\n"
    "v_dot2c_f32_bf16 v60, v41, v28\n"
    "v_dot2c_f32_bf16 v61, v41, v26\n"
    "v_dot2c_f32_bf16 v62, v42, v28\n"
    "v_dot2c_f32_bf16 v63, v42, v26\n"
    "v_dot2c_f32_bf16 v64, v43, v28\n"
    "v_dot2c_f32_bf16 v65, v43, v26\n"
    "v_dot2c_f32_bf16 v66, v44, v28\n"
    "v_dot2c_f32_bf16 v67, v44, v26\n"
    "v_dot2c_f32_bf16 v60, v40, v29\n"
    "v_dot2c_f32_bf16 v62, v41, v29\n"
    "v_dot2c_f32_bf16 v64, v42, v29\n"
    "v_dot2c_f32_bf16 v66, v43, v29\n"
    "v_lshlrev_b32 v68, 16, v46\n"
    "v_and_b32 v69, s66, v46\n"
    "v_lshlrev_b32 v70, 16, v47\n"
    "v_and_b32 v71, s66, v47\n"
    "v_lshlrev_b32 v72, 16, v48\n"
    "v_and_b32 v73, s66, v48\n"
    "v_lshlrev_b32 v74, 16, v49\n"
    "v_and_b32 v75, s66, v49\n"
    "v_pk_mul_f32 v[60:61], v[60:61], v[68:69]\n"
    "v_pk_mul_f32 v[62:63], v[62:63], v[70:71]\n"
    "v_pk_mul_f32 v[64:65], v[64:65], v[72:73]\n"
    "v_pk_mul_f32 v[66:67], v[66:67], v[74:75]\n"
    "v_cvt_pk_bf16_f32 v76, v60, v61\n"
    "v_cvt_pk_bf16_f32 v77, v62, v63\n"
    "v_cvt_pk_bf16_f32 v78, v64, v65\n"
    "v_cvt_pk_bf16_f32 v79, v66, v67\n"
    "ds_write_b128 v84, v[76:79] offset:0\n"
    "ds_read_b64_tr_b16 v[40:41], v6 offset:8704\n"
    "ds_read_b64_tr_b16 v[42:43], v8 offset:9216\n"
    "ds_read_b64_tr_b16 v[44:45], v8 offset:9728\n"
    "ds_read_b64_tr_b16 v[46:47], v8 offset:0\n"
    "ds_read_b64_tr_b16 v[48:49], v8 offset:512\n"
    "s_waitcnt lgkmcnt(5)\n"
    "v_dot2_f32_bf16 v60, v52, v27, 0\n"
    "v_dot2_f32_bf16 v61, v52, v25, 0\n"
    "v_dot2_f32_bf16 v62, v53, v27, 0\n"
    "v_dot2_f32_bf16 v63, v53, v25, 0\n"
    "v_dot2_f32_bf16 v64, v54, v27, 0\n"
    "v_dot2_f32_bf16 v65, v54, v25, 0\n"
    "v_dot2_f32_bf16 v66, v55, v27, 0\n"
    "v_dot2_f32_bf16 v67, v55, v25, 0\n"
    "v_dot2c_f32_bf16 v60, v51, v28\n"
    "v_dot2c_f32_bf16 v61, v51, v26\n"
    "v_dot2c_f32_bf16 v62, v52, v28\n"
    "v_dot2c_f32_bf16 v63, v52, v26\n"
    "v_dot2c_f32_bf16 v64, v53, v28\n"
    "v_dot2c_f32_bf16 v65, v53, v26\n"
    "v_dot2c_f32_bf16 v66, v54, v28\n"
    "v_dot2c_f32_bf16 v67, v54, v26\n"
    "v_dot2c_f32_bf16 v60, v50, v29\n"
    "v_dot2c_f32_bf16 v62, v51, v29\n"
    "v_dot2c_f32_bf16 v64, v52, v29\n"
    "v_dot2c_f32_bf16 v66, v53, v29\n"
    "v_lshlrev_b32 v68, 16, v56\n"
    "v_and_b32 v69, s66, v56\n"
    "v_lshlrev_b32 v70, 16, v57\n"
    "v_and_b32 v71, s66, v57\n"
    "v_lshlrev_b32 v72, 16, v58\n"
    "v_and_b32 v73, s66, v58\n"
    "v_lshlrev_b32 v74, 16, v59\n"
    "v_and_b32 v75, s66, v59\n"
    "v_pk_mul_f32 v[60:61], v[60:61], v[68:69]\n"
    "v_pk_mul_f32 v[62:63], v[62:63], v[70:71]\n"
    "v_pk_mul_f32 v[64:65], v[64:65], v[72:73]\n"
    "v_pk_mul_f32 v[66:67], v[66:67], v[74:75]\n"
    "v_cvt_pk_bf16_f32 v80, v60, v61\n"
    "v_cvt_pk_bf16_f32 v81, v62, v63\n"
    "v_cvt_pk_bf16_f32 v82, v64, v65\n"
    "v_cvt_pk_bf16_f32 v83, v66, v67\n"
    "ds_write_b128 v84, v[80:83] offset:64\n"
    "ds_read_b128 v[216:219], v85\n"
    "ds_read_b128 v[220:223], v85 offset:1152\n"
    "ds_read_b64_tr_b16 v[50:51], v6 offset:12800\n"
    "ds_read_b64_tr_b16 v[52:53], v8 offset:13312\n"
    "ds_read_b64_tr_b16 v[54:55], v8 offset:13824\n"
    "ds_read_b64_tr_b16 v[56:57], v8 offset:4096\n"
    "ds_read_b64_tr_b16 v[58:59], v8 offset:4608\n"
    "s_waitcnt lgkmcnt(5)\n"
    "v_dot2_f32_bf16 v60, v42, v32, 0\n"
    "v_dot2_f32_bf16 v61, v42, v30, 0\n"
    "v_dot2_f32_bf16 v62, v43, v32, 0\n"
    "v_dot2_f32_bf16 v63, v43, v30, 0\n"
    "v_dot2_f32_bf16 v64, v44, v32, 0\n"
    "v_dot2_f32_bf16 v65, v44, v30, 0\n"
    "v_dot2_f32_bf16 v66, v45, v32, 0\n"
    "v_dot2_f32_bf16 v67, v45, v30, 0\n"
    "v_dot2c_f32_bf16 v60, v41, v33\n"
    "v_dot2c_f32_bf16 v61, v41, v31\n"
    "v_dot2c_f32_bf16 v62, v42, v33\n"
    "v_dot2c_f32_bf16 v63, v42, v31\n"
    "v_dot2c_f32_bf16 v64, v43, v33\n"
    "v_dot2c_f32_bf16 v65, v43, v31\n"
    "v_dot2c_f32_bf16 v66, v44, v33\n"
    "v_dot2c_f32_bf16 v67, v44, v31\n"
    "v_dot2c_f32_bf16 v60, v40, v34\n"
    "v_dot2c_f32_bf16 v62, v41, v34\n"
    "v_dot2c_f32_bf16 v64, v42, v34\n"
    "v_dot2c_f32_bf16 v66, v43, v34\n"
    "v_lshlrev_b32 v68, 16, v46\n"
    "v_and_b32 v69, s66, v46\n"
    "v_lshlrev_b32 v70, 16, v47\n"
    "v_and_b32 v71, s66, v47\n"
    "v_lshlrev_b32 v72, 16, v48\n"
    "v_and_b32 v73, s66, v48\n"
    "v_lshlrev_b32 v74, 16, v49\n"
    "v_and_b32 v75, s66, v49\n"
    "v_pk_mul_f32 v[60:61], v[60:61], v[68:69]\n"
    "v_pk_mul_f32 v[62:63], v[62:63], v[70:71]\n"
    "v_pk_mul_f32 v[64:65], v[64:65], v[72:73]\n"
    "v_pk_mul_f32 v[66:67], v[66:67], v[74:75]\n"
    "v_cvt_pk_bf16_f32 v76, v60, v61\n"
    "v_cvt_pk_bf16_f32 v77, v62, v63\n"
    "v_cvt_pk_bf16_f32 v78, v64, v65\n"
    "v_cvt_pk_bf16_f32 v79, v66, v67\n"
    "ds_write_b128 v84, v[76:79] offset:0\n"
    "ds_read_b64_tr_b16 v[40:41], v7 offset:8704\n"
    "ds_read_b64_tr_b16 v[42:43], v9 offset:9216\n"
    "ds_read_b64_tr_b16 v[44:45], v9 offset:9728\n"
    "ds_read_b64_tr_b16 v[46:47], v9 offset:0\n"
    "ds_read_b64_tr_b16 v[48:49], v9 offset:512\n"
    "s_waitcnt lgkmcnt(5)\n"
    "v_dot2_f32_bf16 v60, v52, v32, 0\n"
    "v_dot2_f32_bf16 v61, v52, v30, 0\n"
    "v_dot2_f32_bf16 v62, v53, v32, 0\n"
    "v_dot2_f32_bf16 v63, v53, v30, 0\n"
    "v_dot2_f32_bf16 v64, v54, v32, 0\n"
    "v_dot2_f32_bf16 v65, v54, v30, 0\n"
    "v_dot2_f32_bf16 v66, v55, v32, 0\n"
    "v_dot2_f32_bf16 v67, v55, v30, 0\n"
    "v_dot2c_f32_bf16 v60, v51, v33\n"
    "v_dot2c_f32_bf16 v61, v51, v31\n"
    "v_dot2c_f32_bf16 v62, v52, v33\n"
    "v_dot2c_f32_bf16 v63, v52, v31\n"
    "v_dot2c_f32_bf16 v64, v53, v33\n"
    "v_dot2c_f32_bf16 v65, v53, v31\n"
    "v_dot2c_f32_bf16 v66, v54, v33\n"
    "v_dot2c_f32_bf16 v67, v54, v31\n"
    "v_dot2c_f32_bf16 v60, v50, v34\n"
    "v_dot2c_f32_bf16 v62, v51, v34\n"
    "v_dot2c_f32_bf16 v64, v52, v34\n"
    "v_dot2c_f32_bf16 v66, v53, v34\n"
    "v_lshlrev_b32 v68, 16, v56\n"
    "v_and_b32 v69, s66, v56\n"
    "v_lshlrev_b32 v70, 16, v57\n"
    "v_and_b32 v71, s66, v57\n"
    "v_lshlrev_b32 v72, 16, v58\n"
    "v_and_b32 v73, s66, v58\n"
    "v_lshlrev_b32 v74, 16, v59\n"
    "v_and_b32 v75, s66, v59\n"
    "v_pk_mul_f32 v[60:61], v[60:61], v[68:69]\n"
    "v_pk_mul_f32 v[62:63], v[62:63], v[70:71]\n"
    "v_pk_mul_f32 v[64:65], v[64:65], v[72:73]\n"
    "v_pk_mul_f32 v[66:67], v[66:67], v[74:75]\n"
    "v_cvt_pk_bf16_f32 v80, v60, v61\n"
    "v_cvt_pk_bf16_f32 v81, v62, v63\n"
    "v_cvt_pk_bf16_f32 v82, v64, v65\n"
    "v_cvt_pk_bf16_f32 v83, v66, v67\n"
    "ds_write_b128 v84, v[80:83] offset:64\n"
    "ds_read_b128 v[224:227], v85\n"
    "ds_read_b128 v[228:231], v85 offset:1152\n"
    "ds_read_b64_tr_b16 v[50:51], v7 offset:12800\n"
    "ds_read_b64_tr_b16 v[52:53], v9 offset:13312\n"
    "ds_read_b64_tr_b16 v[54:55], v9 offset:13824\n"
    "ds_read_b64_tr_b16 v[56:57], v9 offset:4096\n"
    "ds_read_b64_tr_b16 v[58:59], v9 offset:4608\n"
    "s_waitcnt lgkmcnt(5)\n"
    "s_waitcnt vmcnt(0)\n"
    "v_cmp_ne_u64 vcc, s[70:71], v[100:101]\n"
    "s_cbranch_vccz AOK_gated_mix__ssm_conv_h3__swiglu_ffn__prompt_kernel\n"
    "s_mov_b32 s29, 0x4000\n"
    "PU2_gated_mix__ssm_conv_h3__swiglu_ffn__prompt_kernel:\n"
    "buffer_load_dwordx2 v[100:101], v175, s[20:23], 0 offen sc1\n"
    "s_waitcnt vmcnt(0)\n"
    "v_cmp_ne_u64 vcc, s[70:71], v[100:101]\n"
    "s_cbranch_vccz PU2d_gated_mix__ssm_conv_h3__swiglu_ffn__prompt_kernel\n"
    "s_sleep 1\n"
    "s_sub_u32 s29, s29, 1\n"
    "s_cmp_lg_u32 s29, 0\n"
    "s_cbranch_scc1 PU2_gated_mix__ssm_conv_h3__swiglu_ffn__prompt_kernel\n"
    "PU2d_gated_mix__ssm_conv_h3__swiglu_ffn__prompt_kernel:\n"
    "AOK_gated_mix__ssm_conv_h3__swiglu_ffn__prompt_kernel:\n"
    "s_lshr_b32 s69, s73, 12\n"
    "s_mul_i32 s69, s69, 4224\n"
    "s_add_u32 s69, s69, 18874368\n"
    "buffer_load_dwordx4 v[192:195], v160, s[20:23], s69 offen offset:0 sc1\n"
    "buffer_load_dwordx4 v[196:199], v160, s[20:23], s69 offen offset:1024 sc1\n"
    "buffer_load_dwordx4 v[200:203], v160, s[20:23], s69 offen offset:2048 sc1\n"
    "buffer_load_dwordx4 v[204:207], v160, s[20:23], s69 offen offset:3072 sc1\n"
    "v_dot2_f32_bf16 v60, v42, v37, 0\n"
    "v_dot2_f32_bf16 v61, v42, v35, 0\n"
    "v_dot2_f32_bf16 v62, v43, v37, 0\n"
    "v_dot2_f32_bf16 v63, v43, v35, 0\n"
    "v_dot2_f32_bf16 v64, v44, v37, 0\n"
    "v_dot2_f32_bf16 v65, v44, v35, 0\n"
    "v_dot2_f32_bf16 v66, v45, v37, 0\n"
    "v_dot2_f32_bf16 v67, v45, v35, 0\n"
    "v_dot2c_f32_bf16 v60, v41, v38\n"
    "v_dot2c_f32_bf16 v61, v41, v36\n"
    "v_dot2c_f32_bf16 v62, v42, v38\n"
    "v_dot2c_f32_bf16 v63, v42, v36\n"
    "v_dot2c_f32_bf16 v64, v43, v38\n"
    "v_dot2c_f32_bf16 v65, v43, v36\n"
    "v_dot2c_f32_bf16 v66, v44, v38\n"
    "v_dot2c_f32_bf16 v67, v44, v36\n"
    "v_dot2c_f32_bf16 v60, v40, v39\n"
    "v_dot2c_f32_bf16 v62, v41, v39\n"
    "v_dot2c_f32_bf16 v64, v42, v39\n"
    "v_dot2c_f32_bf16 v66, v43, v39\n"
    "v_lshlrev_b32 v68, 16, v46\n"
    "v_and_b32 v69, s66, v46\n"
    "v_lshlrev_b32 v70, 16, v47\n"
    "v_and_b32 v71, s66, v47\n"
    "v_lshlrev_b32 v72, 16, v48\n"
    "v_and_b32 v73, s66, v48\n"
    "v_lshlrev_b32 v74, 16, v49\n"
    "v_and_b32 v75, s66, v49\n"
    "v_pk_mul_f32 v[60:61], v[60:61], v[68:69]\n"
    "v_pk_mul_f32 v[62:63], v[62:63], v[70:71]\n"
    "v_pk_mul_f32 v[64:65], v[64:65], v[72:73]\n"
    "v_pk_mul_f32 v[66:67], v[66:67], v[74:75]\n"
    "v_cvt_pk_bf16_f32 v76, v60, v61\n"
    "v_cvt_pk_bf16_f32 v77, v62, v63\n"
    "v_cvt_pk_bf16_f32 v78, v64, v65\n"
    "v_cvt_pk_bf16_f32 v79, v66, v67\n"
    "ds_write_b128 v84, v[76:79] offset:0\n"
    "s_waitcnt lgkmcnt(0)\n"
    "v_dot2_f32_bf16 v60, v52, v37, 0\n"
    "v_dot2_f32_bf16 v61, v52, v35, 0\n"
    "v_dot2_f32_bf16 v62, v53, v37, 0\n"
    "v_dot2_f32_bf16 v63, v53, v35, 0\n"
    "v_dot2_f32_bf16 v64, v54, v37, 0\n"
    "v_dot2_f32_bf16 v65, v54, v35, 0\n"
    "v_dot2_f32_bf16 v66, v55, v37, 0\n"
    "v_dot2_f32_bf16 v67, v55, v35, 0\n"
    "v_dot2c_f32_bf16 v60, v51, v38\n"
    "v_dot2c_f32_bf16 v61, v51, v36\n"
    "v_dot2c_f32_bf16 v62, v52, v38\n"
    "v_dot2c_f32_bf16 v63, v52, v36\n"
    "v_dot2c_f32_bf16 v64, v53, v38\n"
    "v_dot2c_f32_bf16 v65, v53, v36\n"
    "v_dot2c_f32_bf16 v66, v54, v38\n"
    "v_dot2c_f32_bf16 v67, v54, v36\n"
    "v_dot2c_f32_bf16 v60, v50, v39\n"
    "v_dot2c_f32_bf16 v62, v51, v39\n"
    "v_dot2c_f32_bf16 v64, v52, v39\n"
    "v_dot2c_f32_bf16 v66, v53, v39\n"
    "v_lshlrev_b32 v68, 16, v56\n"
    "v_and_b32 v69, s66, v56\n"
    "v_lshlrev_b32 v70, 16, v57\n"
    "v_and_b32 v71, s66, v57\n"
    "v_lshlrev_b32 v72, 16, v58\n"
    "v_and_b32 v73, s66, v58\n"
    "v_lshlrev_b32 v74, 16, v59\n"
    "v_and_b32 v75, s66, v59\n"
    "v_pk_mul_f32 v[60:61], v[60:61], v[68:69]\n"
    "v_pk_mul_f32 v[62:63], v[62:63], v[70:71]\n"
    "v_pk_mul_f32 v[64:65], v[64:65], v[72:73]\n"
    "v_pk_mul_f32 v[66:67], v[66:67], v[74:75]\n"
    "v_cvt_pk_bf16_f32 v80, v60, v61\n"
    "v_cvt_pk_bf16_f32 v81, v62, v63\n"
    "v_cvt_pk_bf16_f32 v82, v64, v65\n"
    "v_cvt_pk_bf16_f32 v83, v66, v67\n"
    "ds_write_b128 v84, v[80:83] offset:64\n"
    "ds_read_b128 v[232:235], v85\n"
    "ds_read_b128 v[236:239], v85 offset:1152\n"
    "s_mov_b32 s38, s24\n"
    "s_waitcnt lgkmcnt(0)\n"
    "s_barrier\n"
    "SPC_gated_mix__ssm_conv_h3__swiglu_ffn__prompt_kernel:\n"
    "s_mov_b32 s68, s73\n"
    "s_mul_i32 s27, s34, 16640\n"
    "s_mul_i32 s26, s34, 21248\n"
    "s_add_u32 s26, s26, 66560\n"
    "s_add_u32 m0, s26, 32\n"
    "s_nop 0\n"
    "buffer_load_dwordx4 v160, s[80:83], s68 offen offset:0 lds\n"
    "buffer_load_dwordx4 v160, s[80:83], s68 offen offset:1024 lds\n"
    "buffer_load_dwordx4 v160, s[80:83], s68 offen offset:2048 lds\n"
    "buffer_load_dwordx4 v160, s[80:83], s68 offen offset:3072 lds\n"
    "s_add_u32 m0, s26, 8356\n"
    "s_nop 0\n"
    "buffer_load_dwordx4 v160, s[80:83], s68 offen offset:1020 lds\n"
    "buffer_load_dwordx4 v160, s[80:83], s68 offen offset:2044 lds\n"
    "buffer_load_dwordx4 v160, s[80:83], s68 offen offset:3068 lds\n"
    "buffer_store_dwordx4 v[208:211], v86, s[20:23], s38 offen sc1\n"
    "buffer_store_dwordx4 v[212:215], v87, s[20:23], s38 offen sc1\n"
    "s_add_u32 s38, s38, 67584\n"
    "buffer_store_dwordx4 v[216:219], v86, s[20:23], s38 offen sc1\n"
    "buffer_store_dwordx4 v[220:223], v87, s[20:23], s38 offen sc1\n"
    "s_add_u32 s38, s38, 67584\n"
    "buffer_store_dwordx4 v[224:227], v86, s[20:23], s38 offen sc1\n"
    "buffer_store_dwordx4 v[228:231], v87, s[20:23], s38 offen sc1\n"
    "s_add_u32 s38, s38, 67584\n"
    "buffer_store_dwordx4 v[232:235], v86, s[20:23], s38 offen sc1\n"
    "buffer_store_dwordx4 v[236:239], v87, s[20:23], s38 offen sc1\n"
    "s_add_u32 s38, s38, 67584\n"
    "s_and_b32 s99, s34, 1\n"
    "s_lshl_b32 s99, s99, 8\n"
    "s_mov_b32 s75, s54\n"
    "v_mov_b32 v72, 0\n"
    "v_mov_b32 v73, 0\n"
    "v_add_u32 v64, s26, v166\n"
    "v_mov_b32 v65, v167\n"
    "ds_write_b32 v162, v72\n"
    "ds_write_b32 v163, v72\n"
    "ds_write_b64 v164, v[72:73]\n"
    "v_mov_b32 v228, v125\n"
    "v_mov_b32 v229, v126\n"
    "v_mov_b32 v230, v127\n"
    "v_mov_b32 v231, v128\n"
    "ds_write_b128 v177, v[228:231] offset:8352\n"
    "v_alignbit_b32 v232, v126, v125, 16\n"
    "v_alignbit_b32 v233, v127, v126, 16\n"
    "v_alignbit_b32 v234, v128, v127, 16\n"
    "v_alignbit_b32 v235, v129, v128, 16\n"
    "ds_write_b128 v177, v[232:235] offset:4192\n"
    "v_alignbit_b32 v236, v125, v124, 16\n"
    "v_alignbit_b32 v237, v126, v125, 16\n"
    "v_alignbit_b32 v238, v127, v126, 16\n"
    "v_alignbit_b32 v239, v128, v127, 16\n"
    "ds_write_b128 v177, v[236:239] offset:12512\n"
    "v_alignbit_b32 v240, v132, v131, 16\n"
    "v_alignbit_b32 v241, v133, v132, 16\n"
    "v_alignbit_b32 v242, v134, v133, 16\n"
    "v_alignbit_b32 v243, v135, v134, 16\n"
    "ds_write_b128 v177, v[240:243] offset:5216\n"
    "v_alignbit_b32 v244, v131, v130, 16\n"
    "v_alignbit_b32 v245, v132, v131, 16\n"
    "v_alignbit_b32 v246, v133, v132, 16\n"
    "v_alignbit_b32 v247, v134, v133, 16\n"
    "ds_write_b128 v177, v[244:247] offset:13536\n"
    "v_alignbit_b32 v248, v138, v137, 16\n"
    "v_alignbit_b32 v249, v139, v138, 16\n"
    "v_alignbit_b32 v250, v140, v139, 16\n"
    "v_alignbit_b32 v251, v141, v140, 16\n"
    "ds_write_b128 v177, v[248:251] offset:6240\n"
    "v_alignbit_b32 v228, v137, v136, 16\n"
    "v_alignbit_b32 v229, v138, v137, 16\n"
    "v_alignbit_b32 v230, v139, v138, 16\n"
    "v_alignbit_b32 v231, v140, v139, 16\n"
    "ds_write_b128 v177, v[228:231] offset:14560\n"
    "v_alignbit_b32 v232, v144, v143, 16\n"
    "v_alignbit_b32 v233, v145, v144, 16\n"
    "v_alignbit_b32 v234, v146, v145, 16\n"
    "v_alignbit_b32 v235, v147, v146, 16\n"
    "ds_write_b128 v177, v[232:235] offset:7264\n"
    "v_alignbit_b32 v236, v143, v142, 16\n"
    "v_alignbit_b32 v237, v144, v143, 16\n"
    "v_alignbit_b32 v238, v145, v144, 16\n"
    "v_alignbit_b32 v239, v146, v145, 16\n"
    "ds_write_b128 v177, v[236:239] offset:15584\n"
    "s_waitcnt vmcnt(15)\n"
    "ds_write_b128 v165, v[192:195] offset:480\n"
    "ds_write_b128 v165, v[196:199] offset:1504\n"
    "ds_write_b128 v165, v[200:203] offset:2528\n"
    "ds_write_b128 v165, v[204:207] offset:3552\n"
    "s_waitcnt vmcnt(8)\n"
    "ds_read_b64_tr_b16 a[128:129], v64 offset:8\n"
    "ds_read_b64_tr_b16 a[130:131], v64 offset:0\n"
    "ds_read_b128 a[160:163], v65 offset:448\n"
    "ds_read_b128 a[164:167], v65 offset:960\n"
    "ds_read_b128 a[168:171], v65 offset:1472\n"
    "ds_read_b128 a[172:175], v65 offset:1984\n"
    "ds_read_b128 a[176:179], v65 offset:2496\n"
    "ds_read_b128 a[180:183], v65 offset:3008\n"
    "ds_read_b128 a[184:187], v65 offset:3520\n"
    "ds_read_b128 a[188:191], v65 offset:4032\n"
    "ds_read_b64_tr_b16 a[132:133], v64 offset:520\n"
    "ds_read_b64_tr_b16 a[134:135], v64 offset:512\n"
    "ds_read_b64_tr_b16 a[136:137], v64 offset:1032\n"
    "ds_read_b64_tr_b16 a[138:139], v64 offset:1024\n"
    "ds_read_b64_tr_b16 a[140:141], v64 offset:1544\n"
    "ds_read_b64_tr_b16 a[142:143], v64 offset:1536\n"
    "ds_read_b64_tr_b16 a[144:145], v64 offset:2056\n"
    "ds_read_b64_tr_b16 a[146:147], v64 offset:2048\n"
    "ds_read_b64_tr_b16 a[148:149], v64 offset:2568\n"
    "ds_read_b64_tr_b16 a[150:151], v64 offset:2560\n"
    "ds_read_b64_tr_b16 a[152:153], v64 offset:3080\n"
    "ds_read_b64_tr_b16 a[154:155], v64 offset:3072\n"
    "ds_read_b64_tr_b16 a[156:157], v64 offset:3592\n"
    "ds_read_b64_tr_b16 a[158:159], v64 offset:3584\n"
    "v_dot2_f32_bf16 v120, v104, v148, 0\n"
    "v_dot2_f32_bf16 v121, v112, v148, 0\n"
    "v_dot2_f32_bf16 v120, v105, v149, v120\n"
    "v_dot2_f32_bf16 v121, v113, v149, v121\n"
    "v_dot2_f32_bf16 v120, v106, v150, v120\n"
    "v_dot2_f32_bf16 v121, v114, v150, v121\n"
    "v_dot2_f32_bf16 v120, v107, v151, v120\n"
    "v_dot2_f32_bf16 v121, v115, v151, v121\n"
    "v_dot2_f32_bf16 v120, v108, v152, v120\n"
    "v_dot2_f32_bf16 v121, v116, v152, v121\n"
    "v_dot2_f32_bf16 v120, v109, v153, v120\n"
    "v_dot2_f32_bf16 v121, v117, v153, v121\n"
    "v_dot2_f32_bf16 v120, v110, v154, v120\n"
    "v_dot2_f32_bf16 v121, v118, v154, v121\n"
    "v_dot2_f32_bf16 v120, v111, v155, v120\n"
    "v_dot2_f32_bf16 v121, v119, v155, v121\n"
    "s_nop 2\n"
    "v_add_f32_dpp v120, v120, v120 row_shr:1 row_mask:0xf bank_mask:0xf bound_ctrl:0\n"
    "v_add_f32_dpp v121, v121, v121 row_shr:1 row_mask:0xf bank_mask:0xf bound_ctrl:0\n"
    "s_nop 0\n"
    "v_add_f32_dpp v120, v120, v120 row_shr:2 row_mask:0xf bank_mask:0xf bound_ctrl:0\n"
    "v_add_f32_dpp v121, v121, v121 row_shr:2 row_mask:0xf bank_mask:0xf bound_ctrl:0\n"
    "s_nop 0\n"
    "v_add_f32_dpp v120, v120, v120 row_shr:4 row_mask:0xf bank_mask:0xf bound_ctrl:0\n"
    "v_add_f32_dpp v121, v121, v121 row_shr:4 row_mask:0xf bank_mask:0xf bound_ctrl:0\n"
    "s_nop 0\n"
    "v_add_f32_dpp v120, v120, v120 row_shr:8 row_mask:0xf bank_mask:0xf bound_ctrl:0\n"
    "v_add_f32_dpp v121, v121, v121 row_shr:8 row_mask:0xf bank_mask:0xf bound_ctrl:0\n"
    "s_nop 0\n"
    "v_add_f32_dpp v120, v120, v120 row_bcast:15 row_mask:0xa bank_mask:0xf\n"
    "v_add_f32_dpp v121, v121, v121 row_bcast:15 row_mask:0xa bank_mask:0xf\n"
    "s_nop 0\n"
    "v_add_f32_dpp v120, v120, v120 row_bcast:31 row_mask:0xc bank_mask:0xf\n"
    "v_add_f32_dpp v121, v121, v121 row_bcast:31 row_mask:0xc bank_mask:0xf\n"
    "s_nop 0\n"
    "v_mul_f32 v120, 0xbfb8aa3b, v120\n"
    "v_mul_f32 v121, 0xbfb8aa3b, v121\n"
    "v_exp_f32 v120, v120\n"
    "v_exp_f32 v121, v121\n"
    "s_nop 0\n"
    "v_add_f32 v120, 1.0, v120\n"
    "v_add_f32 v121, 1.0, v121\n"
    "v_rcp_f32 v120, v120\n"
    "v_rcp_f32 v121, v121\n"
    "s_lshl_b32 s30, s39, 2\n"
    "s_add_u32 s30, s30, s34\n"
    "s_lshl_b32 s30, s30, 3\n"
    "s_add_u32 s30, s30, 40894464\n"
    "v_mov_b32 v122, 0\n"
    "s_mov_b32 exec_lo, 0\n"
    "s_mov_b32 exec_hi, 0x80000000\n"
    "buffer_store_dwordx2 v[120:121], v122, s[20:23], s30 offen sc1\n"
    "s_mov_b64 exec, -1\n"
    "s_mov_b32 s24, 2146435072\n"
    "s_mov_b32 s38, 4\n"
    "s_waitcnt lgkmcnt(0)\n"
    "s_and_b32 s69, s52, 0xffff\n"
    "s_mul_i32 s69, s69, 0x10001\n"
    "s_lshr_b64 s[52:53], s[52:53], 16\n"
    "v_and_b32 v68, s69, v168\n"
    "v_and_b32 v69, s69, v169\n"
    "v_and_b32 v70, s69, v170\n"
    "v_and_b32 v71, s69, v171\n"
    "s_nop 1\n"
    "SCH_gated_mix__ssm_conv_h3__swiglu_ffn__prompt_kernel:\n"
    "v_cvt_pk_bf16_f32 v212, v32, v33\n"
    "v_cvt_pk_bf16_f32 v213, v34, v35\n"
    "v_mfma_f32_16x16x32_bf16 v[32:35], v[68:71], a[160:163], 0\n"
    "v_cvt_pk_bf16_f32 v214, v36, v37\n"
    "v_cvt_pk_bf16_f32 v215, v38, v39\n"
    "v_mfma_f32_16x16x32_bf16 v[36:39], v[68:71], a[164:167], 0\n"
    "v_cvt_pk_bf16_f32 v216, v40, v41\n"
    "v_cvt_pk_bf16_f32 v217, v42, v43\n"
    "v_mfma_f32_16x16x32_bf16 v[40:43], v[68:71], a[168:171], 0\n"
    "v_cvt_pk_bf16_f32 v218, v44, v45\n"
    "v_cvt_pk_bf16_f32 v219, v46, v47\n"
    "v_mfma_f32_16x16x32_bf16 v[44:47], v[68:71], a[172:175], 0\n"
    "v_cvt_pk_bf16_f32 v220, v48, v49\n"
    "v_cvt_pk_bf16_f32 v221, v50, v51\n"
    "v_mfma_f32_16x16x32_bf16 v[48:51], v[68:71], a[176:179], 0\n"
    "v_cvt_pk_bf16_f32 v222, v52, v53\n"
    "v_cvt_pk_bf16_f32 v223, v54, v55\n"
    "v_mfma_f32_16x16x32_bf16 v[52:55], v[68:71], a[180:183], 0\n"
    "v_cvt_pk_bf16_f32 v224, v56, v57\n"
    "v_cvt_pk_bf16_f32 v225, v58, v59\n"
    "v_mfma_f32_16x16x32_bf16 v[56:59], v[68:71], a[184:187], 0\n"
    "v_cvt_pk_bf16_f32 v226, v60, v61\n"
    "v_cvt_pk_bf16_f32 v227, v62, v63\n"
    "v_mfma_f32_16x16x32_bf16 v[60:63], v[68:71], a[188:191], 0\n"
    "v_mfma_f32_16x16x32_bf16 v[32:35], a[128:131], a[160:163], v[32:35]\n"
    "ds_read_b64_tr_b16 a[192:193], v64 offset:72\n"
    "v_mfma_f32_16x16x32_bf16 v[36:39], a[128:131], a[164:167], v[36:39]\n"
    "ds_read_b64_tr_b16 a[194:195], v64 offset:64\n"
    "v_mfma_f32_16x16x32_bf16 v[40:43], a[128:131], a[168:171], v[40:43]\n"
    "s_lshr_b32 s98, s68, 12\n"
    "ds_read_b128 a[224:227], v65 offset:384\n"
    "v_mfma_f32_16x16x32_bf16 v[44:47], a[128:131], a[172:175], v[44:47]\n"
    "ds_read_b128 a[228:231], v65 offset:896\n"
    "v_mfma_f32_16x16x32_bf16 v[48:51], a[128:131], a[176:179], v[48:51]\n"
    "ds_read_b128 a[232:235], v65 offset:1408\n"
    "v_mfma_f32_16x16x32_bf16 v[52:55], a[128:131], a[180:183], v[52:55]\n"
    "s_mul_i32 s98, s98, 4224\n"
    "ds_read_b128 a[236:239], v65 offset:1920\n"
    "v_mfma_f32_16x16x32_bf16 v[56:59], a[128:131], a[184:187], v[56:59]\n"
    "ds_read_b128 a[240:243], v65 offset:2432\n"
    "v_mfma_f32_16x16x32_bf16 v[60:63], a[128:131], a[188:191], v[60:63]\n"
    "ds_read_b128 a[244:247], v65 offset:2944\n"
    "v_mfma_f32_16x16x32_bf16 v[36:39], a[132:135], a[160:163], v[36:39]\n"
    "s_add_u32 s98, s98, 13631488\n"
    "ds_read_b128 a[248:251], v65 offset:3456\n"
    "v_mfma_f32_16x16x32_bf16 v[40:43], a[132:135], a[164:167], v[40:43]\n"
    "ds_read_b128 a[252:255], v65 offset:3968\n"
    "v_mfma_f32_16x16x32_bf16 v[44:47], a[132:135], a[168:171], v[44:47]\n"
    "ds_read_b64_tr_b16 a[196:197], v64 offset:584\n"
    "v_mfma_f32_16x16x32_bf16 v[48:51], a[132:135], a[172:175], v[48:51]\n"
    "s_add_u32 s68, s68, 4096\n"
    "ds_read_b64_tr_b16 a[198:199], v64 offset:576\n"
    "v_mfma_f32_16x16x32_bf16 v[52:55], a[132:135], a[176:179], v[52:55]\n"
    "ds_read_b64_tr_b16 a[200:201], v64 offset:1096\n"
    "v_mfma_f32_16x16x32_bf16 v[56:59], a[132:135], a[180:183], v[56:59]\n"
    "ds_read_b64_tr_b16 a[202:203], v64 offset:1088\n"
    "v_mfma_f32_16x16x32_bf16 v[60:63], a[132:135], a[184:187], v[60:63]\n"
    "s_cmp_eq_u32 s38, 1\n"
    "s_cselect_b32 s68, s74, s68\n"
    "s_cselect_b32 s73, s99, 0\n"
    "s_cbranch_scc0 NOPB_gated_mix__ssm_conv_h3__swiglu_ffn__prompt_kernel\n"
    "s_mov_b32 s29, 0x4000\n"
    "PU3_gated_mix__ssm_conv_h3__swiglu_ffn__prompt_kernel:\n"
    "buffer_load_dwordx2 v[26:27], v173, s[20:23], 0 offen sc1\n"
    "s_waitcnt vmcnt(0)\n"
    "v_cmp_ne_u64 vcc, s[70:71], v[26:27]\n"
    "s_cbranch_vccz PU3d_gated_mix__ssm_conv_h3__swiglu_ffn__prompt_kernel\n"
    "s_sleep 1\n"
    "s_sub_u32 s29, s29, 1\n"
    "s_cmp_lg_u32 s29, 0\n"
    "s_cbranch_scc1 PU3_gated_mix__ssm_conv_h3__swiglu_ffn__prompt_kernel\n"
    "PU3d_gated_mix__ssm_conv_h3__swiglu_ffn__prompt_kernel:\n"
    "NOPB_gated_mix__ssm_conv_h3__swiglu_ffn__prompt_kernel:\n"
    "ds_read_b64_tr_b16 a[204:205], v64 offset:1608\n"
    "v_mfma_f32_16x16x32_bf16 v[40:43], a[136:139], a[160:163], v[40:43]\n"
    "ds_read_b64_tr_b16 a[206:207], v64 offset:1600\n"
    "v_mfma_f32_16x16x32_bf16 v[44:47], a[136:139], a[164:167], v[44:47]\n"
    "ds_read_b64_tr_b16 a[208:209], v64 offset:2120\n"
    "v_mfma_f32_16x16x32_bf16 v[48:51], a[136:139], a[168:171], v[48:51]\n"
    "s_cmp_lg_u32 s38, 3\n"
    "s_cbranch_scc1 NOUF_gated_mix__ssm_conv_h3__swiglu_ffn__prompt_kernel\n"
    "s_barrier\n"
    "s_cmp_lg_u32 s34, 0\n"
    "s_cbranch_scc1 NOUF_gated_mix__ssm_conv_h3__swiglu_ffn__prompt_kernel\n"
    "s_sub_u32 s31, s39, 224\n"
    "s_lshl_b32 s31, s31, 7\n"
    "s_add_u32 s31, s31, 40964096\n"
    "v_mov_b32 v8, s70\n"
    "v_mov_b32 v9, s71\n"
    "v_mov_b32 v10, 0\n"
    "s_mov_b64 exec, 1\n"
    "buffer_store_dwordx2 v[8:9], v10, s[20:23], s31 offen sc1\n"
    "s_mov_b64 exec, -1\n"
    "NOUF_gated_mix__ssm_conv_h3__swiglu_ffn__prompt_kernel:\n"
    "ds_read_b64_tr_b16 a[210:211], v64 offset:2112\n"
    "v_mfma_f32_16x16x32_bf16 v[52:55], a[136:139], a[172:175], v[52:55]\n"
    "ds_read_b64_tr_b16 a[212:213], v64 offset:2632\n"
    "v_mfma_f32_16x16x32_bf16 v[56:59], a[136:139], a[176:179], v[56:59]\n"
    "ds_read_b64_tr_b16 a[214:215], v64 offset:2624\n"
    "v_mfma_f32_16x16x32_bf16 v[60:63], a[136:139], a[180:183], v[60:63]\n"
    "s_add_u32 s29, s68, 4096\n"
    "ds_read_b64_tr_b16 a[216:217], v64 offset:3144\n"
    "v_mfma_f32_16x16x32_bf16 v[44:47], a[140:143], a[160:163], v[44:47]\n"
    "ds_read_b64_tr_b16 a[218:219], v64 offset:3136\n"
    "v_mfma_f32_16x16x32_bf16 v[48:51], a[140:143], a[164:167], v[48:51]\n"
    "ds_read_b64_tr_b16 a[220:221], v64 offset:3656\n"
    "v_mfma_f32_16x16x32_bf16 v[52:55], a[140:143], a[168:171], v[52:55]\n"
    "s_cmp_eq_u32 s38, 2\n"
    "s_cselect_b32 s29, s74, s29\n"
    "ds_read_b64_tr_b16 a[222:223], v64 offset:3648\n"
    "v_mfma_f32_16x16x32_bf16 v[56:59], a[140:143], a[172:175], v[56:59]\n"
    "buffer_load_dwordx4 v[126:129], v160, s[80:83], s68 offen offset:0\n"
    "v_mfma_f32_16x16x32_bf16 v[60:63], a[140:143], a[176:179], v[60:63]\n"
    "buffer_load_dwordx2 v[124:125], v161, s[80:83], s68 offen offset:0\n"
    "v_mfma_f32_16x16x32_bf16 v[48:51], a[144:147], a[160:163], v[48:51]\n"
    "buffer_load_dwordx4 v[132:135], v160, s[80:83], s68 offen offset:1024\n"
    "v_mfma_f32_16x16x32_bf16 v[52:55], a[144:147], a[164:167], v[52:55]\n"
    "buffer_load_dwordx2 v[130:131], v160, s[80:83], s68 offen offset:1016\n"
    "v_mfma_f32_16x16x32_bf16 v[56:59], a[144:147], a[168:171], v[56:59]\n"
    "buffer_load_dwordx4 v[138:141], v160, s[80:83], s68 offen offset:2048\n"
    "v_mfma_f32_16x16x32_bf16 v[60:63], a[144:147], a[172:175], v[60:63]\n"
    "buffer_load_dwordx2 v[136:137], v160, s[80:83], s68 offen offset:2040\n"
    "v_mfma_f32_16x16x32_bf16 v[52:55], a[148:151], a[160:163], v[52:55]\n"
    "buffer_load_dwordx4 v[144:147], v160, s[80:83], s68 offen offset:3072\n"
    "v_mfma_f32_16x16x32_bf16 v[56:59], a[148:151], a[164:167], v[56:59]\n"
    "buffer_load_dwordx2 v[142:143], v160, s[80:83], s68 offen offset:3064\n"
    "v_mfma_f32_16x16x32_bf16 v[60:63], a[148:151], a[168:171], v[60:63]\n"
    "v_mfma_f32_16x16x32_bf16 v[56:59], a[152:155], a[160:163], v[56:59]\n"
    "v_mfma_f32_16x16x32_bf16 v[60:63], a[152:155], a[164:167], v[60:63]\n"
    "v_mfma_f32_16x16x32_bf16 v[60:63], a[156:159], a[160:163], v[60:63]\n"
    "s_waitcnt lgkmcnt(0)\n"
    "v_mfma_f32_16x16x32_bf16 v[32:35], a[192:195], a[224:227], v[32:35]\n"
    "ds_read_b64_tr_b16 a[128:129], v64 offset:136\n"
    "v_mfma_f32_16x16x32_bf16 v[36:39], a[192:195], a[228:231], v[36:39]\n"
    "ds_read_b64_tr_b16 a[130:131], v64 offset:128\n"
    "v_mfma_f32_16x16x32_bf16 v[40:43], a[192:195], a[232:235], v[40:43]\n"
    "ds_read_b128 a[160:163], v65 offset:320\n"
    "v_mfma_f32_16x16x32_bf16 v[44:47], a[192:195], a[236:239], v[44:47]\n"
    "ds_read_b128 a[164:167], v65 offset:832\n"
    "v_mfma_f32_16x16x32_bf16 v[48:51], a[192:195], a[240:243], v[48:51]\n"
    "ds_read_b128 a[168:171], v65 offset:1344\n"
    "v_mfma_f32_16x16x32_bf16 v[52:55], a[192:195], a[244:247], v[52:55]\n"
    "ds_read_b128 a[172:175], v65 offset:1856\n"
    "v_mfma_f32_16x16x32_bf16 v[56:59], a[192:195], a[248:251], v[56:59]\n"
    "ds_read_b128 a[176:179], v65 offset:2368\n"
    "v_mfma_f32_16x16x32_bf16 v[60:63], a[192:195], a[252:255], v[60:63]\n"
    "ds_read_b128 a[180:183], v65 offset:2880\n"
    "v_mfma_f32_16x16x32_bf16 v[36:39], a[196:199], a[224:227], v[36:39]\n"
    "ds_read_b128 a[184:187], v65 offset:3392\n"
    "v_mfma_f32_16x16x32_bf16 v[40:43], a[196:199], a[228:231], v[40:43]\n"
    "ds_read_b128 a[188:191], v65 offset:3904\n"
    "v_mfma_f32_16x16x32_bf16 v[44:47], a[196:199], a[232:235], v[44:47]\n"
    "ds_read_b64_tr_b16 a[132:133], v64 offset:648\n"
    "v_mfma_f32_16x16x32_bf16 v[48:51], a[196:199], a[236:239], v[48:51]\n"
    "ds_read_b64_tr_b16 a[134:135], v64 offset:640\n"
    "v_mfma_f32_16x16x32_bf16 v[52:55], a[196:199], a[240:243], v[52:55]\n"
    "ds_read_b64_tr_b16 a[136:137], v64 offset:1160\n"
    "v_mfma_f32_16x16x32_bf16 v[56:59], a[196:199], a[244:247], v[56:59]\n"
    "ds_read_b64_tr_b16 a[138:139], v64 offset:1152\n"
    "v_mfma_f32_16x16x32_bf16 v[60:63], a[196:199], a[248:251], v[60:63]\n"
    "ds_read_b64_tr_b16 a[140:141], v64 offset:1672\n"
    "v_mfma_f32_16x16x32_bf16 v[40:43], a[200:203], a[224:227], v[40:43]\n"
    "ds_read_b64_tr_b16 a[142:143], v64 offset:1664\n"
    "v_mfma_f32_16x16x32_bf16 v[44:47], a[200:203], a[228:231], v[44:47]\n"
    "ds_read_b64_tr_b16 a[144:145], v64 offset:2184\n"
    "v_mfma_f32_16x16x32_bf16 v[48:51], a[200:203], a[232:235], v[48:51]\n"
    "ds_read_b64_tr_b16 a[146:147], v64 offset:2176\n"
    "v_mfma_f32_16x16x32_bf16 v[52:55], a[200:203], a[236:239], v[52:55]\n"
    "ds_read_b64_tr_b16 a[148:149], v64 offset:2696\n"
    "v_mfma_f32_16x16x32_bf16 v[56:59], a[200:203], a[240:243], v[56:59]\n"
    "ds_read_b64_tr_b16 a[150:151], v64 offset:2688\n"
    "v_mfma_f32_16x16x32_bf16 v[60:63], a[200:203], a[244:247], v[60:63]\n"
    "ds_read_b64_tr_b16 a[152:153], v64 offset:3208\n"
    "v_mfma_f32_16x16x32_bf16 v[44:47], a[204:207], a[224:227], v[44:47]\n"
    "ds_read_b64_tr_b16 a[154:155], v64 offset:3200\n"
    "v_mfma_f32_16x16x32_bf16 v[48:51], a[204:207], a[228:231], v[48:51]\n"
    "ds_read_b64_tr_b16 a[156:157], v64 offset:3720\n"
    "v_mfma_f32_16x16x32_bf16 v[52:55], a[204:207], a[232:235], v[52:55]\n"
    "ds_read_b64_tr_b16 a[158:159], v64 offset:3712\n"
    "v_mfma_f32_16x16x32_bf16 v[56:59], a[204:207], a[236:239], v[56:59]\n"
    "s_add_u32 m0, s27, 32\n"
    "v_mfma_f32_16x16x32_bf16 v[60:63], a[204:207], a[240:243], v[60:63]\n"
    "v_mfma_f32_16x16x32_bf16 v[48:51], a[208:211], a[224:227], v[48:51]\n"
    "buffer_load_dwordx4 v160, s[80:83], s68 offen offset:0 lds\n"
    "v_mfma_f32_16x16x32_bf16 v[52:55], a[208:211], a[228:231], v[52:55]\n"
    "buffer_load_dwordx4 v160, s[80:83], s68 offen offset:1024 lds\n"
    "v_mfma_f32_16x16x32_bf16 v[56:59], a[208:211], a[232:235], v[56:59]\n"
    "buffer_load_dwordx4 v160, s[80:83], s68 offen offset:2048 lds\n"
    "v_mfma_f32_16x16x32_bf16 v[60:63], a[208:211], a[236:239], v[60:63]\n"
    "buffer_load_dwordx4 v160, s[80:83], s68 offen offset:3072 lds\n"
    "s_add_u32 m0, s27, 8356\n"
    "v_mfma_f32_16x16x32_bf16 v[52:55], a[212:215], a[224:227], v[52:55]\n"
    "v_mfma_f32_16x16x32_bf16 v[56:59], a[212:215], a[228:231], v[56:59]\n"
    "buffer_load_dwordx4 v160, s[80:83], s68 offen offset:1020 lds\n"
    "v_mfma_f32_16x16x32_bf16 v[60:63], a[212:215], a[232:235], v[60:63]\n"
    "buffer_load_dwordx4 v160, s[80:83], s68 offen offset:2044 lds\n"
    "v_mfma_f32_16x16x32_bf16 v[56:59], a[216:219], a[224:227], v[56:59]\n"
    "buffer_load_dwordx4 v160, s[80:83], s68 offen offset:3068 lds\n"
    "v_mfma_f32_16x16x32_bf16 v[60:63], a[216:219], a[228:231], v[60:63]\n"
    "v_mfma_f32_16x16x32_bf16 v[60:63], a[220:223], a[224:227], v[60:63]\n"
    "s_waitcnt lgkmcnt(0)\n"
    "v_mfma_f32_16x16x32_bf16 v[32:35], a[128:131], a[160:163], v[32:35]\n"
    "ds_read_b64_tr_b16 a[192:193], v64 offset:200\n"
    "v_mfma_f32_16x16x32_bf16 v[36:39], a[128:131], a[164:167], v[36:39]\n"
    "ds_read_b64_tr_b16 a[194:195], v64 offset:192\n"
    "v_mfma_f32_16x16x32_bf16 v[40:43], a[128:131], a[168:171], v[40:43]\n"
    "ds_read_b128 a[224:227], v65 offset:256\n"
    "v_mfma_f32_16x16x32_bf16 v[44:47], a[128:131], a[172:175], v[44:47]\n"
    "ds_read_b128 a[228:231], v65 offset:768\n"
    "v_mfma_f32_16x16x32_bf16 v[48:51], a[128:131], a[176:179], v[48:51]\n"
    "ds_read_b128 a[232:235], v65 offset:1280\n"
    "v_mfma_f32_16x16x32_bf16 v[52:55], a[128:131], a[180:183], v[52:55]\n"
    "ds_read_b128 a[236:239], v65 offset:1792\n"
    "v_mfma_f32_16x16x32_bf16 v[56:59], a[128:131], a[184:187], v[56:59]\n"
    "ds_read_b128 a[240:243], v65 offset:2304\n"
    "v_mfma_f32_16x16x32_bf16 v[60:63], a[128:131], a[188:191], v[60:63]\n"
    "ds_read_b128 a[244:247], v65 offset:2816\n"
    "v_mfma_f32_16x16x32_bf16 v[36:39], a[132:135], a[160:163], v[36:39]\n"
    "ds_read_b128 a[248:251], v65 offset:3328\n"
    "v_mfma_f32_16x16x32_bf16 v[40:43], a[132:135], a[164:167], v[40:43]\n"
    "ds_read_b128 a[252:255], v65 offset:3840\n"
    "v_mfma_f32_16x16x32_bf16 v[44:47], a[132:135], a[168:171], v[44:47]\n"
    "ds_read_b64_tr_b16 a[196:197], v64 offset:712\n"
    "v_mfma_f32_16x16x32_bf16 v[48:51], a[132:135], a[172:175], v[48:51]\n"
    "ds_read_b64_tr_b16 a[198:199], v64 offset:704\n"
    "v_mfma_f32_16x16x32_bf16 v[52:55], a[132:135], a[176:179], v[52:55]\n"
    "ds_read_b64_tr_b16 a[200:201], v64 offset:1224\n"
    "v_mfma_f32_16x16x32_bf16 v[56:59], a[132:135], a[180:183], v[56:59]\n"
    "ds_read_b64_tr_b16 a[202:203], v64 offset:1216\n"
    "v_mfma_f32_16x16x32_bf16 v[60:63], a[132:135], a[184:187], v[60:63]\n"
    "ds_read_b64_tr_b16 a[204:205], v64 offset:1736\n"
    "v_mfma_f32_16x16x32_bf16 v[40:43], a[136:139], a[160:163], v[40:43]\n"
    "ds_read_b64_tr_b16 a[206:207], v64 offset:1728\n"
    "v_mfma_f32_16x16x32_bf16 v[44:47], a[136:139], a[164:167], v[44:47]\n"
    "ds_read_b64_tr_b16 a[208:209], v64 offset:2248\n"
    "v_mfma_f32_16x16x32_bf16 v[48:51], a[136:139], a[168:171], v[48:51]\n"
    "ds_read_b64_tr_b16 a[210:211], v64 offset:2240\n"
    "v_mfma_f32_16x16x32_bf16 v[52:55], a[136:139], a[172:175], v[52:55]\n"
    "ds_read_b64_tr_b16 a[212:213], v64 offset:2760\n"
    "v_mfma_f32_16x16x32_bf16 v[56:59], a[136:139], a[176:179], v[56:59]\n"
    "ds_read_b64_tr_b16 a[214:215], v64 offset:2752\n"
    "v_mfma_f32_16x16x32_bf16 v[60:63], a[136:139], a[180:183], v[60:63]\n"
    "ds_read_b64_tr_b16 a[216:217], v64 offset:3272\n"
    "v_mfma_f32_16x16x32_bf16 v[44:47], a[140:143], a[160:163], v[44:47]\n"
    "ds_read_b64_tr_b16 a[218:219], v64 offset:3264\n"
    "v_mfma_f32_16x16x32_bf16 v[48:51], a[140:143], a[164:167], v[48:51]\n"
    "ds_read_b64_tr_b16 a[220:221], v64 offset:3784\n"
    "v_mfma_f32_16x16x32_bf16 v[52:55], a[140:143], a[168:171], v[52:55]\n"
    "ds_read_b64_tr_b16 a[222:223], v64 offset:3776\n"
    "v_mfma_f32_16x16x32_bf16 v[56:59], a[140:143], a[172:175], v[56:59]\n"
    "s_lshr_b32 s69, s68, 12\n"
    "s_mul_i32 s69, s69, 4224\n"
    "s_add_u32 s69, s69, 18874368\n"
    "v_mfma_f32_16x16x32_bf16 v[60:63], a[140:143], a[176:179], v[60:63]\n"
    "buffer_load_dwordx4 v[192:195], v160, s[20:23], s69 offen offset:0 sc1\n"
    "v_mfma_f32_16x16x32_bf16 v[48:51], a[144:147], a[160:163], v[48:51]\n"
    "v_mfma_f32_16x16x32_bf16 v[52:55], a[144:147], a[164:167], v[52:55]\n"
    "buffer_load_dwordx4 v[196:199], v160, s[20:23], s69 offen offset:1024 sc1\n"
    "v_mfma_f32_16x16x32_bf16 v[56:59], a[144:147], a[168:171], v[56:59]\n"
    "v_mfma_f32_16x16x32_bf16 v[60:63], a[144:147], a[172:175], v[60:63]\n"
    "buffer_load_dwordx4 v[200:203], v160, s[20:23], s69 offen offset:2048 sc1\n"
    "v_mfma_f32_16x16x32_bf16 v[52:55], a[148:151], a[160:163], v[52:55]\n"
    "v_mfma_f32_16x16x32_bf16 v[56:59], a[148:151], a[164:167], v[56:59]\n"
    "buffer_load_dwordx4 v[204:207], v160, s[20:23], s69 offen offset:3072 sc1\n"
    "v_mfma_f32_16x16x32_bf16 v[60:63], a[148:151], a[168:171], v[60:63]\n"
    "v_mfma_f32_16x16x32_bf16 v[56:59], a[152:155], a[160:163], v[56:59]\n"
    "buffer_load_dword a50, v174, s[80:83], s29 offen\n"
    "v_mfma_f32_16x16x32_bf16 v[60:63], a[152:155], a[164:167], v[60:63]\n"
    "v_add_u32 v177, s27, v160\n"
    "v_mfma_f32_16x16x32_bf16 v[60:63], a[156:159], a[160:163], v[60:63]\n"
    "s_waitcnt lgkmcnt(0)\n"
    "v_mfma_f32_16x16x32_bf16 v[32:35], a[192:195], a[224:227], v[32:35]\n"
    "ds_read_b64_tr_b16 a[128:129], v64 offset:264\n"
    "v_mfma_f32_16x16x32_bf16 v[36:39], a[192:195], a[228:231], v[36:39]\n"
    "ds_read_b64_tr_b16 a[130:131], v64 offset:256\n"
    "v_mfma_f32_16x16x32_bf16 v[40:43], a[192:195], a[232:235], v[40:43]\n"
    "ds_read_b128 a[160:163], v65 offset:192\n"
    "v_mfma_f32_16x16x32_bf16 v[44:47], a[192:195], a[236:239], v[44:47]\n"
    "ds_read_b128 a[164:167], v65 offset:704\n"
    "v_mfma_f32_16x16x32_bf16 v[48:51], a[192:195], a[240:243], v[48:51]\n"
    "ds_read_b128 a[168:171], v65 offset:1216\n"
    "v_mfma_f32_16x16x32_bf16 v[52:55], a[192:195], a[244:247], v[52:55]\n"
    "ds_read_b128 a[172:175], v65 offset:1728\n"
    "v_mfma_f32_16x16x32_bf16 v[56:59], a[192:195], a[248:251], v[56:59]\n"
    "ds_read_b128 a[176:179], v65 offset:2240\n"
    "v_mfma_f32_16x16x32_bf16 v[60:63], a[192:195], a[252:255], v[60:63]\n"
    "ds_read_b128 a[180:183], v65 offset:2752\n"
    "v_mfma_f32_16x16x32_bf16 v[36:39], a[196:199], a[224:227], v[36:39]\n"
    "ds_read_b128 a[184:187], v65 offset:3264\n"
    "v_mfma_f32_16x16x32_bf16 v[40:43], a[196:199], a[228:231], v[40:43]\n"
    "ds_read_b128 a[188:191], v65 offset:3776\n"
    "v_mfma_f32_16x16x32_bf16 v[44:47], a[196:199], a[232:235], v[44:47]\n"
    "ds_read_b64_tr_b16 a[132:133], v64 offset:776\n"
    "v_mfma_f32_16x16x32_bf16 v[48:51], a[196:199], a[236:239], v[48:51]\n"
    "ds_read_b64_tr_b16 a[134:135], v64 offset:768\n"
    "v_mfma_f32_16x16x32_bf16 v[52:55], a[196:199], a[240:243], v[52:55]\n"
    "ds_read_b64_tr_b16 a[136:137], v64 offset:1288\n"
    "v_mfma_f32_16x16x32_bf16 v[56:59], a[196:199], a[244:247], v[56:59]\n"
    "ds_read_b64_tr_b16 a[138:139], v64 offset:1280\n"
    "v_mfma_f32_16x16x32_bf16 v[60:63], a[196:199], a[248:251], v[60:63]\n"
    "ds_read_b64_tr_b16 a[140:141], v64 offset:1800\n"
    "v_mfma_f32_16x16x32_bf16 v[40:43], a[200:203], a[224:227], v[40:43]\n"
    "ds_read_b64_tr_b16 a[142:143], v64 offset:1792\n"
    "v_mfma_f32_16x16x32_bf16 v[44:47], a[200:203], a[228:231], v[44:47]\n"
    "ds_read_b64_tr_b16 a[144:145], v64 offset:2312\n"
    "v_mfma_f32_16x16x32_bf16 v[48:51], a[200:203], a[232:235], v[48:51]\n"
    "ds_read_b64_tr_b16 a[146:147], v64 offset:2304\n"
    "v_mfma_f32_16x16x32_bf16 v[52:55], a[200:203], a[236:239], v[52:55]\n"
    "ds_read_b64_tr_b16 a[148:149], v64 offset:2824\n"
    "v_mfma_f32_16x16x32_bf16 v[56:59], a[200:203], a[240:243], v[56:59]\n"
    "ds_read_b64_tr_b16 a[150:151], v64 offset:2816\n"
    "v_mfma_f32_16x16x32_bf16 v[60:63], a[200:203], a[244:247], v[60:63]\n"
    "ds_read_b64_tr_b16 a[152:153], v64 offset:3336\n"
    "v_mfma_f32_16x16x32_bf16 v[44:47], a[204:207], a[224:227], v[44:47]\n"
    "ds_read_b64_tr_b16 a[154:155], v64 offset:3328\n"
    "v_mfma_f32_16x16x32_bf16 v[48:51], a[204:207], a[228:231], v[48:51]\n"
    "s_waitcnt vmcnt(12)\n"
    "ds_read_b64_tr_b16 a[156:157], v64 offset:3848\n"
    "v_mfma_f32_16x16x32_bf16 v[52:55], a[204:207], a[232:235], v[52:55]\n"
    "ds_read_b64_tr_b16 a[158:159], v64 offset:3840\n"
    "v_mfma_f32_16x16x32_bf16 v[56:59], a[204:207], a[236:239], v[56:59]\n"
    "v_mov_b32 v228, v125\n"
    "v_mfma_f32_16x16x32_bf16 v[60:63], a[204:207], a[240:243], v[60:63]\n"
    "v_mov_b32 v229, v126\n"
    "v_mfma_f32_16x16x32_bf16 v[48:51], a[208:211], a[224:227], v[48:51]\n"
    "v_mov_b32 v230, v127\n"
    "v_mfma_f32_16x16x32_bf16 v[52:55], a[208:211], a[228:231], v[52:55]\n"
    "v_mov_b32 v231, v128\n"
    "v_mfma_f32_16x16x32_bf16 v[56:59], a[208:211], a[232:235], v[56:59]\n"
    "ds_write_b128 v177, v[228:231] offset:8352\n"
    "v_mfma_f32_16x16x32_bf16 v[60:63], a[208:211], a[236:239], v[60:63]\n"
    "v_alignbit_b32 v232, v126, v125, 16\n"
    "v_mfma_f32_16x16x32_bf16 v[52:55], a[212:215], a[224:227], v[52:55]\n"
    "v_alignbit_b32 v233, v127, v126, 16\n"
    "v_mfma_f32_16x16x32_bf16 v[56:59], a[212:215], a[228:231], v[56:59]\n"
    "v_alignbit_b32 v234, v128, v127, 16\n"
    "v_mfma_f32_16x16x32_bf16 v[60:63], a[212:215], a[232:235], v[60:63]\n"
    "v_alignbit_b32 v235, v129, v128, 16\n"
    "v_mfma_f32_16x16x32_bf16 v[56:59], a[216:219], a[224:227], v[56:59]\n"
    "ds_write_b128 v177, v[232:235] offset:4192\n"
    "v_mfma_f32_16x16x32_bf16 v[60:63], a[216:219], a[228:231], v[60:63]\n"
    "v_alignbit_b32 v236, v125, v124, 16\n"
    "v_mfma_f32_16x16x32_bf16 v[60:63], a[220:223], a[224:227], v[60:63]\n"
    "v_alignbit_b32 v237, v126, v125, 16\n"
    "s_waitcnt lgkmcnt(2)\n"
    "v_mfma_f32_16x16x32_bf16 v[32:35], a[128:131], a[160:163], v[32:35]\n"
    "ds_read_b64_tr_b16 a[192:193], v64 offset:328\n"
    "v_mfma_f32_16x16x32_bf16 v[36:39], a[128:131], a[164:167], v[36:39]\n"
    "v_permlane16_swap_b32 v212, v214\n"
    "ds_read_b64_tr_b16 a[194:195], v64 offset:320\n"
    "v_mfma_f32_16x16x32_bf16 v[40:43], a[128:131], a[168:171], v[40:43]\n"
    "ds_read_b128 a[224:227], v65 offset:128\n"
    "v_mfma_f32_16x16x32_bf16 v[44:47], a[128:131], a[172:175], v[44:47]\n"
    "v_permlane16_swap_b32 v213, v215\n"
    "ds_read_b128 a[228:231], v65 offset:640\n"
    "v_mfma_f32_16x16x32_bf16 v[48:51], a[128:131], a[176:179], v[48:51]\n"
    "ds_read_b128 a[232:235], v65 offset:1152\n"
    "v_mfma_f32_16x16x32_bf16 v[52:55], a[128:131], a[180:183], v[52:55]\n"
    "buffer_store_dwordx4 v[212:215], v172, s[20:23], s24 offen offset:0 sc1\n"
    "ds_read_b128 a[236:239], v65 offset:1664\n"
    "v_mfma_f32_16x16x32_bf16 v[56:59], a[128:131], a[184:187], v[56:59]\n"
    "ds_read_b128 a[240:243], v65 offset:2176\n"
    "v_mfma_f32_16x16x32_bf16 v[60:63], a[128:131], a[188:191], v[60:63]\n"
    "v_permlane16_swap_b32 v216, v218\n"
    "ds_read_b128 a[244:247], v65 offset:2688\n"
    "v_mfma_f32_16x16x32_bf16 v[36:39], a[132:135], a[160:163], v[36:39]\n"
    "ds_read_b128 a[248:251], v65 offset:3200\n"
    "v_mfma_f32_16x16x32_bf16 v[40:43], a[132:135], a[164:167], v[40:43]\n"
    "v_permlane16_swap_b32 v217, v219\n"
    "ds_read_b128 a[252:255], v65 offset:3712\n"
    "v_mfma_f32_16x16x32_bf16 v[44:47], a[132:135], a[168:171], v[44:47]\n"
    "ds_read_b64_tr_b16 a[196:197], v64 offset:840\n"
    "v_mfma_f32_16x16x32_bf16 v[48:51], a[132:135], a[172:175], v[48:51]\n"
    "buffer_store_dwordx4 v[216:219], v172, s[20:23], s24 offen offset:1024 sc1\n"
    "ds_read_b64_tr_b16 a[198:199], v64 offset:832\n"
    "v_mfma_f32_16x16x32_bf16 v[52:55], a[132:135], a[176:179], v[52:55]\n"
    "ds_read_b64_tr_b16 a[200:201], v64 offset:1352\n"
    "v_mfma_f32_16x16x32_bf16 v[56:59], a[132:135], a[180:183], v[56:59]\n"
    "v_permlane16_swap_b32 v220, v222\n"
    "ds_read_b64_tr_b16 a[202:203], v64 offset:1344\n"
    "v_mfma_f32_16x16x32_bf16 v[60:63], a[132:135], a[184:187], v[60:63]\n"
    "ds_read_b64_tr_b16 a[204:205], v64 offset:1864\n"
    "v_mfma_f32_16x16x32_bf16 v[40:43], a[136:139], a[160:163], v[40:43]\n"
    "v_permlane16_swap_b32 v221, v223\n"
    "ds_read_b64_tr_b16 a[206:207], v64 offset:1856\n"
    "v_mfma_f32_16x16x32_bf16 v[44:47], a[136:139], a[164:167], v[44:47]\n"
    "ds_read_b64_tr_b16 a[208:209], v64 offset:2376\n"
    "v_mfma_f32_16x16x32_bf16 v[48:51], a[136:139], a[168:171], v[48:51]\n"
    "buffer_store_dwordx4 v[220:223], v172, s[20:23], s24 offen offset:2048 sc1\n"
    "ds_read_b64_tr_b16 a[210:211], v64 offset:2368\n"
    "v_mfma_f32_16x16x32_bf16 v[52:55], a[136:139], a[172:175], v[52:55]\n"
    "ds_read_b64_tr_b16 a[212:213], v64 offset:2888\n"
    "v_mfma_f32_16x16x32_bf16 v[56:59], a[136:139], a[176:179], v[56:59]\n"
    "v_permlane16_swap_b32 v224, v226\n"
    "ds_read_b64_tr_b16 a[214:215], v64 offset:2880\n"
    "v_mfma_f32_16x16x32_bf16 v[60:63], a[136:139], a[180:183], v[60:63]\n"
    "ds_read_b64_tr_b16 a[216:217], v64 offset:3400\n"
    "v_mfma_f32_16x16x32_bf16 v[44:47], a[140:143], a[160:163], v[44:47]\n"
    "v_permlane16_swap_b32 v225, v227\n"
    "ds_read_b64_tr_b16 a[218:219], v64 offset:3392\n"
    "v_mfma_f32_16x16x32_bf16 v[48:51], a[140:143], a[164:167], v[48:51]\n"
    "ds_read_b64_tr_b16 a[220:221], v64 offset:3912\n"
    "v_mfma_f32_16x16x32_bf16 v[52:55], a[140:143], a[168:171], v[52:55]\n"
    "buffer_store_dwordx4 v[224:227], v172, s[20:23], s24 offen offset:3072 sc1\n"
    "ds_read_b64_tr_b16 a[222:223], v64 offset:3904\n"
    "v_mfma_f32_16x16x32_bf16 v[56:59], a[140:143], a[172:175], v[56:59]\n"
    "v_alignbit_b32 v238, v127, v126, 16\n"
    "v_mfma_f32_16x16x32_bf16 v[60:63], a[140:143], a[176:179], v[60:63]\n"
    "s_mov_b32 s24, s98\n"
    "v_alignbit_b32 v239, v128, v127, 16\n"
    "v_mfma_f32_16x16x32_bf16 v[48:51], a[144:147], a[160:163], v[48:51]\n"
    "ds_write_b128 v177, v[236:239] offset:12512\n"
    "v_mfma_f32_16x16x32_bf16 v[52:55], a[144:147], a[164:167], v[52:55]\n"
    "v_alignbit_b32 v240, v132, v131, 16\n"
    "v_mfma_f32_16x16x32_bf16 v[56:59], a[144:147], a[168:171], v[56:59]\n"
    "v_alignbit_b32 v241, v133, v132, 16\n"
    "v_mfma_f32_16x16x32_bf16 v[60:63], a[144:147], a[172:175], v[60:63]\n"
    "v_alignbit_b32 v242, v134, v133, 16\n"
    "v_mfma_f32_16x16x32_bf16 v[52:55], a[148:151], a[160:163], v[52:55]\n"
    "v_alignbit_b32 v243, v135, v134, 16\n"
    "v_mfma_f32_16x16x32_bf16 v[56:59], a[148:151], a[164:167], v[56:59]\n"
    "ds_write_b128 v177, v[240:243] offset:5216\n"
    "v_mfma_f32_16x16x32_bf16 v[60:63], a[148:151], a[168:171], v[60:63]\n"
    "v_alignbit_b32 v244, v131, v130, 16\n"
    "v_mfma_f32_16x16x32_bf16 v[56:59], a[152:155], a[160:163], v[56:59]\n"
    "v_alignbit_b32 v245, v132, v131, 16\n"
    "v_mfma_f32_16x16x32_bf16 v[60:63], a[152:155], a[164:167], v[60:63]\n"
    "v_alignbit_b32 v246, v133, v132, 16\n"
    "v_mfma_f32_16x16x32_bf16 v[60:63], a[156:159], a[160:163], v[60:63]\n"
    "v_alignbit_b32 v247, v134, v133, 16\n"
    "s_waitcnt lgkmcnt(2)\n"
    "v_mfma_f32_16x16x32_bf16 v[32:35], a[192:195], a[224:227], v[32:35]\n"
    "ds_read_b64_tr_b16 a[128:129], v64 offset:392\n"
    "v_mfma_f32_16x16x32_bf16 v[36:39], a[192:195], a[228:231], v[36:39]\n"
    "ds_read_b64_tr_b16 a[130:131], v64 offset:384\n"
    "v_mfma_f32_16x16x32_bf16 v[40:43], a[192:195], a[232:235], v[40:43]\n"
    "ds_read_b128 a[160:163], v65 offset:64\n"
    "v_mfma_f32_16x16x32_bf16 v[44:47], a[192:195], a[236:239], v[44:47]\n"
    "ds_read_b128 a[164:167], v65 offset:576\n"
    "v_mfma_f32_16x16x32_bf16 v[48:51], a[192:195], a[240:243], v[48:51]\n"
    "ds_read_b128 a[168:171], v65 offset:1088\n"
    "v_mfma_f32_16x16x32_bf16 v[52:55], a[192:195], a[244:247], v[52:55]\n"
    "ds_read_b128 a[172:175], v65 offset:1600\n"
    "v_mfma_f32_16x16x32_bf16 v[56:59], a[192:195], a[248:251], v[56:59]\n"
    "ds_read_b128 a[176:179], v65 offset:2112\n"
    "v_mfma_f32_16x16x32_bf16 v[60:63], a[192:195], a[252:255], v[60:63]\n"
    "ds_read_b128 a[180:183], v65 offset:2624\n"
    "v_mfma_f32_16x16x32_bf16 v[36:39], a[196:199], a[224:227], v[36:39]\n"
    "ds_read_b128 a[184:187], v65 offset:3136\n"
    "v_mfma_f32_16x16x32_bf16 v[40:43], a[196:199], a[228:231], v[40:43]\n"
    "ds_read_b128 a[188:191], v65 offset:3648\n"
    "v_mfma_f32_16x16x32_bf16 v[44:47], a[196:199], a[232:235], v[44:47]\n"
    "ds_read_b64_tr_b16 a[132:133], v64 offset:904\n"
    "v_mfma_f32_16x16x32_bf16 v[48:51], a[196:199], a[236:239], v[48:51]\n"
    "ds_read_b64_tr_b16 a[134:135], v64 offset:896\n"
    "v_mfma_f32_16x16x32_bf16 v[52:55], a[196:199], a[240:243], v[52:55]\n"
    "ds_read_b64_tr_b16 a[136:137], v64 offset:1416\n"
    "v_mfma_f32_16x16x32_bf16 v[56:59], a[196:199], a[244:247], v[56:59]\n"
    "ds_read_b64_tr_b16 a[138:139], v64 offset:1408\n"
    "v_mfma_f32_16x16x32_bf16 v[60:63], a[196:199], a[248:251], v[60:63]\n"
    "ds_read_b64_tr_b16 a[140:141], v64 offset:1928\n"
    "v_mfma_f32_16x16x32_bf16 v[40:43], a[200:203], a[224:227], v[40:43]\n"
    "ds_read_b64_tr_b16 a[142:143], v64 offset:1920\n"
    "v_mfma_f32_16x16x32_bf16 v[44:47], a[200:203], a[228:231], v[44:47]\n"
    "ds_read_b64_tr_b16 a[144:145], v64 offset:2440\n"
    "v_mfma_f32_16x16x32_bf16 v[48:51], a[200:203], a[232:235], v[48:51]\n"
    "ds_read_b64_tr_b16 a[146:147], v64 offset:2432\n"
    "v_mfma_f32_16x16x32_bf16 v[52:55], a[200:203], a[236:239], v[52:55]\n"
    "ds_read_b64_tr_b16 a[148:149], v64 offset:2952\n"
    "v_mfma_f32_16x16x32_bf16 v[56:59], a[200:203], a[240:243], v[56:59]\n"
    "ds_read_b64_tr_b16 a[150:151], v64 offset:2944\n"
    "v_mfma_f32_16x16x32_bf16 v[60:63], a[200:203], a[244:247], v[60:63]\n"
    "ds_read_b64_tr_b16 a[152:153], v64 offset:3464\n"
    "v_mfma_f32_16x16x32_bf16 v[44:47], a[204:207], a[224:227], v[44:47]\n"
    "ds_read_b64_tr_b16 a[154:155], v64 offset:3456\n"
    "v_mfma_f32_16x16x32_bf16 v[48:51], a[204:207], a[228:231], v[48:51]\n"
    "ds_read_b64_tr_b16 a[156:157], v64 offset:3976\n"
    "v_mfma_f32_16x16x32_bf16 v[52:55], a[204:207], a[232:235], v[52:55]\n"
    "ds_read_b64_tr_b16 a[158:159], v64 offset:3968\n"
    "v_mfma_f32_16x16x32_bf16 v[56:59], a[204:207], a[236:239], v[56:59]\n"
    "ds_write_b128 v177, v[244:247] offset:13536\n"
    "v_mfma_f32_16x16x32_bf16 v[60:63], a[204:207], a[240:243], v[60:63]\n"
    "v_alignbit_b32 v248, v138, v137, 16\n"
    "v_mfma_f32_16x16x32_bf16 v[48:51], a[208:211], a[224:227], v[48:51]\n"
    "v_alignbit_b32 v249, v139, v138, 16\n"
    "v_mfma_f32_16x16x32_bf16 v[52:55], a[208:211], a[228:231], v[52:55]\n"
    "v_alignbit_b32 v250, v140, v139, 16\n"
    "v_mfma_f32_16x16x32_bf16 v[56:59], a[208:211], a[232:235], v[56:59]\n"
    "v_alignbit_b32 v251, v141, v140, 16\n"
    "v_mfma_f32_16x16x32_bf16 v[60:63], a[208:211], a[236:239], v[60:63]\n"
    "ds_write_b128 v177, v[248:251] offset:6240\n"
    "v_mfma_f32_16x16x32_bf16 v[52:55], a[212:215], a[224:227], v[52:55]\n"
    "v_alignbit_b32 v228, v137, v136, 16\n"
    "v_mfma_f32_16x16x32_bf16 v[56:59], a[212:215], a[228:231], v[56:59]\n"
    "v_alignbit_b32 v229, v138, v137, 16\n"
    "v_mfma_f32_16x16x32_bf16 v[60:63], a[212:215], a[232:235], v[60:63]\n"
    "v_alignbit_b32 v230, v139, v138, 16\n"
    "v_mfma_f32_16x16x32_bf16 v[56:59], a[216:219], a[224:227], v[56:59]\n"
    "v_alignbit_b32 v231, v140, v139, 16\n"
    "v_mfma_f32_16x16x32_bf16 v[60:63], a[216:219], a[228:231], v[60:63]\n"
    "ds_write_b128 v177, v[228:231] offset:14560\n"
    "v_mfma_f32_16x16x32_bf16 v[60:63], a[220:223], a[224:227], v[60:63]\n"
    "v_alignbit_b32 v232, v144, v143, 16\n"
    "s_waitcnt lgkmcnt(3)\n"
    "v_mfma_f32_16x16x32_bf16 v[32:35], a[128:131], a[160:163], v[32:35]\n"
    "ds_read_b64_tr_b16 a[192:193], v64 offset:456\n"
    "v_mfma_f32_16x16x32_bf16 v[36:39], a[128:131], a[164:167], v[36:39]\n"
    "ds_read_b64_tr_b16 a[194:195], v64 offset:448\n"
    "v_mfma_f32_16x16x32_bf16 v[40:43], a[128:131], a[168:171], v[40:43]\n"
    "ds_read_b128 a[224:227], v65 offset:0\n"
    "v_mfma_f32_16x16x32_bf16 v[44:47], a[128:131], a[172:175], v[44:47]\n"
    "ds_read_b128 a[228:231], v65 offset:512\n"
    "v_mfma_f32_16x16x32_bf16 v[48:51], a[128:131], a[176:179], v[48:51]\n"
    "ds_read_b128 a[232:235], v65 offset:1024\n"
    "v_mfma_f32_16x16x32_bf16 v[52:55], a[128:131], a[180:183], v[52:55]\n"
    "ds_read_b128 a[236:239], v65 offset:1536\n"
    "v_mfma_f32_16x16x32_bf16 v[56:59], a[128:131], a[184:187], v[56:59]\n"
    "ds_read_b128 a[240:243], v65 offset:2048\n"
    "v_mfma_f32_16x16x32_bf16 v[60:63], a[128:131], a[188:191], v[60:63]\n"
    "ds_read_b128 a[244:247], v65 offset:2560\n"
    "v_mfma_f32_16x16x32_bf16 v[36:39], a[132:135], a[160:163], v[36:39]\n"
    "ds_read_b128 a[248:251], v65 offset:3072\n"
    "v_mfma_f32_16x16x32_bf16 v[40:43], a[132:135], a[164:167], v[40:43]\n"
    "ds_read_b128 a[252:255], v65 offset:3584\n"
    "v_mfma_f32_16x16x32_bf16 v[44:47], a[132:135], a[168:171], v[44:47]\n"
    "ds_read_b64_tr_b16 a[196:197], v64 offset:968\n"
    "v_mfma_f32_16x16x32_bf16 v[48:51], a[132:135], a[172:175], v[48:51]\n"
    "ds_read_b64_tr_b16 a[198:199], v64 offset:960\n"
    "v_mfma_f32_16x16x32_bf16 v[52:55], a[132:135], a[176:179], v[52:55]\n"
    "ds_read_b64_tr_b16 a[200:201], v64 offset:1480\n"
    "v_mfma_f32_16x16x32_bf16 v[56:59], a[132:135], a[180:183], v[56:59]\n"
    "ds_read_b64_tr_b16 a[202:203], v64 offset:1472\n"
    "v_mfma_f32_16x16x32_bf16 v[60:63], a[132:135], a[184:187], v[60:63]\n"
    "ds_read_b64_tr_b16 a[204:205], v64 offset:1992\n"
    "v_mfma_f32_16x16x32_bf16 v[40:43], a[136:139], a[160:163], v[40:43]\n"
    "ds_read_b64_tr_b16 a[206:207], v64 offset:1984\n"
    "v_mfma_f32_16x16x32_bf16 v[44:47], a[136:139], a[164:167], v[44:47]\n"
    "ds_read_b64_tr_b16 a[208:209], v64 offset:2504\n"
    "v_mfma_f32_16x16x32_bf16 v[48:51], a[136:139], a[168:171], v[48:51]\n"
    "ds_read_b64_tr_b16 a[210:211], v64 offset:2496\n"
    "v_mfma_f32_16x16x32_bf16 v[52:55], a[136:139], a[172:175], v[52:55]\n"
    "ds_read_b64_tr_b16 a[212:213], v64 offset:3016\n"
    "v_mfma_f32_16x16x32_bf16 v[56:59], a[136:139], a[176:179], v[56:59]\n"
    "ds_read_b64_tr_b16 a[214:215], v64 offset:3008\n"
    "v_mfma_f32_16x16x32_bf16 v[60:63], a[136:139], a[180:183], v[60:63]\n"
    "ds_read_b64_tr_b16 a[216:217], v64 offset:3528\n"
    "v_mfma_f32_16x16x32_bf16 v[44:47], a[140:143], a[160:163], v[44:47]\n"
    "ds_read_b64_tr_b16 a[218:219], v64 offset:3520\n"
    "v_mfma_f32_16x16x32_bf16 v[48:51], a[140:143], a[164:167], v[48:51]\n"
    "ds_read_b64_tr_b16 a[220:221], v64 offset:4040\n"
    "v_mfma_f32_16x16x32_bf16 v[52:55], a[140:143], a[168:171], v[52:55]\n"
    "ds_read_b64_tr_b16 a[222:223], v64 offset:4032\n"
    "v_mfma_f32_16x16x32_bf16 v[56:59], a[140:143], a[172:175], v[56:59]\n"
    "v_alignbit_b32 v233, v145, v144, 16\n"
    "v_mfma_f32_16x16x32_bf16 v[60:63], a[140:143], a[176:179], v[60:63]\n"
    "v_alignbit_b32 v234, v146, v145, 16\n"
    "v_mfma_f32_16x16x32_bf16 v[48:51], a[144:147], a[160:163], v[48:51]\n"
    "v_alignbit_b32 v235, v147, v146, 16\n"
    "v_mfma_f32_16x16x32_bf16 v[52:55], a[144:147], a[164:167], v[52:55]\n"
    "ds_write_b128 v177, v[232:235] offset:7264\n"
    "v_mfma_f32_16x16x32_bf16 v[56:59], a[144:147], a[168:171], v[56:59]\n"
    "v_alignbit_b32 v236, v143, v142, 16\n"
    "v_mfma_f32_16x16x32_bf16 v[60:63], a[144:147], a[172:175], v[60:63]\n"
    "v_alignbit_b32 v237, v144, v143, 16\n"
    "v_mfma_f32_16x16x32_bf16 v[52:55], a[148:151], a[160:163], v[52:55]\n"
    "v_alignbit_b32 v238, v145, v144, 16\n"
    "v_mfma_f32_16x16x32_bf16 v[56:59], a[148:151], a[164:167], v[56:59]\n"
    "v_alignbit_b32 v239, v146, v145, 16\n"
    "v_mfma_f32_16x16x32_bf16 v[60:63], a[148:151], a[168:171], v[60:63]\n"
    "ds_write_b128 v177, v[236:239] offset:15584\n"
    "v_mfma_f32_16x16x32_bf16 v[56:59], a[152:155], a[160:163], v[56:59]\n"
    "v_mfma_f32_16x16x32_bf16 v[60:63], a[152:155], a[164:167], v[60:63]\n"
    "v_mfma_f32_16x16x32_bf16 v[60:63], a[156:159], a[160:163], v[60:63]\n"
    "s_waitcnt lgkmcnt(2)\n"
    "v_mfma_f32_16x16x32_bf16 v[32:35], a[192:195], a[224:227], v[32:35]\n"
    "s_waitcnt vmcnt(5)\n"
    "v_mfma_f32_16x16x32_bf16 v[36:39], a[192:195], a[228:231], v[36:39]\n"
    "v_mov_b32 v26, s73\n"
    "v_mfma_f32_16x16x32_bf16 v[40:43], a[192:195], a[232:235], v[40:43]\n"
    "ds_write_b128 v165, v[192:195] offset:480\n"
    "v_mfma_f32_16x16x32_bf16 v[44:47], a[192:195], a[236:239], v[44:47]\n"
    "ds_write_b128 v165, v[196:199] offset:1504\n"
    "v_mfma_f32_16x16x32_bf16 v[48:51], a[192:195], a[240:243], v[48:51]\n"
    "ds_write_b128 v165, v[200:203] offset:2528\n"
    "v_mfma_f32_16x16x32_bf16 v[52:55], a[192:195], a[244:247], v[52:55]\n"
    "ds_write_b128 v165, v[204:207] offset:3552\n"
    "v_mfma_f32_16x16x32_bf16 v[56:59], a[192:195], a[248:251], v[56:59]\n"
    "v_add3_u32 v64, s27, v166, v26\n"
    "v_mfma_f32_16x16x32_bf16 v[60:63], a[192:195], a[252:255], v[60:63]\n"
    "v_sub_u32 v65, v167, v26\n"
    "v_mfma_f32_16x16x32_bf16 v[36:39], a[196:199], a[224:227], v[36:39]\n"
    "v_mfma_f32_16x16x32_bf16 v[40:43], a[196:199], a[228:231], v[40:43]\n"
    "ds_read_b64_tr_b16 a[128:129], v64 offset:8\n"
    "v_mfma_f32_16x16x32_bf16 v[44:47], a[196:199], a[232:235], v[44:47]\n"
    "ds_read_b64_tr_b16 a[130:131], v64 offset:0\n"
    "v_mfma_f32_16x16x32_bf16 v[48:51], a[196:199], a[236:239], v[48:51]\n"
    "ds_read_b128 a[160:163], v65 offset:448\n"
    "v_mfma_f32_16x16x32_bf16 v[52:55], a[196:199], a[240:243], v[52:55]\n"
    "s_xor_b32 s26, s26, s27\n"
    "ds_read_b128 a[164:167], v65 offset:960\n"
    "v_mfma_f32_16x16x32_bf16 v[56:59], a[196:199], a[244:247], v[56:59]\n"
    "ds_read_b128 a[168:171], v65 offset:1472\n"
    "v_mfma_f32_16x16x32_bf16 v[60:63], a[196:199], a[248:251], v[60:63]\n"
    "s_xor_b32 s27, s26, s27\n"
    "ds_read_b128 a[172:175], v65 offset:1984\n"
    "v_mfma_f32_16x16x32_bf16 v[40:43], a[200:203], a[224:227], v[40:43]\n"
    "ds_read_b128 a[176:179], v65 offset:2496\n"
    "v_mfma_f32_16x16x32_bf16 v[44:47], a[200:203], a[228:231], v[44:47]\n"
    "s_xor_b32 s26, s26, s27\n"
    "ds_read_b128 a[180:183], v65 offset:3008\n"
    "v_mfma_f32_16x16x32_bf16 v[48:51], a[200:203], a[232:235], v[48:51]\n"
    "ds_read_b128 a[184:187], v65 offset:3520\n"
    "v_mfma_f32_16x16x32_bf16 v[52:55], a[200:203], a[236:239], v[52:55]\n"
    "s_and_b32 s69, s52, 0xffff\n"
    "ds_read_b128 a[188:191], v65 offset:4032\n"
    "v_mfma_f32_16x16x32_bf16 v[56:59], a[200:203], a[240:243], v[56:59]\n"
    "ds_read_b64_tr_b16 a[132:133], v64 offset:520\n"
    "v_mfma_f32_16x16x32_bf16 v[60:63], a[200:203], a[244:247], v[60:63]\n"
    "s_mul_i32 s69, s69, 0x10001\n"
    "ds_read_b64_tr_b16 a[134:135], v64 offset:512\n"
    "v_mfma_f32_16x16x32_bf16 v[44:47], a[204:207], a[224:227], v[44:47]\n"
    "ds_read_b64_tr_b16 a[136:137], v64 offset:1032\n"
    "v_mfma_f32_16x16x32_bf16 v[48:51], a[204:207], a[228:231], v[48:51]\n"
    "s_lshr_b64 s[52:53], s[52:53], 16\n"
    "ds_read_b64_tr_b16 a[138:139], v64 offset:1024\n"
    "v_mfma_f32_16x16x32_bf16 v[52:55], a[204:207], a[232:235], v[52:55]\n"
    "ds_read_b64_tr_b16 a[140:141], v64 offset:1544\n"
    "v_mfma_f32_16x16x32_bf16 v[56:59], a[204:207], a[236:239], v[56:59]\n"
    "v_and_b32 v68, s69, v168\n"
    "ds_read_b64_tr_b16 a[142:143], v64 offset:1536\n"
    "v_mfma_f32_16x16x32_bf16 v[60:63], a[204:207], a[240:243], v[60:63]\n"
    "ds_read_b64_tr_b16 a[144:145], v64 offset:2056\n"
    "v_mfma_f32_16x16x32_bf16 v[48:51], a[208:211], a[224:227], v[48:51]\n"
    "v_and_b32 v69, s69, v169\n"
    "ds_read_b64_tr_b16 a[146:147], v64 offset:2048\n"
    "v_mfma_f32_16x16x32_bf16 v[52:55], a[208:211], a[228:231], v[52:55]\n"
    "ds_read_b64_tr_b16 a[148:149], v64 offset:2568\n"
    "v_mfma_f32_16x16x32_bf16 v[56:59], a[208:211], a[232:235], v[56:59]\n"
    "v_and_b32 v70, s69, v170\n"
    "ds_read_b64_tr_b16 a[150:151], v64 offset:2560\n"
    "v_mfma_f32_16x16x32_bf16 v[60:63], a[208:211], a[236:239], v[60:63]\n"
    "ds_read_b64_tr_b16 a[152:153], v64 offset:3080\n"
    "v_mfma_f32_16x16x32_bf16 v[52:55], a[212:215], a[224:227], v[52:55]\n"
    "v_and_b32 v71, s69, v171\n"
    "ds_read_b64_tr_b16 a[154:155], v64 offset:3072\n"
    "v_mfma_f32_16x16x32_bf16 v[56:59], a[212:215], a[228:231], v[56:59]\n"
    "ds_read_b64_tr_b16 a[156:157], v64 offset:3592\n"
    "v_mfma_f32_16x16x32_bf16 v[60:63], a[212:215], a[232:235], v[60:63]\n"
    "s_sub_u32 s38, s38, 1\n"
    "ds_read_b64_tr_b16 a[158:159], v64 offset:3584\n"
    "v_mfma_f32_16x16x32_bf16 v[56:59], a[216:219], a[224:227], v[56:59]\n"
    "v_mfma_f32_16x16x32_bf16 v[60:63], a[216:219], a[228:231], v[60:63]\n"
    "v_mfma_f32_16x16x32_bf16 v[60:63], a[220:223], a[224:227], v[60:63]\n"
    "s_waitcnt lgkmcnt(0)\n"
    "s_cmp_lg_u32 s38, 0\n"
    "s_cbranch_scc1 SCH_gated_mix__ssm_conv_h3__swiglu_ffn__prompt_kernel\n"
    "v_cvt_pk_bf16_f32 v212, v32, v33\n"
    "v_cvt_pk_bf16_f32 v213, v34, v35\n"
    "v_cvt_pk_bf16_f32 v214, v36, v37\n"
    "v_cvt_pk_bf16_f32 v215, v38, v39\n"
    "v_cvt_pk_bf16_f32 v216, v40, v41\n"
    "v_cvt_pk_bf16_f32 v217, v42, v43\n"
    "v_cvt_pk_bf16_f32 v218, v44, v45\n"
    "v_cvt_pk_bf16_f32 v219, v46, v47\n"
    "v_cvt_pk_bf16_f32 v220, v48, v49\n"
    "v_cvt_pk_bf16_f32 v221, v50, v51\n"
    "v_cvt_pk_bf16_f32 v222, v52, v53\n"
    "v_cvt_pk_bf16_f32 v223, v54, v55\n"
    "v_cvt_pk_bf16_f32 v224, v56, v57\n"
    "v_cvt_pk_bf16_f32 v225, v58, v59\n"
    "v_cvt_pk_bf16_f32 v226, v60, v61\n"
    "v_cvt_pk_bf16_f32 v227, v62, v63\n"
    "s_lshr_b32 s72, s74, 12\n"
    "s_and_b32 s68, s34, 1\n"
    "s_bitcmp1_b32 s72, 0\n"
    "s_cselect_b32 s30, 16, 0\n"
    "s_lshr_b32 s69, s75, s30\n"
    "s_and_b32 s69, s69, 0xffff\n"
    "s_mul_i32 s69, s69, 0x10001\n"
    "s_cmp_eq_u32 s68, 0\n"
    "s_cselect_b32 s69, s69, 0\n"
    "v_and_b32 v68, s69, v168\n"
    "v_and_b32 v69, s69, v169\n"
    "v_and_b32 v70, s69, v170\n"
    "v_and_b32 v71, s69, v171\n"
    "s_nop 1\n"
    "v_permlane16_swap_b32 v212, v214\n"
    "v_permlane16_swap_b32 v213, v215\n"
    "buffer_store_dwordx4 v[212:215], v172, s[20:23], s24 offen offset:0 sc1\n"
    "v_permlane16_swap_b32 v216, v218\n"
    "v_permlane16_swap_b32 v217, v219\n"
    "buffer_store_dwordx4 v[216:219], v172, s[20:23], s24 offen offset:1024 sc1\n"
    "v_permlane16_swap_b32 v220, v222\n"
    "v_permlane16_swap_b32 v221, v223\n"
    "buffer_store_dwordx4 v[220:223], v172, s[20:23], s24 offen offset:2048 sc1\n"
    "v_permlane16_swap_b32 v224, v226\n"
    "v_permlane16_swap_b32 v225, v227\n"
    "buffer_store_dwordx4 v[224:227], v172, s[20:23], s24 offen offset:3072 sc1\n"
    "s_mov_b32 s24, s98\n"
    "s_bfe_u32 s37, s39, 0x20001\n"
    "s_and_b32 s35, s39, 1\n"
    "s_lshl_b32 s35, s35, 2\n"
    "s_bfe_u32 s30, s39, 0x20003\n"
    "s_add_u32 s35, s35, s30\n"
    "s_lshr_b32 s36, s39, 5\n"
    "s_mul_i32 s25, s37, 1835008\n"
    "s_lshl_b32 s30, s36, 8\n"
    "s_add_u32 s25, s25, s30\n"
    "s_add_u32 s96, s25, 1835008\n"
    "s_cmp_eq_u32 s36, 7\n"
    "s_cselect_b32 s27, 3, s36\n"
    "s_mul_i32 s27, s27, 0\n"
    "s_lshl_b32 s30, s27, 18\n"
    "s_add_u32 s25, s25, s30\n"
    "s_lshl_b32 s28, s34, 12\n"
    "v_lshrrev_b32 v38, 4, v1\n"
    "s_lshl_b32 s30, s34, 4\n"
    "v_add_u32 v39, s30, v38\n"
    "v_lshlrev_b32 v39, 11, v39\n"
    "v_and_b32 v40, 1, v1\n"
    "v_lshl_add_u32 v39, v40, 4, v39\n"
    "v_bfe_u32 v40, v1, 1, 3\n"
    "v_xor_b32 v38, v38, v40\n"
    "v_lshl_add_u32 v22, v38, 5, v39\n"
    "v_xor_b32 v38, 4, v38\n"
    "v_lshl_add_u32 v23, v38, 5, v39\n"
    "v_add_u32 v24, 14336, v22\n"
    "v_add_u32 v25, 21504, v23\n"
    "v_add_u32 v23, 7168, v23\n"
    "s_barrier\n"
    "v_mfma_f32_16x16x32_bf16 v[128:131], v[68:71], a[160:163], 0\n"
    "v_mfma_f32_16x16x32_bf16 v[132:135], v[68:71], a[164:167], 0\n"
    "v_mfma_f32_16x16x32_bf16 v[136:139], v[68:71], a[168:171], 0\n"
    "v_mfma_f32_16x16x32_bf16 v[140:143], v[68:71], a[172:175], 0\n"
    "v_mfma_f32_16x16x32_bf16 v[144:147], v[68:71], a[176:179], 0\n"
    "v_mfma_f32_16x16x32_bf16 v[148:151], v[68:71], a[180:183], 0\n"
    "v_mfma_f32_16x16x32_bf16 v[152:155], v[68:71], a[184:187], 0\n"
    "v_mfma_f32_16x16x32_bf16 v[156:159], v[68:71], a[188:191], 0\n"
    "v_mfma_f32_16x16x32_bf16 v[128:131], a[128:131], a[160:163], v[128:131]\n"
    "ds_read_b64_tr_b16 a[192:193], v64 offset:72\n"
    "v_mfma_f32_16x16x32_bf16 v[132:135], a[128:131], a[164:167], v[132:135]\n"
    "ds_read_b64_tr_b16 a[194:195], v64 offset:64\n"
    "v_mfma_f32_16x16x32_bf16 v[136:139], a[128:131], a[168:171], v[136:139]\n"
    "ds_read_b128 a[224:227], v65 offset:384\n"
    "v_mfma_f32_16x16x32_bf16 v[140:143], a[128:131], a[172:175], v[140:143]\n"
    "s_mul_i32 s30, s35, 28\n"
    "s_add_u32 m0, s28, 0\n"
    "s_nop 0\n"
    "ds_read_b128 a[228:231], v65 offset:896\n"
    "v_mfma_f32_16x16x32_bf16 v[144:147], a[128:131], a[176:179], v[144:147]\n"
    "ds_read_b128 a[232:235], v65 offset:1408\n"
    "v_mfma_f32_16x16x32_bf16 v[148:151], a[128:131], a[180:183], v[148:151]\n"
    "s_mul_i32 s31, s37, 7\n"
    "s_add_u32 s30, s30, s31\n"
    "ds_read_b128 a[236:239], v65 offset:1920\n"
    "v_mfma_f32_16x16x32_bf16 v[152:155], a[128:131], a[184:187], v[152:155]\n"
    "ds_read_b128 a[240:243], v65 offset:2432\n"
    "v_mfma_f32_16x16x32_bf16 v[156:159], a[128:131], a[188:191], v[156:159]\n"
    "buffer_load_dwordx4 v22, s[76:79], s25 offen offset:0 lds\n"
    "s_lshl_b32 s30, s30, 7\n"
    "ds_read_b128 a[244:247], v65 offset:2944\n"
    "v_mfma_f32_16x16x32_bf16 v[132:135], a[132:135], a[160:163], v[132:135]\n"
    "ds_read_b128 a[248:251], v65 offset:3456\n"
    "v_mfma_f32_16x16x32_bf16 v[136:139], a[132:135], a[164:167], v[136:139]\n"
    "s_add_u32 s30, s30, 40968192\n"
    "buffer_load_dwordx4 v23, s[76:79], s25 offen offset:1024 lds\n"
    "ds_read_b128 a[252:255], v65 offset:3968\n"
    "v_mfma_f32_16x16x32_bf16 v[140:143], a[132:135], a[168:171], v[140:143]\n"
    "ds_read_b64_tr_b16 a[196:197], v64 offset:584\n"
    "v_mfma_f32_16x16x32_bf16 v[144:147], a[132:135], a[172:175], v[144:147]\n"
    "v_min_u32 v80, 6, v1\n"
    "ds_read_b64_tr_b16 a[198:199], v64 offset:576\n"
    "v_mfma_f32_16x16x32_bf16 v[148:151], a[132:135], a[176:179], v[148:151]\n"
    "ds_read_b64_tr_b16 a[200:201], v64 offset:1096\n"
    "v_mfma_f32_16x16x32_bf16 v[152:155], a[132:135], a[180:183], v[152:155]\n"
    "v_lshlrev_b32 v80, 7, v80\n"
    "buffer_load_dwordx4 v24, s[76:79], s25 offen offset:2048 lds\n"
    "v_add_u32 v80, s30, v80\n"
    "ds_read_b64_tr_b16 a[202:203], v64 offset:1088\n"
    "v_mfma_f32_16x16x32_bf16 v[156:159], a[132:135], a[184:187], v[156:159]\n"
    "ds_read_b64_tr_b16 a[204:205], v64 offset:1608\n"
    "v_mfma_f32_16x16x32_bf16 v[136:139], a[136:139], a[160:163], v[136:139]\n"
    "v_lshlrev_b32 v2, 4, v1\n"
    "ds_read_b64_tr_b16 a[206:207], v64 offset:1600\n"
    "v_mfma_f32_16x16x32_bf16 v[140:143], a[136:139], a[164:167], v[140:143]\n"
    "ds_read_b64_tr_b16 a[208:209], v64 offset:2120\n"
    "v_mfma_f32_16x16x32_bf16 v[144:147], a[136:139], a[168:171], v[144:147]\n"
    "buffer_load_dwordx4 v25, s[76:79], s25 offen offset:3072 lds\n"
    "v_add_u32 v3, 114688, v2\n"
    "ds_read_b64_tr_b16 a[210:211], v64 offset:2112\n"
    "v_mfma_f32_16x16x32_bf16 v[148:151], a[136:139], a[172:175], v[148:151]\n"
    "ds_read_b64_tr_b16 a[212:213], v64 offset:2632\n"
    "v_mfma_f32_16x16x32_bf16 v[152:155], a[136:139], a[176:179], v[152:155]\n"
    "v_add_u32 v4, 229376, v2\n"
    "s_add_u32 s25, s25, 131072\n"
    "v_add_u32 v5, 344064, v2\n"
    "ds_read_b64_tr_b16 a[214:215], v64 offset:2624\n"
    "v_mfma_f32_16x16x32_bf16 v[156:159], a[136:139], a[180:183], v[156:159]\n"
    "ds_read_b64_tr_b16 a[216:217], v64 offset:3144\n"
    "v_mfma_f32_16x16x32_bf16 v[140:143], a[140:143], a[160:163], v[140:143]\n"
    "s_lshl_b32 s30, s35, 4\n"
    "ds_read_b64_tr_b16 a[218:219], v64 offset:3136\n"
    "v_mfma_f32_16x16x32_bf16 v[144:147], a[140:143], a[164:167], v[144:147]\n"
    "ds_read_b64_tr_b16 a[220:221], v64 offset:3656\n"
    "v_mfma_f32_16x16x32_bf16 v[148:151], a[140:143], a[168:171], v[148:151]\n"
    "s_add_u32 m0, s28, 16384\n"
    "s_nop 0\n"
    "s_lshl_b32 s31, s34, 2\n"
    "ds_read_b64_tr_b16 a[222:223], v64 offset:3648\n"
    "v_mfma_f32_16x16x32_bf16 v[152:155], a[140:143], a[172:175], v[152:155]\n"
    "s_add_u32 s30, s30, s31\n"
    "buffer_load_dwordx4 v22, s[76:79], s25 offen offset:0 lds\n"
    "v_mfma_f32_16x16x32_bf16 v[156:159], a[140:143], a[176:179], v[156:159]\n"
    "s_mul_i32 s30, s30, 112\n"
    "s_mul_i32 s31, s37, 28\n"
    "v_mfma_f32_16x16x32_bf16 v[144:147], a[144:147], a[160:163], v[144:147]\n"
    "buffer_load_dwordx4 v23, s[76:79], s25 offen offset:1024 lds\n"
    "s_add_u32 s30, s30, s31\n"
    "v_mfma_f32_16x16x32_bf16 v[148:151], a[144:147], a[164:167], v[148:151]\n"
    "s_lshl_b32 s38, s30, 10\n"
    "v_mfma_f32_16x16x32_bf16 v[152:155], a[144:147], a[168:171], v[152:155]\n"
    "buffer_load_dwordx4 v24, s[76:79], s25 offen offset:2048 lds\n"
    "s_add_u32 s38, s38, 24117248\n"
    "s_add_u32 s97, s38, 28672\n"
    "v_mfma_f32_16x16x32_bf16 v[156:159], a[144:147], a[172:175], v[156:159]\n"
    "buffer_load_dwordx4 v25, s[76:79], s25 offen offset:3072 lds\n"
    "s_lshl_b32 s30, s27, 12\n"
    "v_mfma_f32_16x16x32_bf16 v[148:151], a[148:151], a[160:163], v[148:151]\n"
    "s_add_u32 s38, s38, s30\n"
    "v_mfma_f32_16x16x32_bf16 v[152:155], a[148:151], a[164:167], v[152:155]\n"
    "s_add_u32 s25, s25, 131072\n"
    "v_lshrrev_b32 v38, 2, v1\n"
    "v_mfma_f32_16x16x32_bf16 v[156:159], a[148:151], a[168:171], v[156:159]\n"
    "v_lshlrev_b32 v39, 8, v38\n"
    "s_add_u32 m0, s28, 32768\n"
    "s_nop 0\n"
    "v_and_b32 v40, 3, v1\n"
    "v_mfma_f32_16x16x32_bf16 v[152:155], a[152:155], a[160:163], v[152:155]\n"
    "v_lshl_add_u32 v39, v40, 3, v39\n"
    "v_mfma_f32_16x16x32_bf16 v[156:159], a[152:155], a[164:167], v[156:159]\n"
    "buffer_load_dwordx4 v22, s[76:79], s25 offen offset:0 lds\n"
    "v_and_b32 v38, 7, v38\n"
    "v_mfma_f32_16x16x32_bf16 v[156:159], a[156:159], a[160:163], v[156:159]\n"
    "s_lshl_b32 s30, s37, 1\n"
    "buffer_load_dwordx4 v23, s[76:79], s25 offen offset:1024 lds\n"
    "s_waitcnt lgkmcnt(0)\n"
    "v_mfma_f32_16x16x32_bf16 v[128:131], a[192:195], a[224:227], v[128:131]\n"
    "ds_read_b64_tr_b16 a[128:129], v64 offset:136\n"
    "v_mfma_f32_16x16x32_bf16 v[132:135], a[192:195], a[228:231], v[132:135]\n"
    "ds_read_b64_tr_b16 a[130:131], v64 offset:128\n"
    "v_mfma_f32_16x16x32_bf16 v[136:139], a[192:195], a[232:235], v[136:139]\n"
    "ds_read_b128 a[160:163], v65 offset:320\n"
    "v_mfma_f32_16x16x32_bf16 v[140:143], a[192:195], a[236:239], v[140:143]\n"
    "v_xor_b32 v38, s30, v38\n"
    "v_xor_b32 v40, 0, v38\n"
    "ds_read_b128 a[164:167], v65 offset:832\n"
    "v_mfma_f32_16x16x32_bf16 v[144:147], a[192:195], a[240:243], v[144:147]\n"
    "ds_read_b128 a[168:171], v65 offset:1344\n"
    "v_mfma_f32_16x16x32_bf16 v[148:151], a[192:195], a[244:247], v[148:151]\n"
    "buffer_load_dwordx4 v24, s[76:79], s25 offen offset:2048 lds\n"
    "v_lshl_add_u32 v6, v40, 5, v39\n"
    "ds_read_b128 a[172:175], v65 offset:1856\n"
    "v_mfma_f32_16x16x32_bf16 v[152:155], a[192:195], a[248:251], v[152:155]\n"
    "ds_read_b128 a[176:179], v65 offset:2368\n"
    "v_mfma_f32_16x16x32_bf16 v[156:159], a[192:195], a[252:255], v[156:159]\n"
    "v_xor_b32 v40, 1, v38\n"
    "ds_read_b128 a[180:183], v65 offset:2880\n"
    "v_mfma_f32_16x16x32_bf16 v[132:135], a[196:199], a[224:227], v[132:135]\n"
    "ds_read_b128 a[184:187], v65 offset:3392\n"
    "v_mfma_f32_16x16x32_bf16 v[136:139], a[196:199], a[228:231], v[136:139]\n"
    "v_lshl_add_u32 v7, v40, 5, v39\n"
    "buffer_load_dwordx4 v25, s[76:79], s25 offen offset:3072 lds\n"
    "v_xor_b32 v40, 2, v38\n"
    "ds_read_b128 a[188:191], v65 offset:3904\n"
    "v_mfma_f32_16x16x32_bf16 v[140:143], a[196:199], a[232:235], v[140:143]\n"
    "ds_read_b64_tr_b16 a[132:133], v64 offset:648\n"
    "v_mfma_f32_16x16x32_bf16 v[144:147], a[196:199], a[236:239], v[144:147]\n"
    "v_lshl_add_u32 v8, v40, 5, v39\n"
    "s_add_u32 s25, s25, 131072\n"
    "ds_read_b64_tr_b16 a[134:135], v64 offset:640\n"
    "v_mfma_f32_16x16x32_bf16 v[148:151], a[196:199], a[240:243], v[148:151]\n"
    "ds_read_b64_tr_b16 a[136:137], v64 offset:1160\n"
    "v_mfma_f32_16x16x32_bf16 v[152:155], a[196:199], a[244:247], v[152:155]\n"
    "v_xor_b32 v40, 3, v38\n"
    "ds_read_b64_tr_b16 a[138:139], v64 offset:1152\n"
    "v_mfma_f32_16x16x32_bf16 v[156:159], a[196:199], a[248:251], v[156:159]\n"
    "ds_read_b64_tr_b16 a[140:141], v64 offset:1672\n"
    "v_mfma_f32_16x16x32_bf16 v[136:139], a[200:203], a[224:227], v[136:139]\n"
    "v_lshl_add_u32 v9, v40, 5, v39\n"
    "ds_read_b64_tr_b16 a[142:143], v64 offset:1664\n"
    "v_mfma_f32_16x16x32_bf16 v[140:143], a[200:203], a[228:231], v[140:143]\n"
    "ds_read_b64_tr_b16 a[144:145], v64 offset:2184\n"
    "v_mfma_f32_16x16x32_bf16 v[144:147], a[200:203], a[232:235], v[144:147]\n"
    "v_xor_b32 v40, 4, v38\n"
    "buffer_load_dwordx2 v[84:85], v80, s[20:23], 0 offen sc1\n"
    "v_lshl_add_u32 v10, v40, 5, v39\n"
    "ds_read_b64_tr_b16 a[146:147], v64 offset:2176\n"
    "v_mfma_f32_16x16x32_bf16 v[148:151], a[200:203], a[236:239], v[148:151]\n"
    "ds_read_b64_tr_b16 a[148:149], v64 offset:2696\n"
    "v_mfma_f32_16x16x32_bf16 v[152:155], a[200:203], a[240:243], v[152:155]\n"
    "v_xor_b32 v40, 5, v38\n"
    "ds_read_b64_tr_b16 a[150:151], v64 offset:2688\n"
    "v_mfma_f32_16x16x32_bf16 v[156:159], a[200:203], a[244:247], v[156:159]\n"
    "ds_read_b64_tr_b16 a[152:153], v64 offset:3208\n"
    "v_mfma_f32_16x16x32_bf16 v[140:143], a[204:207], a[224:227], v[140:143]\n"
    "v_lshl_add_u32 v11, v40, 5, v39\n"
    "ds_read_b64_tr_b16 a[154:155], v64 offset:3200\n"
    "v_mfma_f32_16x16x32_bf16 v[144:147], a[204:207], a[228:231], v[144:147]\n"
    "ds_read_b64_tr_b16 a[156:157], v64 offset:3720\n"
    "v_mfma_f32_16x16x32_bf16 v[148:151], a[204:207], a[232:235], v[148:151]\n"
    "v_xor_b32 v40, 6, v38\n"
    "s_add_u32 m0, s28, 49152\n"
    "s_nop 0\n"
    "v_lshl_add_u32 v12, v40, 5, v39\n"
    "ds_read_b64_tr_b16 a[158:159], v64 offset:3712\n"
    "v_mfma_f32_16x16x32_bf16 v[152:155], a[204:207], a[236:239], v[152:155]\n"
    "v_xor_b32 v40, 7, v38\n"
    "v_mfma_f32_16x16x32_bf16 v[156:159], a[204:207], a[240:243], v[156:159]\n"
    "buffer_load_dwordx4 v22, s[76:79], s25 offen offset:0 lds\n"
    "v_lshl_add_u32 v13, v40, 5, v39\n"
    "v_mfma_f32_16x16x32_bf16 v[144:147], a[208:211], a[224:227], v[144:147]\n"
    "v_and_b32 v34, 15, v1\n"
    "v_mfma_f32_16x16x32_bf16 v[148:151], a[208:211], a[228:231], v[148:151]\n"
    "v_lshlrev_b32 v34, 5, v34\n"
    "buffer_load_dwordx4 v23, s[76:79], s25 offen offset:1024 lds\n"
    "v_bfe_u32 v72, v1, 4, 1\n"
    "v_mfma_f32_16x16x32_bf16 v[152:155], a[208:211], a[232:235], v[152:155]\n"
    "v_lshl_add_u32 v34, v72, 9, v34\n"
    "v_mfma_f32_16x16x32_bf16 v[156:159], a[208:211], a[236:239], v[156:159]\n"
    "v_lshrrev_b32 v72, 5, v1\n"
    "buffer_load_dwordx4 v24, s[76:79], s25 offen offset:2048 lds\n"
    "v_mfma_f32_16x16x32_bf16 v[148:151], a[212:215], a[224:227], v[148:151]\n"
    "v_lshl_add_u32 v34, v72, 4, v34\n"
    "s_lshl_b32 s30, s34, 12\n"
    "v_mfma_f32_16x16x32_bf16 v[152:155], a[212:215], a[228:231], v[152:155]\n"
    "s_add_u32 s30, s30, 147456\n"
    "buffer_load_dwordx4 v25, s[76:79], s25 offen offset:3072 lds\n"
    "v_mfma_f32_16x16x32_bf16 v[156:159], a[212:215], a[232:235], v[156:159]\n"
    "v_add_u32 v82, s30, v2\n"
    "v_mfma_f32_16x16x32_bf16 v[152:155], a[216:219], a[224:227], v[152:155]\n"
    "v_xor_b32 v35, 0x1000, v82\n"
    "v_mfma_f32_16x16x32_bf16 v[156:159], a[216:219], a[228:231], v[156:159]\n"
    "s_add_u32 s25, s25, 131072\n"
    "v_mfma_f32_16x16x32_bf16 v[156:159], a[220:223], a[224:227], v[156:159]\n"
    "s_waitcnt lgkmcnt(0)\n"
    "s_waitcnt vmcnt(4)\n"
    "v_cmp_ne_u64 vcc, s[70:71], v[84:85]\n"
    "s_cbranch_vccnz PHSB_gated_mix__ssm_conv_h3__swiglu_ffn__prompt_kernel\n"
    "PHRB_gated_mix__ssm_conv_h3__swiglu_ffn__prompt_kernel:\n"
    "v_mfma_f32_16x16x32_bf16 v[128:131], a[128:131], a[160:163], v[128:131]\n"
    "ds_read_b64_tr_b16 a[192:193], v64 offset:200\n"
    "v_mfma_f32_16x16x32_bf16 v[132:135], a[128:131], a[164:167], v[132:135]\n"
    "ds_read_b64_tr_b16 a[194:195], v64 offset:192\n"
    "v_mfma_f32_16x16x32_bf16 v[136:139], a[128:131], a[168:171], v[136:139]\n"
    "buffer_load_dwordx4 a[0:3], v2, s[20:23], s38 offen offset:0 sc1\n"
    "ds_read_b128 a[224:227], v65 offset:256\n"
    "v_mfma_f32_16x16x32_bf16 v[140:143], a[128:131], a[172:175], v[140:143]\n"
    "ds_read_b128 a[228:231], v65 offset:768\n"
    "v_mfma_f32_16x16x32_bf16 v[144:147], a[128:131], a[176:179], v[144:147]\n"
    "ds_read_b128 a[232:235], v65 offset:1280\n"
    "v_mfma_f32_16x16x32_bf16 v[148:151], a[128:131], a[180:183], v[148:151]\n"
    "buffer_load_dwordx4 a[4:7], v3, s[20:23], s38 offen offset:0 sc1\n"
    "ds_read_b128 a[236:239], v65 offset:1792\n"
    "v_mfma_f32_16x16x32_bf16 v[152:155], a[128:131], a[184:187], v[152:155]\n"
    "ds_read_b128 a[240:243], v65 offset:2304\n"
    "v_mfma_f32_16x16x32_bf16 v[156:159], a[128:131], a[188:191], v[156:159]\n"
    "ds_read_b128 a[244:247], v65 offset:2816\n"
    "v_mfma_f32_16x16x32_bf16 v[132:135], a[132:135], a[160:163], v[132:135]\n"
    "ds_read_b128 a[248:251], v65 offset:3328\n"
    "v_mfma_f32_16x16x32_bf16 v[136:139], a[132:135], a[164:167], v[136:139]\n"
    "buffer_load_dwordx4 a[8:11], v4, s[20:23], s38 offen offset:0 sc1\n"
    "ds_read_b128 a[252:255], v65 offset:3840\n"
    "v_mfma_f32_16x16x32_bf16 v[140:143], a[132:135], a[168:171], v[140:143]\n"
    "ds_read_b64_tr_b16 a[196:197], v64 offset:712\n"
    "v_mfma_f32_16x16x32_bf16 v[144:147], a[132:135], a[172:175], v[144:147]\n"
    "ds_read_b64_tr_b16 a[198:199], v64 offset:704\n"
    "v_mfma_f32_16x16x32_bf16 v[148:151], a[132:135], a[176:179], v[148:151]\n"
    "ds_read_b64_tr_b16 a[200:201], v64 offset:1224\n"
    "v_mfma_f32_16x16x32_bf16 v[152:155], a[132:135], a[180:183], v[152:155]\n"
    "buffer_load_dwordx4 a[12:15], v5, s[20:23], s38 offen offset:0 sc1\n"
    "ds_read_b64_tr_b16 a[202:203], v64 offset:1216\n"
    "v_mfma_f32_16x16x32_bf16 v[156:159], a[132:135], a[184:187], v[156:159]\n"
    "ds_read_b64_tr_b16 a[204:205], v64 offset:1736\n"
    "v_mfma_f32_16x16x32_bf16 v[136:139], a[136:139], a[160:163], v[136:139]\n"
    "ds_read_b64_tr_b16 a[206:207], v64 offset:1728\n"
    "v_mfma_f32_16x16x32_bf16 v[140:143], a[136:139], a[164:167], v[140:143]\n"
    "ds_read_b64_tr_b16 a[208:209], v64 offset:2248\n"
    "v_mfma_f32_16x16x32_bf16 v[144:147], a[136:139], a[168:171], v[144:147]\n"
    "buffer_load_dwordx4 a[16:19], v2, s[20:23], s38 offen offset:1024 sc1\n"
    "ds_read_b64_tr_b16 a[210:211], v64 offset:2240\n"
    "v_mfma_f32_16x16x32_bf16 v[148:151], a[136:139], a[172:175], v[148:151]\n"
    "ds_read_b64_tr_b16 a[212:213], v64 offset:2760\n"
    "v_mfma_f32_16x16x32_bf16 v[152:155], a[136:139], a[176:179], v[152:155]\n"
    "ds_read_b64_tr_b16 a[214:215], v64 offset:2752\n"
    "v_mfma_f32_16x16x32_bf16 v[156:159], a[136:139], a[180:183], v[156:159]\n"
    "buffer_load_dwordx4 a[20:23], v3, s[20:23], s38 offen offset:1024 sc1\n"
    "ds_read_b64_tr_b16 a[216:217], v64 offset:3272\n"
    "v_mfma_f32_16x16x32_bf16 v[140:143], a[140:143], a[160:163], v[140:143]\n"
    "ds_read_b64_tr_b16 a[218:219], v64 offset:3264\n"
    "v_mfma_f32_16x16x32_bf16 v[144:147], a[140:143], a[164:167], v[144:147]\n"
    "ds_read_b64_tr_b16 a[220:221], v64 offset:3784\n"
    "v_mfma_f32_16x16x32_bf16 v[148:151], a[140:143], a[168:171], v[148:151]\n"
    "ds_read_b64_tr_b16 a[222:223], v64 offset:3776\n"
    "v_mfma_f32_16x16x32_bf16 v[152:155], a[140:143], a[172:175], v[152:155]\n"
    "buffer_load_dwordx4 a[24:27], v4, s[20:23], s38 offen offset:1024 sc1\n"
    "v_mfma_f32_16x16x32_bf16 v[156:159], a[140:143], a[176:179], v[156:159]\n"
    "v_mfma_f32_16x16x32_bf16 v[144:147], a[144:147], a[160:163], v[144:147]\n"
    "v_mfma_f32_16x16x32_bf16 v[148:151], a[144:147], a[164:167], v[148:151]\n"
    "v_mfma_f32_16x16x32_bf16 v[152:155], a[144:147], a[168:171], v[152:155]\n"
    "buffer_load_dwordx4 a[28:31], v5, s[20:23], s38 offen offset:1024 sc1\n"
    "v_mfma_f32_16x16x32_bf16 v[156:159], a[144:147], a[172:175], v[156:159]\n"
    "v_mfma_f32_16x16x32_bf16 v[148:151], a[148:151], a[160:163], v[148:151]\n"
    "v_mfma_f32_16x16x32_bf16 v[152:155], a[148:151], a[164:167], v[152:155]\n"
    "v_mfma_f32_16x16x32_bf16 v[156:159], a[148:151], a[168:171], v[156:159]\n"
    "buffer_load_dwordx4 a[32:35], v2, s[20:23], s38 offen offset:2048 sc1\n"
    "v_mfma_f32_16x16x32_bf16 v[152:155], a[152:155], a[160:163], v[152:155]\n"
    "v_mfma_f32_16x16x32_bf16 v[156:159], a[152:155], a[164:167], v[156:159]\n"
    "v_mfma_f32_16x16x32_bf16 v[156:159], a[156:159], a[160:163], v[156:159]\n"
    "s_waitcnt lgkmcnt(0)\n"
    "v_mfma_f32_16x16x32_bf16 v[128:131], a[192:195], a[224:227], v[128:131]\n"
    "v_mfma_f32_16x16x32_bf16 v[132:135], a[192:195], a[228:231], v[132:135]\n"
    "v_mfma_f32_16x16x32_bf16 v[136:139], a[192:195], a[232:235], v[136:139]\n"
    "v_mfma_f32_16x16x32_bf16 v[140:143], a[192:195], a[236:239], v[140:143]\n"
    "buffer_load_dwordx4 a[36:39], v3, s[20:23], s38 offen offset:2048 sc1\n"
    "v_mfma_f32_16x16x32_bf16 v[144:147], a[192:195], a[240:243], v[144:147]\n"
    "v_mfma_f32_16x16x32_bf16 v[148:151], a[192:195], a[244:247], v[148:151]\n"
    "v_mfma_f32_16x16x32_bf16 v[152:155], a[192:195], a[248:251], v[152:155]\n"
    "v_mfma_f32_16x16x32_bf16 v[156:159], a[192:195], a[252:255], v[156:159]\n"
    "buffer_load_dwordx4 a[40:43], v4, s[20:23], s38 offen offset:2048 sc1\n"
    "v_mfma_f32_16x16x32_bf16 v[132:135], a[196:199], a[224:227], v[132:135]\n"
    "v_mfma_f32_16x16x32_bf16 v[136:139], a[196:199], a[228:231], v[136:139]\n"
    "v_mfma_f32_16x16x32_bf16 v[140:143], a[196:199], a[232:235], v[140:143]\n"
    "v_mfma_f32_16x16x32_bf16 v[144:147], a[196:199], a[236:239], v[144:147]\n"
    "buffer_load_dwordx4 a[44:47], v5, s[20:23], s38 offen offset:2048 sc1\n"
    "v_mfma_f32_16x16x32_bf16 v[148:151], a[196:199], a[240:243], v[148:151]\n"
    "v_mfma_f32_16x16x32_bf16 v[152:155], a[196:199], a[244:247], v[152:155]\n"
    "v_mfma_f32_16x16x32_bf16 v[156:159], a[196:199], a[248:251], v[156:159]\n"
    "v_mfma_f32_16x16x32_bf16 v[136:139], a[200:203], a[224:227], v[136:139]\n"
    "buffer_load_dwordx4 a[48:51], v2, s[20:23], s38 offen offset:3072 sc1\n"
    "v_mfma_f32_16x16x32_bf16 v[140:143], a[200:203], a[228:231], v[140:143]\n"
    "v_mfma_f32_16x16x32_bf16 v[144:147], a[200:203], a[232:235], v[144:147]\n"
    "v_mfma_f32_16x16x32_bf16 v[148:151], a[200:203], a[236:239], v[148:151]\n"
    "buffer_load_dwordx4 a[52:55], v3, s[20:23], s38 offen offset:3072 sc1\n"
    "v_mfma_f32_16x16x32_bf16 v[152:155], a[200:203], a[240:243], v[152:155]\n"
    "v_mfma_f32_16x16x32_bf16 v[156:159], a[200:203], a[244:247], v[156:159]\n"
    "v_mfma_f32_16x16x32_bf16 v[140:143], a[204:207], a[224:227], v[140:143]\n"
    "v_mfma_f32_16x16x32_bf16 v[144:147], a[204:207], a[228:231], v[144:147]\n"
    "buffer_load_dwordx4 a[56:59], v4, s[20:23], s38 offen offset:3072 sc1\n"
    "v_mfma_f32_16x16x32_bf16 v[148:151], a[204:207], a[232:235], v[148:151]\n"
    "v_mfma_f32_16x16x32_bf16 v[152:155], a[204:207], a[236:239], v[152:155]\n"
    "v_mfma_f32_16x16x32_bf16 v[156:159], a[204:207], a[240:243], v[156:159]\n"
    "v_mfma_f32_16x16x32_bf16 v[144:147], a[208:211], a[224:227], v[144:147]\n"
    "buffer_load_dwordx4 a[60:63], v5, s[20:23], s38 offen offset:3072 sc1\n"
    "v_mfma_f32_16x16x32_bf16 v[148:151], a[208:211], a[228:231], v[148:151]\n"
    "v_mfma_f32_16x16x32_bf16 v[152:155], a[208:211], a[232:235], v[152:155]\n"
    "v_mfma_f32_16x16x32_bf16 v[156:159], a[208:211], a[236:239], v[156:159]\n"
    "v_mfma_f32_16x16x32_bf16 v[148:151], a[212:215], a[224:227], v[148:151]\n"
    "s_add_u32 s38, s38, 4096\n"
    "v_mfma_f32_16x16x32_bf16 v[152:155], a[212:215], a[228:231], v[152:155]\n"
    "v_mfma_f32_16x16x32_bf16 v[156:159], a[212:215], a[232:235], v[156:159]\n"
    "v_mfma_f32_16x16x32_bf16 v[152:155], a[216:219], a[224:227], v[152:155]\n"
    "v_mfma_f32_16x16x32_bf16 v[156:159], a[216:219], a[228:231], v[156:159]\n"
    "v_mfma_f32_16x16x32_bf16 v[156:159], a[220:223], a[224:227], v[156:159]\n"
    "s_waitcnt lgkmcnt(0)\n"
    "s_barrier\n"
    "s_branch BEND_gated_mix__ssm_conv_h3__swiglu_ffn__prompt_kernel\n"
    "PHSB_gated_mix__ssm_conv_h3__swiglu_ffn__prompt_kernel:\n"
    "s_mov_b32 s29, 0x4000\n"
    "PHB_gated_mix__ssm_conv_h3__swiglu_ffn__prompt_kernel:\n"
    "buffer_load_dwordx2 v[84:85], v80, s[20:23], 0 offen sc1\n"
    "s_waitcnt vmcnt(0)\n"
    "v_cmp_ne_u64 vcc, s[70:71], v[84:85]\n"
    "s_cbranch_vccz PHBd_gated_mix__ssm_conv_h3__swiglu_ffn__prompt_kernel\n"
    "s_sub_u32 s29, s29, 1\n"
    "s_cmp_lg_u32 s29, 0\n"
    "s_cbranch_scc1 PHB_gated_mix__ssm_conv_h3__swiglu_ffn__prompt_kernel\n"
    "PHBd_gated_mix__ssm_conv_h3__swiglu_ffn__prompt_kernel:\n"
    "s_branch PHRB_gated_mix__ssm_conv_h3__swiglu_ffn__prompt_kernel\n"
    "KEND_gated_mix__ssm_conv_h3__swiglu_ffn__prompt_kernel:\n"
  :: "s"(__builtin_amdgcn_kernarg_segment_ptr()), "s"(blockIdx.x), "v"(threadIdx.x) : "memory", "scc", "vcc", "v1", "v2", "v3", "v4", "v5", "v6", "v7", "v8", "v9", "v10", "v11", "v12", "v13", "v14", "v15", "v16", "v17", "v18", "v19", "v20", "v21", "v22", "v23", "v24", "v25", "v26", "v27", "v28", "v29", "v30", "v31", "v32", "v33", "v34", "v35", "v36", "v37", "v38", "v39", "v40", "v41", "v42", "v43", "v44", "v45", "v46", "v47", "v48", "v49", "v50", "v51", "v52", "v53", "v54", "v55", "v56", "v57", "v58", "v59", "v60", "v61", "v62", "v63", "v64", "v65", "v66", "v67", "v68", "v69", "v70", "v71", "v72", "v73", "v74", "v75", "v76", "v77", "v78", "v79", "v80", "v81", "v82", "v83", "v84", "v85", "v86", "v87", "v88", "v89", "v90", "v91", "v92", "v93", "v94", "v95", "v96", "v97", "v98", "v99", "v100", "v101", "v102", "v103", "v104", "v105", "v106", "v107", "v108", "v109", "v110", "v111", "v112", "v113", "v114", "v115", "v116", "v117", "v118", "v119", "v120", "v121", "v122", "v123", "v124", "v125", "v126", "v127", "v128", "v129", "v130", "v131", "v132", "v133", "v134", "v135", "v136", "v137", "v138", "v139", "v140", "v141", "v142", "v143", "v144", "v145", "v146", "v147", "v148", "v149", "v150", "v151", "v152", "v153", "v154", "v155", "v156", "v157", "v158", "v159", "v160", "v161", "v162", "v163", "v164", "v165", "v166", "v167", "v168", "v169", "v170", "v171", "v172", "v173", "v174", "v175", "v176", "v177", "v178", "v179", "v180", "v181", "v182", "v183", "v184", "v185", "v186", "v187", "v188", "v189", "v190", "v191", "v192", "v193", "v194", "v195", "v196", "v197", "v198", "v199", "v200", "v201", "v202", "v203", "v204", "v205", "v206", "v207", "v208", "v209", "v210", "v211", "v212", "v213", "v214", "v215", "v216", "v217", "v218", "v219", "v220", "v221", "v222", "v223", "v224", "v225", "v226", "v227", "v228", "v229", "v230", "v231", "v232", "v233", "v234", "v235", "v236", "v237", "v238", "v239", "v240", "v241", "v242", "v243", "v244", "v245", "v246", "v247", "v248", "v249", "v250", "v251", "v252", "v253", "v254", "v255", "a0", "a1", "a2", "a3", "a4", "a5", "a6", "a7", "a8", "a9", "a10", "a11", "a12", "a13", "a14", "a15", "a16", "a17", "a18", "a19", "a20", "a21", "a22", "a23", "a24", "a25", "a26", "a27", "a28", "a29", "a30", "a31", "a32", "a33", "a34", "a35", "a36", "a37", "a38", "a39", "a40", "a41", "a42", "a43", "a44", "a45", "a46", "a47", "a48", "a49", "a50", "a51", "a52", "a53", "a54", "a55", "a56", "a57", "a58", "a59", "a60", "a61", "a62", "a63", "a64", "a65", "a66", "a67", "a68", "a69", "a70", "a71", "a72", "a73", "a74", "a75", "a76", "a77", "a78", "a79", "a80", "a81", "a82", "a83", "a84", "a85", "a86", "a87", "a88", "a89", "a90", "a91", "a92", "a93", "a94", "a95", "a96", "a97", "a98", "a99", "a100", "a101", "a102", "a103", "a104", "a105", "a106", "a107", "a108", "a109", "a110", "a111", "a112", "a113", "a114", "a115", "a116", "a117", "a118", "a119", "a120", "a121", "a122", "a123", "a124", "a125", "a126", "a127", "a128", "a129", "a130", "a131", "a132", "a133", "a134", "a135", "a136", "a137", "a138", "a139", "a140", "a141", "a142", "a143", "a144", "a145", "a146", "a147", "a148", "a149", "a150", "a151", "a152", "a153", "a154", "a155", "a156", "a157", "a158", "a159", "a160", "a161", "a162", "a163", "a164", "a165", "a166", "a167", "a168", "a169", "a170", "a171", "a172", "a173", "a174", "a175", "a176", "a177", "a178", "a179", "a180", "a181", "a182", "a183", "a184", "a185", "a186", "a187", "a188", "a189", "a190", "a191", "a192", "a193", "a194", "a195", "a196", "a197", "a198", "a199", "a200", "a201", "a202", "a203", "a204", "a205", "a206", "a207", "a208", "a209", "a210", "a211", "a212", "a213", "a214", "a215", "a216", "a217", "a218", "a219", "a220", "a221", "a222", "a223", "a224", "a225", "a226", "a227", "a228", "a229", "a230", "a231", "a232", "a233", "a234", "a235", "a236", "a237", "a238", "a239", "a240", "a241", "a242", "a243", "a244", "a245", "a246", "a247", "a248", "a249", "a250", "a251", "a252", "a253", "a254", "a255", "s8", "s9", "s10", "s11", "s12", "s13", "s14", "s15", "s16", "s17", "s18", "s19", "s20", "s21", "s22", "s23", "s24", "s25", "s26", "s27", "s28", "s29", "s30", "s31", "s34", "s35", "s36", "s37", "s38", "s39", "s40", "s41", "s42", "s43", "s44", "s45", "s46", "s47", "s48", "s49", "s50", "s51", "s52", "s53", "s54", "s55", "s56", "s57", "s58", "s59", "s60", "s61", "s62", "s63", "s64", "s65", "s66", "s67", "s68", "s69", "s70", "s71", "s72", "s73", "s74", "s75", "s76", "s77", "s78", "s79", "s80", "s81", "s82", "s83", "s84", "s85", "s86", "s87", "s88", "s89", "s90", "s91", "s92", "s93", "s94", "s95", "s96", "s97", "s98", "s99");
}

extern "C" void kernel_launch(void* const* d_in, const int* in_sizes, int n_in, void* d_out, int out_size, void* d_ws, size_t ws_size, hipStream_t stream){
  (void)hipFuncSetAttribute((const void*)gated_mix__ssm_conv_h3__swiglu_ffn__prompt_kernel, hipFuncAttributeMaxDynamicSharedMemorySize, 163840);
  gated_mix__ssm_conv_h3__swiglu_ffn__prompt_kernel<<<256,256,163840,stream>>>(d_in[0], 4194304u, 0x20000u, d_in[7], 7340032u, 0x20000u, d_in[8], 7342080u, 0x20000u, d_ws, 41943040u, 0x20000u, d_in[9], 7340032u, 0x20000u, d_in[4], 4194304u, 0x20000u, d_out, 4194304u, 0x20000u, d_in[5], d_in[10], d_in[1], 4194304u, 0x20000u, d_in[2], 4194304u, 0x20000u, d_in[3]);
}
```
